```python
import torch

def _softmax(x, axis=-1):
    return torch.softmax(x, dim=axis)
def _gelu(x):
    return torch.nn.functional.gelu(x, approximate="tanh")
def _silu(x):
    return torch.nn.functional.silu(x)
def _sigmoid(x):
    return torch.sigmoid(x)
def _layernorm(x, axis=-1, eps=1e-5):
    m = x.mean(dim=axis, keepdim=True)
    v = x.var(dim=axis, keepdim=True, unbiased=False)
    return (x - m) / torch.sqrt(v + eps)
def _scatter_add(tgt, idx, src, axis=0):
    return tgt.clone().index_add_(axis, idx, src)
def _segment_sum(src, seg, n):
    out = torch.zeros((n,) + src.shape[1:], dtype=src.dtype)
    return out.index_add_(0, seg, src)
def _pad(x, pw):
    flat = tuple(v for pair in reversed(pw) for v in pair)
    return torch.nn.functional.pad(x, flat)

kernel_name = 'interleave_layers__sliding_block__glu_gated_swiglu__prompt'

def setup_inputs():

    _w = (lambda a: (lambda t: t.to(torch.bfloat16) if t.is_floating_point() else t)(torch.as_tensor(a)))
    def _A_setup():
        B, S, D = 1, 2048, 448
        return {
            "x": _w((lambda *s: torch.randn(*s, dtype=torch.bfloat16) * 0.05)(B, S, D)),
            "wqkv": _w((lambda *s: torch.randn(*s, dtype=torch.bfloat16) * 0.05)(D, 3 * D)),
            "wo": _w((lambda *s: torch.randn(*s, dtype=torch.bfloat16) * 0.05)(D, D)),
        }
    _A_in = _A_setup()

    def _B_setup():
        B, S, D, F = 1, 2048, 448, 1792
        return {
            "x": _w((lambda *s: torch.randn(*s, dtype=torch.bfloat16) * 0.05)(B, S, D)),
            "w1": _w((lambda *s: torch.randn(*s, dtype=torch.bfloat16) * 0.8)(D, F) * (2.0 / D) ** 0.5),
            "w3": _w((lambda *s: torch.randn(*s, dtype=torch.bfloat16) * 0.8)(D, F) * (2.0 / D) ** 0.5),
            "w2": _w((lambda *s: torch.randn(*s, dtype=torch.bfloat16) * 0.8)(F, D) * (2.0 / F) ** 0.5),
        }
    _B_in = _B_setup()

    return {
        "A_x": _A_in["x"],
        "A_wqkv": _A_in["wqkv"],
        "A_wo": _A_in["wo"],
        "B_x": _B_in["x"],
        "B_w1": _B_in["w1"],
        "B_w3": _B_in["w3"],
        "B_w2": _B_in["w2"],
    }

def reference(A_x, A_wqkv, A_wo, B_x, B_w1, B_w3, B_w2):
    def _A_ref(x, wqkv, wo):
        B, S, D = x.shape
        H = 16; Dh = D // H; W = 256
        qkv = (lambda x, *d: x.permute(*d))((x @ wqkv).reshape(B, S, 3, H, Dh), 2, 0, 3, 1, 4)
        q, k, v = qkv[0], qkv[1], qkv[2]
        scores = torch.einsum("bhqd,bhkd->bhqk", q, k) * (Dh ** -0.5)
        idx = torch.arange(S)
        mask = (idx[:, None] - idx[None, :] >= 0) & (idx[:, None] - idx[None, :] < W)
        scores = torch.where(mask[None, None], scores, -1e9)
        p = _softmax(scores, axis=-1)
        o = torch.einsum("bhqk,bhkd->bhqd", p, v)
        return (lambda x, *d: x.permute(*d))(o, 0, 2, 1, 3).reshape(B, S, D) @ wo

    def _B_ref(x, w1, w3, w2):
        gate = torch.einsum("bsd,df->bsf", x, w1)
        up = torch.einsum("bsd,df->bsf", x, w3)
        h = _silu(gate) * up
        out = torch.einsum("bsf,fd->bsd", h, w2)
        return out

    x = A_x
    x = x + _A_ref(_layernorm(x, axis=-1), A_wqkv, A_wo)
    x = x + _B_ref(_layernorm(x, axis=-1), B_w1, B_w3, B_w2)
    x = x + _A_ref(_layernorm(x, axis=-1), A_wqkv, A_wo)
    x = x + _B_ref(_layernorm(x, axis=-1), B_w1, B_w3, B_w2)
    return x
```

```cpp
#include <hip/hip_runtime.h>
#include <stdint.h>

typedef __attribute__((ext_vector_type(8))) __bf16 bf16x8;
typedef __attribute__((ext_vector_type(2))) __bf16 bf16x2;
typedef __attribute__((ext_vector_type(2))) float f32x2;
typedef __attribute__((ext_vector_type(4))) float f32x4;
typedef __attribute__((ext_vector_type(16))) float f32x16;
typedef __attribute__((ext_vector_type(4))) unsigned u32x4;
typedef __attribute__((ext_vector_type(2))) unsigned u32x2;
#define DEV __device__ __forceinline__
#define LAS __attribute__((address_space(3)))
#define GAS __attribute__((address_space(1)))

namespace {

constexpr int S_ = 2048, D_ = 448, F_ = 1792;
constexpr int NTT = 64;
constexpr int KSD = 28;
constexpr int KSO = 32;
constexpr int KSF = 112;

constexpr size_t OFF_WQKV = 0;
constexpr size_t OFF_WO   = OFF_WQKV + 16 * 3 * 28 * 1024;
constexpr size_t OFF_WGU  = OFF_WO + 7 * 64 * 1024;
constexpr size_t OFF_WDN  = OFF_WGU + 16 * 7 * 28 * 1024;
constexpr size_t OFF_CSQ  = OFF_WDN + 7 * 224 * 1024;
constexpr size_t OFF_CSG  = OFF_CSQ + 16 * 3 * 32 * 4;
constexpr size_t SZ_XA    = (size_t)NTT * KSD * 1024;
constexpr size_t OFF_XA   = OFF_CSG + 16 * 7 * 32 * 4;
constexpr size_t SZ_ST    = (size_t)NTT * 14 * 32 * 8;
constexpr size_t OFF_ST   = OFF_XA + 4 * SZ_XA;
constexpr size_t SZ_KV    = (size_t)NTT * 16 * 2048;
constexpr size_t OFF_KF   = OFF_ST + 4 * SZ_ST;
constexpr size_t OFF_VF   = OFF_KF + 2 * SZ_KV;
constexpr size_t OFF_QF   = OFF_VF + 2 * SZ_KV;
constexpr size_t SZ_OA    = (size_t)NTT * KSO * 1024;
constexpr size_t OFF_OA   = OFF_QF + 2 * SZ_KV;
constexpr size_t SZ_HA    = (size_t)NTT * KSF * 1024;
constexpr size_t OFF_HA   = OFF_OA + 2 * SZ_OA;
constexpr size_t WS_END   = OFF_HA + 2 * SZ_HA;

DEV unsigned pk2(float a, float b) { f32x2 v = {a, b}; return __builtin_bit_cast(unsigned, __builtin_convertvector(v, bf16x2)); }
DEV f32x16 mfma(u32x4 a, u32x4 b, f32x16 c) {
  return __builtin_amdgcn_mfma_f32_32x32x16_bf16(__builtin_bit_cast(bf16x8, a), __builtin_bit_cast(bf16x8, b), c, 0, 0, 0);
}
DEV float bflo(unsigned u) { return __uint_as_float(u << 16); }
DEV float bfhi(unsigned u) { return __uint_as_float(u & 0xffff0000u); }
DEV float swap32(float v) {
  return __shfl_xor(v, 32);
}
DEV void dma4(const char* gbase, unsigned voff, unsigned lds) {
  unsigned keep;
  asm volatile("s_mov_b32 %0, m0\n\ts_mov_b32 m0, %2\n\ts_nop 4\n\t"
               "global_load_lds_dwordx4 %1, %3 offset:0\n\t"
               "global_load_lds_dwordx4 %1, %3 offset:1024\n\t"
               "global_load_lds_dwordx4 %1, %3 offset:2048\n\t"
               "global_load_lds_dwordx4 %1, %3 offset:3072\n\t"
               "s_mov_b32 m0, %0"
               : "=&s"(keep) : "v"(voff), "s"(lds), "s"(gbase) : "memory");
}
DEV void dma1(const char* gbase, unsigned voff, unsigned lds) {
  unsigned keep;
  asm volatile("s_mov_b32 %0, m0\n\ts_mov_b32 m0, %2\n\ts_nop 4\n\t"
               "global_load_lds_dwordx4 %1, %3 offset:0\n\t"
               "s_mov_b32 m0, %0"
               : "=&s"(keep) : "v"(voff), "s"(lds), "s"(gbase) : "memory");
}
#define WAIT_ALL_BAR() asm volatile("s_waitcnt vmcnt(0) lgkmcnt(0)\n\ts_barrier" ::: "memory")
#define LGKM_BAR() asm volatile("s_waitcnt lgkmcnt(0)\n\ts_barrier" ::: "memory")
DEV u32x4 lds_rd(const LAS char* base, int off) { return *(const LAS u32x4*)(base + off); }
DEV u32x4 gld(const char* p) { return *(const GAS u32x4*)(p); }
DEV void gst(char* p, u32x4 v) { *(GAS u32x4*)(p) = v; }

DEV void dma_block(const char* g, unsigned lds, int nkb, int w, unsigned voff) {
  const int n4 = nkb >> 2;
  for (int c = w; c < n4; c += 4) dma4(g + (size_t)c * 4096, voff, lds + c * 4096);
  const int rem = nkb & 3;
  if (w < rem) dma1(g + (size_t)(n4 * 4 + w) * 1024, voff, lds + (n4 * 4 + w) * 1024);
}

struct Stats { float mean, rs; };
DEV Stats load_stats(const char* st, int tt, int lane) {
  const int r = lane & 31, h2 = lane >> 5;
  const GAS f32x2* p = (const GAS f32x2*)(st + ((size_t)tt * 14 + h2 * 7) * 256 + r * 8);
  float s1 = 0.f, s2 = 0.f;
#pragma unroll
  for (int i = 0; i < 7; ++i) { f32x2 v = p[i * 32]; s1 += v.x; s2 += v.y; }
  s1 += swap32(s1); s2 += swap32(s2);
  Stats s; s.mean = s1 * (1.0f / D_);
  float var = fmaxf(s2 * (1.0f / D_) - s.mean * s.mean, 0.f);
  s.rs = 1.0f / sqrtf(var + 1e-5f);
  return s;
}

__global__ __launch_bounds__(256) void k_prep(const unsigned short* __restrict__ x, const unsigned short* __restrict__ wqkv,
                                              const unsigned short* __restrict__ wo, const unsigned short* __restrict__ w1,
                                              const unsigned short* __restrict__ w3, const unsigned short* __restrict__ w2, char* __restrict__ ws) {
  __shared__ float red[4][32];
  const int b = blockIdx.x, tid = threadIdx.x, lane = tid & 63, w = tid >> 6, r = lane & 31, h2 = lane >> 5;
  if (b < 64) {
    const int tt = b;
    const unsigned short* xr = x + (size_t)(tt * 32 + r) * D_;
    for (int ft = w; ft < 14; ft += 4) {
      float s1 = 0.f, s2 = 0.f;
#pragma unroll
      for (int s = 0; s < 2; ++s) {
        const int ks = 2 * ft + s;
        u32x2 a = *(const u32x2*)(xr + 16 * ks + 4 * h2), c = *(const u32x2*)(xr + 16 * ks + 8 + 4 * h2);
        u32x4 v = {a.x, a.y, c.x, c.y};
        gst(ws + OFF_XA + ((size_t)tt * KSD + ks) * 1024 + lane * 16, v);
#pragma unroll
        for (int q = 0; q < 4; ++q) { float lo = bflo(v[q]), hi = bfhi(v[q]); s1 += lo + hi; s2 += lo * lo + hi * hi; }
      }
      s1 += swap32(s1); s2 += swap32(s2);
      if (h2 == 0) { f32x2 o = {s1, s2}; *(f32x2*)(ws + OFF_ST + ((size_t)tt * 14 + ft) * 256 + r * 8) = o; }
    }
    return;
  }
  int u = b - 64;
  const unsigned short* src; int ld; int nks = 28; char* dst; float* cs = nullptr; int kind;
  int a0 = 0, a1 = 0, a2 = 0;
  long col = 0; bool colok = true;
  if (u < 48) { kind = 0; a0 = u / 3; a1 = u % 3;
    src = wqkv; ld = 3 * D_; col = (a1 == 2 ? 0 : a1 == 0 ? D_ : 2 * D_) + 28 * a0 + r; colok = r < 28;
    dst = ws + OFF_WQKV + (size_t)u * 28 * 1024; cs = (float*)(ws + OFF_CSQ) + u * 32;
  } else if (u < 48 + 112) { kind = 1; u -= 48; a0 = u / 7; a1 = u % 7;
    const int g = r >> 3, hh = (r >> 2) & 1, e = r & 3;
    src = e < 2 ? w1 : w3; ld = F_; col = 112 * a0 + 16 * a1 + 8 * hh + 2 * g + (e & 1);
    dst = ws + OFF_WGU + (size_t)u * 28 * 1024; cs = (float*)(ws + OFF_CSG) + u * 32;
  } else if (u < 48 + 112 + 14) { kind = 2; u -= 160; a0 = u >> 1; a1 = u & 1; nks = 32;
    src = wo; ld = D_; col = 64 * a0 + 32 * a1 + r; dst = ws + OFF_WO + (size_t)a0 * 64 * 1024;
  } else { kind = 3; u -= 174; a0 = u >> 3; a1 = (u >> 2) & 1; a2 = u & 3;
    src = w2; ld = D_; col = 64 * a0 + 32 * a1 + r; dst = ws + OFF_WDN + (size_t)a0 * 224 * 1024;
  }
  float sum = 0.f;
  for (int i = w; i < nks; i += 4) {
    unsigned short e[8];
    size_t doff;
#pragma unroll
    for (int jj = 0; jj < 8; ++jj) {
      int k; bool ok = colok;
      if (kind <= 1) k = 16 * i + 8 * (jj >> 2) + 4 * h2 + (jj & 3);
      else if (kind == 2) { const int d = 16 * (i & 1) + 8 * (jj >> 2) + 4 * h2 + (jj & 3); ok = d < 28; k = 28 * (i >> 1) + (ok ? d : 0); }
      else k = 16 * (28 * a2 + i) + 8 * h2 + jj;
      e[jj] = ok ? src[(size_t)k * ld + (colok ? col : 0)] : (unsigned short)0;
    }
    if (kind <= 1) doff = (size_t)i * 1024;
    else if (kind == 2) doff = (size_t)(((i & 15) * 2 + (i >> 4)) * 2 + a1) * 1024;
    else { const int ks = 28 * a2 + i; doff = (size_t)(((ks % 56) * 2 + (ks / 56)) * 2 + a1) * 1024; }
    u32x4 v;
#pragma unroll
    for (int q = 0; q < 4; ++q) { v[q] = (unsigned)e[2 * q] | ((unsigned)e[2 * q + 1] << 16); sum += bflo(v[q]) + bfhi(v[q]); }
    gst(dst + doff + lane * 16, v);
  }
  if (kind <= 1) {
    sum += swap32(sum);
    if (h2 == 0) red[w][r] = sum;
    __syncthreads();
    if (tid < 32) cs[tid] = red[0][tid] + red[1][tid] + red[2][tid] + red[3][tid];
  }
}

DEV void cluster_of(int& rb, int& mem) { const int b = blockIdx.x; const int idx = b >> 3; rb = 2 * (b & 7) + (idx >> 4); mem = idx & 15; }

__global__ __launch_bounds__(256, 2) void k_qkv(char* __restrict__ ws, int layer) {
  extern __shared__ __attribute__((aligned(16))) char smem[];
  const int tid = threadIdx.x, lane = tid & 63, w = __builtin_amdgcn_readfirstlane(tid >> 6), r = lane & 31, h2 = lane >> 5;
  int rb, j; cluster_of(rb, j);
  const int tt = 4 * rb + w;
  const unsigned lds0 = (unsigned)(uintptr_t)smem;
  dma_block(ws + OFF_WQKV + (size_t)j * 84 * 1024, lds0, 84, w, lane * 16);
  const char* xa = ws + OFF_XA + (size_t)(2 * layer) * SZ_XA + (size_t)tt * KSD * 1024 + lane * 16;
  u32x4 af[KSD];
#pragma unroll
  for (int ks = 0; ks < KSD; ++ks) af[ks] = gld(xa + ks * 1024);
  const Stats st = load_stats(ws + OFF_ST + (size_t)(2 * layer) * SZ_ST, tt, lane);
  const GAS float* cs = (const GAS float*)(ws + OFF_CSQ) + j * 96;
  f32x4 csk[4], csq[4];
#pragma unroll
  for (int g = 0; g < 4; ++g) { csk[g] = *(const GAS f32x4*)(cs + 8 * g + 4 * h2); csq[g] = *(const GAS f32x4*)(cs + 64 + 8 * g + 4 * h2); }
  const float csv = cs[32 + r];
  LAS f32x2* stl = (LAS f32x2*)(smem + 84 * 1024) + w * 32;
  if (h2 == 0) { f32x2 o = {st.rs, st.mean * st.rs}; stl[r] = o; }
  WAIT_ALL_BAR();
  const LAS char* wl = (const LAS char*)smem + lane * 16;
  const size_t kvoff = (size_t)layer * SZ_KV + ((size_t)tt * 16 + j) * 2048 + lane * 16;
  {
    f32x16 acc = {};
#pragma unroll
    for (int ks = 0; ks < KSD; ++ks) acc = mfma(lds_rd(wl, ks * 1024), af[ks], acc);
    u32x4 o[2];
#pragma unroll
    for (int g = 0; g < 4; ++g) {
      float v[4];
#pragma unroll
      for (int e = 0; e < 4; ++e) v[e] = st.rs * (acc[4 * g + e] - st.mean * csk[g][e]);
      o[g >> 1][(g & 1) * 2] = pk2(v[0], v[1]); o[g >> 1][(g & 1) * 2 + 1] = pk2(v[2], v[3]);
    }
    gst(ws + OFF_KF + kvoff, o[0]); gst(ws + OFF_KF + kvoff + 1024, o[1]);
  }
  {
    f32x16 acc = {};
#pragma unroll
    for (int ks = 0; ks < KSD; ++ks) acc = mfma(af[ks], lds_rd(wl, (28 + ks) * 1024), acc);
    u32x4 o[2];
#pragma unroll
    for (int g = 0; g < 4; ++g) {
      float v[4];
#pragma unroll
      for (int e = 0; e < 4; ++e) {
        f32x2 s = stl[8 * g + 4 * h2 + e];
        v[e] = s.x * acc[4 * g + e] - s.y * csv;
        if (r == 28) v[e] = 1.0f;
      }
      o[g >> 1][(g & 1) * 2] = pk2(v[0], v[1]); o[g >> 1][(g & 1) * 2 + 1] = pk2(v[2], v[3]);
    }
    gst(ws + OFF_VF + kvoff, o[0]); gst(ws + OFF_VF + kvoff + 1024, o[1]);
  }
  {
    f32x16 acc = {};
#pragma unroll
    for (int ks = 0; ks < KSD; ++ks) acc = mfma(lds_rd(wl, (56 + ks) * 1024), af[ks], acc);
    const float qs = st.rs * (0.18898223650461362f * 1.4426950408889634f);
    u32x4 o[2];
#pragma unroll
    for (int g = 0; g < 4; ++g) {
      float v[4];
#pragma unroll
      for (int e = 0; e < 4; ++e) v[e] = qs * (acc[4 * g + e] - st.mean * csq[g][e]);
      o[g >> 1][(g & 1) * 2] = pk2(v[0], v[1]); o[g >> 1][(g & 1) * 2 + 1] = pk2(v[2], v[3]);
    }
    gst(ws + OFF_QF + kvoff, o[0]); gst(ws + OFF_QF + kvoff + 1024, o[1]);
  }
}

__global__ __launch_bounds__(256, 2) void k_attn(char* __restrict__ ws, int layer) {
  const int tid = threadIdx.x, lane = tid & 63, w = __builtin_amdgcn_readfirstlane(tid >> 6), r = lane & 31, h2 = lane >> 5;
  int rb, j; cluster_of(rb, j);
  const int tt = 4 * rb + w;
  const size_t lo = (size_t)layer * SZ_KV + (size_t)j * 2048 + lane * 16;
  u32x4 qf0 = gld(ws + OFF_QF + lo + (size_t)tt * 32768), qf1 = gld(ws + OFF_QF + lo + (size_t)tt * 32768 + 1024);
  u32x4 kf[9][2], vf[9][2];
#pragma unroll
  for (int it = 0; it < 9; ++it) {
    const int kt = max(tt - 8 + it, 0);
    kf[it][0] = gld(ws + OFF_KF + lo + (size_t)kt * 32768); kf[it][1] = gld(ws + OFF_KF + lo + (size_t)kt * 32768 + 1024);
  }
#pragma unroll
  for (int it = 0; it < 9; ++it) {
    const int kt = max(tt - 8 + it, 0);
    vf[it][0] = gld(ws + OFF_VF + lo + (size_t)kt * 32768); vf[it][1] = gld(ws + OFF_VF + lo + (size_t)kt * 32768 + 1024);
  }
  const float NEG = -1e30f;
  float m = NEG;
  {
    f32x16 cd, co;
#pragma unroll
    for (int i = 0; i < 16; ++i) { const int key = (i & 3) + 8 * (i >> 2) + 4 * h2; cd[i] = key <= r ? 0.f : NEG; co[i] = key > r ? 0.f : NEG; }
    const f32x16 z = {};
#pragma unroll
    for (int it = 0; it < 9; ++it) {
      if (tt - 8 + it >= 0) {
        f32x16 s = mfma(kf[it][0], qf0, it == 0 ? co : it == 8 ? cd : z);
        s = mfma(kf[it][1], qf1, s);
#pragma unroll
        for (int i = 0; i < 16; i += 2) m = fmaxf(fmaxf(m, s[i]), s[i + 1]);
      }
    }
  }
  m = fmaxf(m, swap32(m));
  f32x16 o = {};
  {
    f32x16 cd, co, cm;
#pragma unroll
    for (int i = 0; i < 16; ++i) { const int key = (i & 3) + 8 * (i >> 2) + 4 * h2; cd[i] = key <= r ? -m : NEG; co[i] = key > r ? -m : NEG; cm[i] = -m; }
#pragma unroll
    for (int it = 0; it < 9; ++it) {
      if (tt - 8 + it >= 0) {
        f32x16 s = mfma(kf[it][0], qf0, it == 0 ? co : it == 8 ? cd : cm);
        s = mfma(kf[it][1], qf1, s);
        u32x4 p[2];
#pragma unroll
        for (int q = 0; q < 8; ++q) p[q >> 2][q & 3] = pk2(__builtin_amdgcn_exp2f(s[2 * q]), __builtin_amdgcn_exp2f(s[2 * q + 1]));
        o = mfma(vf[it][0], p[0], o);
        o = mfma(vf[it][1], p[1], o);
      }
    }
  }
  const float l = __shfl(o[12], 32 + r);
  const float inv = 1.0f / l;
  u32x4 ob[2];
#pragma unroll
  for (int q = 0; q < 8; ++q) ob[q >> 2][q & 3] = pk2(o[2 * q] * inv, o[2 * q + 1] * inv);
  char* oa = ws + OFF_OA + (size_t)layer * SZ_OA + ((size_t)tt * KSO + 2 * j) * 1024 + lane * 16;
  gst(oa, ob[0]); gst(oa + 1024, ob[1]);
}

template <int NST, bool FINAL>
__global__ __launch_bounds__(256, 2) void k_proj(char* __restrict__ ws, size_t off_w, size_t off_act, int xin, unsigned short* __restrict__ out) {
  extern __shared__ __attribute__((aligned(16))) char smem[];
  const int tid = threadIdx.x, lane = tid & 63, w = __builtin_amdgcn_readfirstlane(tid >> 6), r = lane & 31, h2 = lane >> 5;
  int rb, mem; cluster_of(rb, mem);
  if (mem >= 14) return;
  const int th = mem / 7, fs = mem % 7;
  const int tt2 = w & 1, kh = w >> 1;
  const int tt = 4 * rb + 2 * th + tt2;
  const unsigned lds0 = (unsigned)(uintptr_t)smem;
  constexpr int PRE = NST <= 36 ? NST : 36;
  constexpr int XCH = PRE * 4096;
  const char* wsrc = ws + off_w + (size_t)fs * NST * 4096;
  dma_block(wsrc, lds0, PRE * 4, w, lane * 16);
  const char* act = ws + off_act + ((size_t)tt * (2 * NST) + kh * NST) * 1024 + lane * 16;
  constexpr int WIN = 16;
  u32x4 af[WIN];
#pragma unroll
  for (int i = 0; i < WIN; ++i) af[i] = gld(act + i * 1024);
  const char* xr = ws + OFF_XA + (size_t)xin * SZ_XA + ((size_t)tt * KSD + 4 * fs + 2 * kh) * 1024 + lane * 16;
  const u32x4 x0 = gld(xr), x1 = gld(xr + 1024);
  asm volatile("s_waitcnt vmcnt(18)\n\ts_barrier" ::: "memory");
  const LAS char* wl = (const LAS char*)smem + kh * 2048 + lane * 16;
  f32x16 acc0 = {}, acc1 = {};
#pragma unroll
  for (int i = 0; i < NST; ++i) {
    if (NST > PRE && i == NST - PRE) {
      LGKM_BAR();
      dma_block(wsrc + (size_t)PRE * 4096, lds0, (NST - PRE) * 4, w, lane * 16);
    }
    if (NST > PRE && i == PRE) { WAIT_ALL_BAR(); }
    const int slot = i < PRE ? i : i - PRE;
    const u32x4 a = af[i % WIN];
    if (i + WIN < NST) af[i % WIN] = gld(act + (i + WIN) * 1024);
    acc0 = mfma(lds_rd(wl, slot * 4096), a, acc0);
    acc1 = mfma(lds_rd(wl, slot * 4096 + 1024), a, acc1);
  }
  LAS f32x4* xs = (LAS f32x4*)(smem + XCH) + (tt2 * 2 + kh) * 256 + lane;
  LAS f32x4* xrv = (LAS f32x4*)(smem + XCH) + (tt2 * 2 + (kh ^ 1)) * 256 + lane;
  {
    const f32x16& snd = kh ? acc0 : acc1;
#pragma unroll
    for (int g = 0; g < 4; ++g) { f32x4 v = {snd[4 * g], snd[4 * g + 1], snd[4 * g + 2], snd[4 * g + 3]}; xs[g * 64] = v; }
  }
  LGKM_BAR();
  f32x16 acc = kh ? acc1 : acc0;
#pragma unroll
  for (int g = 0; g < 4; ++g) { f32x4 v = xrv[g * 64]; acc[4 * g] += v.x; acc[4 * g + 1] += v.y; acc[4 * g + 2] += v.z; acc[4 * g + 3] += v.w; }
  u32x4 o[2]; float s1 = 0.f, s2 = 0.f;
#pragma unroll
  for (int q = 0; q < 8; ++q) {
    const unsigned xv = q < 4 ? x0[q] : x1[q - 4];
    const unsigned pv = pk2(bflo(xv) + acc[2 * q], bfhi(xv) + acc[2 * q + 1]);
    o[q >> 2][q & 3] = pv;
    const float a = bflo(pv), b = bfhi(pv); s1 += a + b; s2 += a * a + b * b;
  }
  if (FINAL) {
    unsigned short* orow = out + (size_t)(tt * 32 + r) * D_ + 64 * fs + 32 * kh + 4 * h2;
#pragma unroll
    for (int g = 0; g < 4; ++g) { u32x2 v = {o[g >> 1][(g & 1) * 2], o[g >> 1][(g & 1) * 2 + 1]}; *(GAS u32x2*)(orow + 8 * g) = v; }
  } else {
    char* xo = ws + OFF_XA + (size_t)(xin + 1) * SZ_XA + ((size_t)tt * KSD + 4 * fs + 2 * kh) * 1024 + lane * 16;
    gst(xo, o[0]); gst(xo + 1024, o[1]);
    s1 += swap32(s1); s2 += swap32(s2);
    if (h2 == 0) { f32x2 v = {s1, s2}; *(GAS f32x2*)(ws + OFF_ST + (size_t)(xin + 1) * SZ_ST + ((size_t)tt * 14 + 2 * fs + kh) * 256 + r * 8) = v; }
  }
}

__global__ __launch_bounds__(256, 2) void k_gu(char* __restrict__ ws, int layer) {
  extern __shared__ __attribute__((aligned(16))) char smem[];
  const int tid = threadIdx.x, lane = tid & 63, w = __builtin_amdgcn_readfirstlane(tid >> 6), h2 = lane >> 5;
  int rb, j; cluster_of(rb, j);
  const int tt = 4 * rb + w;
  const unsigned lds0 = (unsigned)(uintptr_t)smem;
  const char* wsrc = ws + OFF_WGU + (size_t)j * 196 * 1024;
  dma_block(wsrc, lds0, 140, w, lane * 16);
  const int xin = 2 * layer + 1;
  const char* xa = ws + OFF_XA + (size_t)xin * SZ_XA + (size_t)tt * KSD * 1024 + lane * 16;
  u32x4 af[KSD];
#pragma unroll
  for (int ks = 0; ks < KSD; ++ks) af[ks] = gld(xa + ks * 1024);
  const Stats st = load_stats(ws + OFF_ST + (size_t)xin * SZ_ST, tt, lane);
  const GAS float* cs = (const GAS float*)(ws + OFF_CSG) + j * 7 * 32 + 4 * h2;
  WAIT_ALL_BAR();
  const LAS char* wl = (const LAS char*)smem + lane * 16;
  char* ha = ws + OFF_HA + (size_t)layer * SZ_HA + ((size_t)tt * KSF + 7 * j) * 1024 + lane * 16;
#pragma unroll
  for (int t = 0; t < 7; ++t) {
    if (t == 1 || t == 2) { LGKM_BAR(); dma_block(wsrc + (size_t)(4 + t) * 28 * 1024, lds0 + (t - 1) * 28 * 1024, 28, w, lane * 16); }
    if (t == 5) { WAIT_ALL_BAR(); }
    f32x4 c[4];
#pragma unroll
    for (int g = 0; g < 4; ++g) c[g] = *(const GAS f32x4*)(cs + t * 32 + 8 * g);
    const int slot = t % 5;
    f32x16 acc = {};
#pragma unroll
    for (int ks = 0; ks < KSD; ++ks) acc = mfma(lds_rd(wl, (slot * 28 + ks) * 1024), af[ks], acc);
    u32x4 o;
#pragma unroll
    for (int g = 0; g < 4; ++g) {
      float hv[2];
#pragma unroll
      for (int e = 0; e < 2; ++e) {
        const float gt = st.rs * (acc[4 * g + e] - st.mean * c[g][e]);
        const float up = st.rs * (acc[4 * g + 2 + e] - st.mean * c[g][2 + e]);
        const float sg = __builtin_amdgcn_rcpf(1.0f + __builtin_amdgcn_exp2f(-1.4426950408889634f * gt));
        hv[e] = gt * sg * up;
      }
      o[g] = pk2(hv[0], hv[1]);
    }
    gst(ha + t * 1024, o);
  }
}

}

extern "C" void kernel_launch(void* const* d_in, const int* in_sizes, int n_in, void* d_out, int out_size, void* d_ws, size_t ws_size,
                              hipStream_t stream) {
  const unsigned short* x = (const unsigned short*)d_in[0];
  const unsigned short* wqkv = (const unsigned short*)d_in[1];
  const unsigned short* wo = (const unsigned short*)d_in[2];
  const unsigned short* w1 = (const unsigned short*)d_in[4];
  const unsigned short* w3 = (const unsigned short*)d_in[5];
  const unsigned short* w2 = (const unsigned short*)d_in[6];
  char* ws = (char*)d_ws;
  (void)hipFuncSetAttribute((const void*)k_qkv, hipFuncAttributeMaxDynamicSharedMemorySize, 85 * 1024);
  (void)hipFuncSetAttribute((const void*)k_gu, hipFuncAttributeMaxDynamicSharedMemorySize, 140 * 1024);
  (void)hipFuncSetAttribute((const void*)k_proj<16, false>, hipFuncAttributeMaxDynamicSharedMemorySize, 80 * 1024);
  (void)hipFuncSetAttribute((const void*)k_proj<56, false>, hipFuncAttributeMaxDynamicSharedMemorySize, 160 * 1024);
  (void)hipFuncSetAttribute((const void*)k_proj<56, true>, hipFuncAttributeMaxDynamicSharedMemorySize, 160 * 1024);
  k_prep<<<64 + 174 + 56, 256, 0, stream>>>(x, wqkv, wo, w1, w3, w2, ws);
  for (int layer = 0; layer < 2; ++layer) {
    k_qkv<<<256, 256, 85 * 1024, stream>>>(ws, layer);
    k_attn<<<256, 256, 0, stream>>>(ws, layer);
    k_proj<16, false><<<256, 256, 80 * 1024, stream>>>(ws, OFF_WO, OFF_OA + layer * SZ_OA, 2 * layer, nullptr);
    k_gu<<<256, 256, 140 * 1024, stream>>>(ws, layer);
    if (layer == 0) k_proj<56, false><<<256, 256, 160 * 1024, stream>>>(ws, OFF_WDN, OFF_HA + layer * SZ_HA, 2 * layer + 1, nullptr);
    else k_proj<56, true><<<256, 256, 160 * 1024, stream>>>(ws, OFF_WDN, OFF_HA + layer * SZ_HA, 2 * layer + 1, (unsigned short*)d_out);
  }
}
```

```cpp
#include <hip/hip_runtime.h>
#include <stdint.h>
#include <utility>

typedef __attribute__((ext_vector_type(8))) __bf16 bf16x8;
typedef __attribute__((ext_vector_type(2))) __bf16 bf16x2;
typedef __attribute__((ext_vector_type(2))) float f32x2;
typedef __attribute__((ext_vector_type(4))) float f32x4;
typedef __attribute__((ext_vector_type(16))) float f32x16;
typedef __attribute__((ext_vector_type(4))) unsigned u32x4;
typedef __attribute__((ext_vector_type(2))) unsigned u32x2;
#define DEV __device__ __forceinline__
#define LAS __attribute__((address_space(3)))
#define GAS __attribute__((address_space(1)))
typedef GAS unsigned gu32;
#define RLX __ATOMIC_RELAXED, __HIP_MEMORY_SCOPE_AGENT

namespace {

constexpr int D_ = 448, F_ = 1792;
constexpr int NTT = 64;
constexpr int KSD = 28;
constexpr int KSO = 32;
constexpr int KSF = 112;
constexpr int CHB = 28672;

constexpr unsigned OFF_FLAG = 0;
constexpr int FL_KV = 0;
constexpr int FL_O = FL_KV + 512;
constexpr int FL_X1 = FL_O + 32;
constexpr int FL_H = FL_X1 + 32;
constexpr int FL_X2 = FL_H + 64;
constexpr int FL_TMO = FL_X2 + 16;
constexpr int NFLAG = FL_TMO + 1;
constexpr unsigned OFF_WQKV = 96 * 1024;
constexpr unsigned OFF_WO   = OFF_WQKV + 16 * 3 * CHB;
constexpr unsigned OFF_WGU  = OFF_WO + 7 * 64 * 1024;
constexpr unsigned OFF_WDN  = OFF_WGU + 16 * 7 * CHB;
constexpr unsigned OFF_CSQ  = OFF_WDN + 7 * 224 * 1024;
constexpr unsigned OFF_CSG  = OFF_CSQ + 16 * 3 * 32 * 4;
constexpr unsigned SZ_XA    = NTT * KSD * 1024;
constexpr unsigned OFF_XA   = OFF_CSG + 16 * 7 * 32 * 4;
constexpr unsigned SZ_ST    = NTT * 14 * 32 * 8;
constexpr unsigned OFF_ST   = OFF_XA + 4 * SZ_XA;
constexpr unsigned SZ_KV    = NTT * 16 * 4096;
constexpr unsigned OFF_KV   = OFF_ST + 4 * SZ_ST;
constexpr unsigned SZ_OA    = NTT * KSO * 1024;
constexpr unsigned OFF_OA   = OFF_KV + 2 * SZ_KV;
constexpr unsigned SZ_HA    = NTT * KSF * 1024;
constexpr unsigned OFF_HA   = OFF_OA + 2 * SZ_OA;
constexpr unsigned WS_END   = OFF_HA + 2 * SZ_HA;
constexpr unsigned OFF_STAMP = (WS_END + 4095) / 4096 * 4096;
static_assert(NFLAG * 128 <= OFF_WQKV, "flags");
static_assert(OFF_XA % 128 == 0 && OFF_ST % 128 == 0 && OFF_KV % 128 == 0 && OFF_OA % 128 == 0 && OFF_HA % 128 == 0, "lines");

#define STAMP(i) do {} while (0)
#define FS(i) do {} while (0)

template <class F, int... I> DEV void static_for_impl(F&& f, std::integer_sequence<int, I...>) { (f(std::integral_constant<int, I>{}), ...); }
template <int N, class F> DEV void static_for(F&& f) { static_for_impl(f, std::make_integer_sequence<int, N>{}); }

DEV int lane_id() { int l; asm volatile("v_mbcnt_lo_u32_b32 %0, -1, 0\n\tv_mbcnt_hi_u32_b32 %0, -1, %0" : "=v"(l)); return l; }
DEV unsigned pk2(float a, float b) { f32x2 v = {a, b}; return __builtin_bit_cast(unsigned, __builtin_convertvector(v, bf16x2)); }
DEV f32x16 mfma(u32x4 a, u32x4 b, f32x16 c) {
  return __builtin_amdgcn_mfma_f32_32x32x16_bf16(__builtin_bit_cast(bf16x8, a), __builtin_bit_cast(bf16x8, b), c, 0, 0, 0);
}
DEV float bflo(unsigned u) { return __uint_as_float(u << 16); }
DEV float bfhi(unsigned u) { return __uint_as_float(u & 0xffff0000u); }
DEV float swap32(float v) {
  const unsigned u = __float_as_uint(v);
  auto rr = __builtin_amdgcn_permlane32_swap(u, u, false, false);
  return __uint_as_float((threadIdx.x & 32) ? rr[0] : rr[1]);
}
DEV float sum32(float v) {
  const unsigned u = __float_as_uint(v);
  auto rr = __builtin_amdgcn_permlane32_swap(u, u, false, false);
  return __uint_as_float(rr[0]) + __uint_as_float(rr[1]);
}
DEV float max32(float v) {
  const unsigned u = __float_as_uint(v);
  auto rr = __builtin_amdgcn_permlane32_swap(u, u, false, false);
  return fmaxf(__uint_as_float(rr[0]), __uint_as_float(rr[1]));
}
DEV void dma7(const char* g, unsigned voff, unsigned lds) {
  unsigned keep;
  const char* g2 = g + 4096;
  lds = __builtin_amdgcn_readfirstlane(lds);
  asm volatile("s_mov_b32 %0, m0\n\ts_mov_b32 m0, %2\n\ts_nop 0\n\t"
               "global_load_lds_dwordx4 %1, %3 offset:0\n\t"
               "global_load_lds_dwordx4 %1, %3 offset:1024\n\t"
               "global_load_lds_dwordx4 %1, %3 offset:2048\n\t"
               "global_load_lds_dwordx4 %1, %3 offset:3072\n\t"
               "s_add_u32 m0, m0, 0x1000\n\ts_nop 0\n\t"
               "global_load_lds_dwordx4 %1, %4 offset:0\n\t"
               "global_load_lds_dwordx4 %1, %4 offset:1024\n\t"
               "global_load_lds_dwordx4 %1, %4 offset:2048\n\t"
               "s_mov_b32 m0, %0"
               : "=&s"(keep) : "v"(voff), "s"(lds), "s"(g), "s"(g2) : "memory", "scc");
}
template <int N> DEV void wait_vm() { asm volatile("s_waitcnt vmcnt(%0)" ::"n"(N) : "memory"); }
template <int N> DEV void wait_vm_bar() { asm volatile("s_waitcnt vmcnt(%0) lgkmcnt(0)\n\ts_barrier" ::"n"(N) : "memory"); }
DEV void bar_lgkm() { asm volatile("s_waitcnt lgkmcnt(0)\n\ts_barrier" ::: "memory"); }
DEV u32x4 lds_rd(const LAS char* base, int off) { return *(const LAS u32x4*)(base + off); }
DEV void lds_wr(LAS char* base, int off, u32x4 v) { *(LAS u32x4*)(base + off) = v; }
DEV u32x4 gld(const char* p) { return *(const GAS u32x4*)(p); }
DEV void gst(char* p, u32x4 v) { *(GAS u32x4*)(p) = v; }
typedef __amdgpu_buffer_rsrc_t rsrc_t;
DEV u32x4 hld(rsrc_t rs, unsigned voff, unsigned soff) { return __builtin_amdgcn_raw_buffer_load_b128(rs, voff, soff, 16); }
DEV void hst(rsrc_t rs, unsigned voff, unsigned soff, u32x4 v) { __builtin_amdgcn_raw_buffer_store_b128(v, rs, voff, soff, 16); }

constexpr unsigned SPIN_CAP = 1u << 17;
DEV void poll_ge(char* ws, int flag, unsigned target) {
  gu32* c = (gu32*)(ws + OFF_FLAG + (size_t)flag * 128);
  gu32* tmo = (gu32*)(ws + OFF_FLAG + (size_t)FL_TMO * 128);
  for (unsigned sp = 0; __hip_atomic_load(c, RLX) < target;) {
    __builtin_amdgcn_s_sleep(1);
    if (++sp > SPIN_CAP) { __hip_atomic_store(tmo, 1u, RLX); break; }
    if ((sp & 1023u) == 0u && __hip_atomic_load(tmo, RLX) != 0u) break;
  }
}
DEV void sig_add(char* ws, int flag) { (void)__hip_atomic_fetch_add((gu32*)(ws + OFF_FLAG + (size_t)flag * 128), 1u, RLX); }

struct Stats { float mean, rs; };
DEV Stats load_stats(rsrc_t rs, unsigned off_st, int tt, int lane) {
  const int r = lane & 31, h2 = lane >> 5;
  float s1 = 0.f, s2 = 0.f;
  u32x2 v[7];
#pragma unroll
  for (int i = 0; i < 7; ++i) v[i] = __builtin_amdgcn_raw_buffer_load_b64(rs, h2 * 1792 + r * 8 + i * 256, off_st + tt * 3584, 16);
#pragma unroll
  for (int i = 0; i < 7; ++i) { s1 += __uint_as_float(v[i].x); s2 += __uint_as_float(v[i].y); }
  s1 = sum32(s1); s2 = sum32(s2);
  Stats s; s.mean = s1 * (1.0f / D_);
  const float var = fmaxf(s2 * (1.0f / D_) - s.mean * s.mean, 0.f);
  s.rs = __builtin_amdgcn_rsqf(var + 1e-5f);
  return s;
}
constexpr int G1T = 5, G1S = 8 * G1T;
__host__ __device__ constexpr int kk_of(int i) { return i < G1S ? 7 * (i / G1T) + i % G1T : 7 * ((i - G1S) / (7 - G1T)) + G1T + (i - G1S) % (7 - G1T); }

typedef short s16x4 __attribute__((ext_vector_type(4)));
DEV u32x2 tr_read(const LAS char* p) { return __builtin_bit_cast(u32x2, __builtin_amdgcn_ds_read_tr16_b64_v4i16((LAS s16x4*)p)); }
template <int KIND>
DEV void prep_unit(const unsigned short* __restrict__ src, const unsigned short* __restrict__ src2, int ld, int col, char* __restrict__ dst, float* cs, int a1, int a2, char* smem) {
  const int tid = threadIdx.x, lane = tid & 63, w = tid >> 6, r = lane & 31, h2 = lane >> 5;
  LAS char* img = (LAS char*)smem;
  if (KIND == 0) {
    u32x2 v[14];
    const int p = tid & 7, row0 = tid >> 3;
#pragma unroll
    for (int n = 0; n < 14; ++n) v[n] = p < 7 ? *(const GAS u32x2*)(src + (size_t)(row0 + 32 * n) * ld + col + 4 * p) : u32x2{0u, 0u};
#pragma unroll
    for (int n = 0; n < 14; ++n) *(LAS u32x2*)(img + (row0 + 32 * n) * 64 + p * 8) = v[n];
  } else {
    u32x4 v[7];
    const int p = tid & 3, row0 = tid >> 2;
    const unsigned short* sp = KIND == 1 ? (p < 2 ? src + col + 8 * p : src2 + col + 8 * (p - 2)) : src + col + 8 * p;
#pragma unroll
    for (int n = 0; n < 7; ++n) v[n] = *(const GAS u32x4*)(sp + (size_t)((KIND == 3 ? 448 * a2 : 0) + row0 + 64 * n) * ld);
#pragma unroll
    for (int n = 0; n < 7; ++n) {
      const int row = row0 + 64 * n;
      const int irow = KIND == 2 ? 32 * (row / 28) + row % 28 : row;
      *(LAS u32x4*)(img + irow * 64 + p * 16) = v[n];
    }
    if (KIND == 2) { const int head = tid >> 4, d = 28 + ((tid >> 2) & 3); *(LAS u32x4*)(img + (32 * head + d) * 64 + p * 16) = u32x4{0u, 0u, 0u, 0u}; }
  }
  __syncthreads();
  constexpr int NKS = KIND == 2 ? 32 : 28;
  const LAS char* rd = img + (4 * h2 + ((lane & 15) >> 2)) * 64 + ((lane >> 4) & 1) * 32 + (lane & 3) * 8;
  float sum = 0.f;
#pragma unroll
  for (int n = 0; n < NKS / 4; ++n) {
    const int i = w + 4 * n;
    const u32x2 lo = tr_read(rd + i * 1024), hi = tr_read(rd + i * 1024 + 512);
    const u32x4 v = {lo.x, lo.y, hi.x, hi.y};
    size_t doff;
    if (KIND <= 1) doff = (size_t)i * 1024;
    else if (KIND == 2) doff = (size_t)(((i & 15) * 2 + (i >> 4)) * 2 + a1) * 1024;
    else {
      const int ks = 28 * a2 + i, kh = ks / 56, kk = ks % 56, j8 = kk / 7, t = kk % 7;
      const int step = t < G1T ? G1T * j8 + t : G1S + (7 - G1T) * j8 + (t - G1T);
      doff = (size_t)((step * 2 + kh) * 2 + a1) * 1024;
    }
    gst(dst + doff + lane * 16, v);
    if (KIND <= 1) {
#pragma unroll
      for (int q = 0; q < 4; ++q) sum += bflo(v[q]) + bfhi(v[q]);
    }
  }
  if (KIND <= 1) {
    sum = sum32(sum);
    __syncthreads();
    LAS float* red = (LAS float*)img;
    if (h2 == 0) red[w * 32 + r] = sum;
    __syncthreads();
    if (tid < 32) cs[tid] = (red[tid] + red[32 + tid]) + (red[64 + tid] + red[96 + tid]);
  }
}
constexpr int PREP_LDS = 32768;
constexpr int PREP_BLOCKS = 64 + 112 + 48 + 56 + 14;
__global__ __launch_bounds__(256) void k_prep(const unsigned short* __restrict__ x, const unsigned short* __restrict__ wqkv,
                                              const unsigned short* __restrict__ wo, const unsigned short* __restrict__ w1,
                                              const unsigned short* __restrict__ w3, const unsigned short* __restrict__ w2, char* __restrict__ ws) {
  extern __shared__ __attribute__((aligned(16))) char smem[];
  const int b = blockIdx.x, tid = threadIdx.x, lane = tid & 63, w = tid >> 6, r = lane & 31, h2 = lane >> 5;
  if (b >= PREP_BLOCKS - 64) {
    const int tt = b - (PREP_BLOCKS - 64);
    if (tt < 3) { const int f = tt * 256 + tid; if (f < NFLAG) *(GAS unsigned*)(ws + OFF_FLAG + (size_t)f * 128) = 0u; }
    LAS char* img = (LAS char*)smem;
    u32x4 v[7];
#pragma unroll
    for (int n = 0; n < 7; ++n) { const int c = tid + 256 * n; v[n] = *(const GAS u32x4*)(x + (size_t)tt * 32 * D_ + c * 8); }
#pragma unroll
    for (int n = 0; n < 7; ++n) { const int c = tid + 256 * n; *(LAS u32x4*)(img + (c / 56) * 912 + (c % 56) * 16) = v[n]; }
    __syncthreads();
    for (int ft = w; ft < 14; ft += 4) {
      float s1 = 0.f, s2 = 0.f;
#pragma unroll
      for (int s = 0; s < 2; ++s) {
        const int ks = 2 * ft + s;
        const u32x2 a = *(const LAS u32x2*)(img + r * 912 + ks * 32 + 8 * h2), c = *(const LAS u32x2*)(img + r * 912 + ks * 32 + 16 + 8 * h2);
        const u32x4 o = {a.x, a.y, c.x, c.y};
        gst(ws + OFF_XA + ((size_t)tt * KSD + ks) * 1024 + lane * 16, o);
#pragma unroll
        for (int q = 0; q < 4; ++q) { const float lo = bflo(o[q]), hi = bfhi(o[q]); s1 += lo + hi; s2 += lo * lo + hi * hi; }
      }
      s1 = sum32(s1); s2 = sum32(s2);
      if (h2 == 0) { f32x2 o = {s1, s2}; *(GAS f32x2*)(ws + OFF_ST + ((size_t)tt * 14 + ft) * 256 + r * 8) = o; }
    }
    return;
  }
  int u = b;
  if (u < 48) {
    const int j = u / 3, p = u % 3;
    prep_unit<0>(wqkv, nullptr, 3 * D_, (p == 2 ? 0 : p == 0 ? D_ : 2 * D_) + 28 * j, ws + OFF_WQKV + (size_t)u * CHB, (float*)(ws + OFF_CSQ) + u * 32, 0, 0, smem);
  } else if (u < 48 + 112) {
    u -= 48; const int j = u / 7, t = u % 7;
    prep_unit<1>(w1, w3, F_, 112 * j + 16 * t, ws + OFF_WGU + (size_t)u * CHB, (float*)(ws + OFF_CSG) + u * 32, 0, 0, smem);
  } else if (u < 48 + 112 + 14) {
    u -= 160; const int fs = u >> 1, tile = u & 1;
    prep_unit<2>(wo, nullptr, D_, 64 * fs + 32 * tile, ws + OFF_WO + (size_t)fs * 64 * 1024, nullptr, tile, 0, smem);
  } else {
    u -= 174; const int fs = u >> 3, tile = (u >> 2) & 1, q = u & 3;
    prep_unit<3>(w2, nullptr, D_, 64 * fs + 32 * tile, ws + OFF_WDN + (size_t)fs * 224 * 1024, nullptr, tile, q, smem);
  }
}

constexpr int AUX = 4 * CHB;
constexpr int LDS_BYTES = AUX + 49152;
#define SB() __builtin_amdgcn_sched_barrier(0)
#define PIN(x) asm volatile("" : "+v"(x))
#define PACK16(o, expr) _Pragma("unroll") for (int q_ = 0; q_ < 8; ++q_) { const int i0 = 2 * q_, i1 = 2 * q_ + 1; (void)i0; (void)i1; o[q_ >> 2][q_ & 3] = expr; }
template <class T> DEV void opaque_s(T& v) { asm volatile("" : "+s"(v)); }

struct StatLd { u32x2 v[7]; };
DEV void stats_issue(StatLd& s, rsrc_t rs, unsigned soff, int lane) {
  const unsigned vo = (lane >> 5) * 1792 + (lane & 31) * 8;
#pragma unroll
  for (int i = 0; i < 7; ++i) s.v[i] = __builtin_amdgcn_raw_buffer_load_b64(rs, vo + i * 256, soff, 16);
}
DEV Stats stats_finish(const StatLd& s) {
  float s1 = 0.f, s2 = 0.f;
#pragma unroll
  for (int i = 0; i < 7; ++i) { s1 += __uint_as_float(s.v[i].x); s2 += __uint_as_float(s.v[i].y); }
  s1 = sum32(s1); s2 = sum32(s2);
  Stats o; o.mean = s1 * (1.0f / D_);
  const float var = fmaxf(s2 * (1.0f / D_) - o.mean * o.mean, 0.f);
  o.rs = __builtin_amdgcn_rsqf(var + 1e-5f);
  return o;
}

template <int N> DEV void dma_n(rsrc_t rs, unsigned voff, unsigned lds, unsigned soff) {
  if constexpr (N == 4)
    asm volatile("s_mov_b32 m0, %0\n\ts_nop 0\n\tbuffer_load_dwordx4 %1, %2, %3 offen lds\n\tbuffer_load_dwordx4 %1, %2, %3 offen offset:1024 lds\n\t"
                 "buffer_load_dwordx4 %1, %2, %3 offen offset:2048 lds\n\tbuffer_load_dwordx4 %1, %2, %3 offen offset:3072 lds" ::"s"(lds), "v"(voff), "s"(rs), "s"(soff) : "memory");
  else if constexpr (N == 3)
    asm volatile("s_mov_b32 m0, %0\n\ts_nop 0\n\tbuffer_load_dwordx4 %1, %2, %3 offen lds\n\tbuffer_load_dwordx4 %1, %2, %3 offen offset:1024 lds\n\t"
                 "buffer_load_dwordx4 %1, %2, %3 offen offset:2048 lds" ::"s"(lds), "v"(voff), "s"(rs), "s"(soff) : "memory");
  else
    asm volatile("s_mov_b32 m0, %0\n\ts_nop 0\n\tbuffer_load_dwordx4 %1, %2, %3 offen lds\n\tbuffer_load_dwordx4 %1, %2, %3 offen offset:1024 lds" ::"s"(lds), "v"(voff), "s"(rs), "s"(soff) : "memory");
}
DEV void dma_chunk(rsrc_t rs, unsigned voff, unsigned lds, unsigned soff, int w) {
  const unsigned o = (w - 1) * 9216;
  dma_n<4>(rs, voff, lds + o, soff + o); dma_n<4>(rs, voff, lds + o + 4096, soff + o + 4096);
  if (w == 3) dma_n<2>(rs, voff, lds + o + 8192, soff + o + 8192);
  else asm volatile("s_mov_b32 m0, %0\n\ts_nop 0\n\tbuffer_load_dwordx4 %1, %2, %3 offen lds" ::"s"(lds + o + 8192), "v"(voff), "s"(rs), "s"(soff + o + 8192) : "memory");
}
DEV void dma2(rsrc_t rs, unsigned voff, unsigned lds, unsigned soff) {
  asm volatile("s_mov_b32 m0, %0\n\ts_nop 0\n\t"
               "buffer_load_dwordx4 %1, %2, %3 offen lds\n\t"
               "buffer_load_dwordx4 %1, %2, %3 offen offset:1024 lds"
               ::"s"(lds), "v"(voff), "s"(rs), "s"(soff) : "memory");
}
DEV void bar() { asm volatile("s_barrier" ::: "memory"); }
DEV const LAS char* opq(const LAS char* p) { asm volatile("" : "+v"(p)); return p; }

constexpr int DN_WIN = 23;

__global__ __launch_bounds__(512, 2) void k_main(char* __restrict__ ws, unsigned short* __restrict__ out) {
  extern __shared__ __attribute__((aligned(16))) char smem[];
  const int w8_ = __builtin_amdgcn_readfirstlane(threadIdx.x >> 6);
  const bool helper = w8_ >= 4;
  const int w_ = w8_ & 3;
  const int b = blockIdx.x, idx = b >> 3;
#define MAPA 1
  const int x_ = b & 7;
  const int rb_ = MAPA == 0 ? 2 * x_ + (idx >> 4) : MAPA == 1 ? 4 * (x_ & 3) + (idx >> 3) : 8 * (x_ & 1) + (idx >> 2);
  const int j_ = MAPA == 0 ? (idx & 15) : MAPA == 1 ? 8 * (x_ >> 2) + (idx & 7) : 4 * (x_ >> 1) + (idx & 3);
  const int rp_ = 2 * x_ + (idx >> 4), m_ = idx & 15;
  const bool active = m_ < 14;
  const unsigned lds0 = __builtin_amdgcn_readfirstlane((unsigned)(uintptr_t)smem);
  const rsrc_t rs = __builtin_amdgcn_make_buffer_rsrc(ws, 0, WS_END, 0x00020000);
#define IDS() \
  int w = w_, rb = rb_, j = j_, rp = rp_, mm = m_; asm volatile("" : "+s"(w), "+s"(rb), "+s"(j), "+s"(rp), "+s"(mm)); \
  const int lane = lane_id(), r = lane & 31, h2 = lane >> 5; \
  const bool t0 = w == 0 && lane == 0; (void)t0; \
  const int th = mm >= 7 ? 1 : 0, fs = mm - 7 * th, tt2 = w & 1, kh = w >> 1; \
  const int tt = 4 * rb + w, ttp = 4 * rp + 2 * th + tt2; \
  const unsigned voff = lane * 16; \
  const LAS char* ring = (const LAS char*)smem + lane * 16; \
  LAS char* aux = (LAS char*)smem + AUX; \
  (void)r; (void)h2; (void)fs; (void)kh; (void)tt; (void)ttp; (void)voff; (void)ring; (void)aux; (void)tt2; (void)rp;
#define slot(c) opq(ring + ((c) & 3) * CHB)
  constexpr int CSQ_L = 0;
  constexpr int CSG_L = 16384;
  constexpr int WOT_L = 24576;

  if (helper) {
    auto issue = [&](int c, int l) {
      const int sl = c & 3;
      if (c >= 20) { c -= 20; ++l; }
      const bool proj = (c >= 3 && c < 5) || c >= 12;
      int w = w_, j = j_, mm = m_; asm volatile("" : "+s"(w), "+s"(j), "+s"(mm));
      const int fs = mm >= 7 ? mm - 7 : mm;
      unsigned src;
      if (c < 3) src = OFF_WQKV + (j * 3 + c) * CHB;
      else if (c < 5) src = OFF_WO + fs * 65536 + (c - 3) * CHB;
      else if (c < 12) src = OFF_WGU + (j * 7 + c - 5) * CHB;
      else src = OFF_WDN + fs * 229376 + (c - 12) * CHB;
      if (!(l < 2 && (active || !proj))) src = WS_END;
      if (w != 0) dma_chunk(rs, lane_id() * 16, lds0 + sl * CHB, src, w);
    };
    auto stage_csq = [&]() { IDS(); if (w == 3 && lane < 24) *(LAS u32x4*)(aux + CSQ_L + lane * 16) = gld(ws + OFF_CSQ + j * 384 + lane * 16); };
    stage_csq();
    issue(0, 0); issue(1, 0); issue(2, 0);
#pragma unroll 1
    for (int l = 0; l < 2; ++l) {
      IDS();
      if (l == 1 && t0) poll_ge(ws, FL_X2 + rb, 14);
      wait_vm_bar<0>();
      bar();
      bar();
      if (t0) sig_add(ws, FL_KV + (l * 16 + rb) * 16 + j);
      issue(4, l);
      if (rb >= 1 && t0) { poll_ge(ws, FL_KV + (l * 16 + rb - 1) * 16 + j, 1); if (rb >= 2) poll_ge(ws, FL_KV + (l * 16 + rb - 2) * 16 + j, 1); }
      bar();
      if (rb >= 1) {
        u32x4 hv[8];
#pragma unroll
        for (int n = 0; n < 2; ++n) {
          const int rel = 2 * w + n, kt = max(4 * rb - 8 + rel, 0);
#pragma unroll
          for (int q = 0; q < 4; ++q) hv[n * 4 + q] = hld(rs, voff + q * 1024, OFF_KV + l * SZ_KV + (kt * 16 + j) * 4096);
        }
        SB();
#pragma unroll
        for (int n = 0; n < 8; ++n) lds_wr(aux + lane * 16, (2 * w) * 4096 + n * 1024, hv[n]);
      }
      bar_lgkm();
      issue(3, l); issue(5, l); issue(6, l);
      bar();
      if (t0) sig_add(ws, FL_O + l * 16 + rb);
      if (w == 3 && lane < 56) *(LAS u32x4*)(aux + CSG_L + lane * 16) = gld(ws + OFF_CSG + j * 896 + lane * 16);
      if (active) {
        if (w == 1 || w == 2) dma_n<4>(rs, voff, lds0 + AUX + WOT_L + (w - 1) * 4096, OFF_WO + fs * 65536 + 2 * CHB + (w - 1) * 4096);
        if (t0) poll_ge(ws, FL_O + l * 16 + rp, 16);
        wait_vm_bar<0>();
        bar();
        bar();
        if (t0) sig_add(ws, FL_X1 + l * 16 + rp);
      }
      issue(7, l); issue(8, l);
      if (t0) poll_ge(ws, FL_X1 + l * 16 + rb, 14);
      asm volatile("s_waitcnt vmcnt(18) lgkmcnt(0)\n\ts_barrier" ::: "memory");
      wait_vm_bar<9>();
      issue(9, l); issue(10, l);
      wait_vm_bar<18>();
      wait_vm_bar<9>(); issue(11, l);
      wait_vm_bar<9>(); issue(12, l);
      wait_vm_bar<9>(); issue(13, l);
      bar();
      if (t0) sig_add(ws, FL_H + l * 16 + rb);
      issue(14, l);
      bar();
      if (t0) sig_add(ws, FL_H + 32 + l * 16 + rb);
      if (active) {
        issue(15, l);
        if (t0) poll_ge(ws, FL_H + l * 16 + rp, 16);
        wait_vm_bar<18>();
        bar();
        wait_vm_bar<9>(); issue(16, l);
        if (t0) poll_ge(ws, FL_H + 32 + l * 16 + rp, 16);
        wait_vm_bar<9>(); issue(17, l);
        wait_vm_bar<9>(); issue(18, l);
        wait_vm_bar<9>(); issue(19, l);
        wait_vm_bar<9>();
        wait_vm_bar<0>();
        bar();
        bar();
        bar();
        if (l == 0 && t0) sig_add(ws, FL_X2 + rp);
      }
      if (l == 0) { stage_csq(); issue(20, l); issue(21, l); issue(22, l); }
    }
    return;
  }

  FS(60);
#pragma unroll 1
  for (int l = 0; l < 2; ++l) {
    u32x4 qf[2];
    {
      IDS();
      bar();
      FS(20);
      constexpr int AH = 10;
      u32x4 af[KSD];
      const unsigned xo = OFF_XA + 2 * l * SZ_XA + tt * (KSD * 1024);
      StatLd sl; stats_issue(sl, rs, OFF_ST + 2 * l * SZ_ST + tt * 3584, lane);
      Stats st;
      static_for<AH>([&](auto I) { constexpr int ks = I; af[ks] = hld(rs, voff + ks * 1024, xo); });
      const LAS char* wk = slot(0); const LAS char* wv = slot(1); const LAS char* wq = slot(2);
      f32x16 ak = {}, av = {};
      constexpr int PF = 3;
      u32x4 rk[PF], rv[PF];
#pragma unroll
      for (int i = 0; i < PF; ++i) { rk[i] = lds_rd(wk, i * 1024); rv[i] = lds_rd(wv, i * 1024); }
      SB();
      FS(21);
      u32x4 rq[4];
      static_for<KSD>([&](auto I) {
        constexpr int ks = I;
        ak = mfma(rk[ks % PF], af[ks], ak); PIN(ak);
        if constexpr (ks + PF < KSD) rk[ks % PF] = lds_rd(wk, (ks + PF) * 1024);
        if constexpr (ks + AH < KSD) af[ks + AH] = hld(rs, voff + (ks + AH) * 1024, xo);
        if constexpr (ks == 20) { st = stats_finish(sl); PIN(st.mean); PIN(st.rs); }
        SB();
        av = mfma(af[ks], rv[ks % PF], av); PIN(av);
        if constexpr (ks + PF < KSD) rv[ks % PF] = lds_rd(wv, (ks + PF) * 1024);
        if constexpr (ks >= KSD - 4) rq[ks - (KSD - 4)] = lds_rd(wq, (ks - (KSD - 4)) * 1024);
        SB();
      });
      FS(22);
      const LAS float* csl = (const LAS float*)(aux + CSQ_L);
      f32x4 ck[4];
#pragma unroll
      for (int g = 0; g < 4; ++g) ck[g] = *(const LAS f32x4*)(csl + 8 * g + 4 * h2);
      const float csv = csl[32 + r];
      LAS float* stl = (LAS float*)(aux + 8 * 4096 + w * 4096);
      if (h2 == 0) stl[r] = st.rs; else stl[32 + r] = st.mean;
      bar();
      FS(23);
      f32x16 aq = {};
      u32x4 ko[2], vo[2];
      f32x4 srs[2], smn[2];
      f32x4 cq[4];
      float e0, e1;
      const unsigned kvo = OFF_KV + l * SZ_KV + (tt * 16 + j) * 4096;
      static_for<KSD>([&](auto I) {
        constexpr int ks = I;
        aq = mfma(rq[ks & 3], af[ks], aq); PIN(aq);
        if constexpr (ks + 4 < KSD) rq[ks & 3] = lds_rd(wq, (ks + 4) * 1024);
        if constexpr (ks >= 1 && ks < 9) {
          constexpr int q = ks - 1;
          e0 = ak[2 * q] - st.mean * ck[q >> 1][(2 * q) & 3]; e1 = ak[2 * q + 1] - st.mean * ck[q >> 1][(2 * q + 1) & 3]; PIN(e0); PIN(e1);
          e0 *= st.rs; e1 *= st.rs; PIN(e0); PIN(e1);
          ko[q >> 2][q & 3] = pk2(e0, e1);
          PIN(ko[q >> 2]);
        }
        if constexpr (ks == 9) { hst(rs, voff, kvo, ko[0]); hst(rs, voff + 1024, kvo, ko[1]); }
        if constexpr (ks >= 9 && ks < 17) {
          constexpr int q = ks - 9, g = q >> 1, e = (q & 1) * 2;
          if constexpr ((q & 1) == 0) { PIN(srs[g & 1]); PIN(smn[g & 1]); }
          e0 = av[2 * q] - smn[g & 1][e] * csv; e1 = av[2 * q + 1] - smn[g & 1][e + 1] * csv; PIN(e0); PIN(e1);
          e0 *= srs[g & 1][e]; e1 *= srs[g & 1][e + 1]; PIN(e0); PIN(e1);
          const unsigned pv = pk2(e0, e1);
          vo[q >> 2][q & 3] = r == 28 ? 0x3f803f80u : pv;
          PIN(vo[q >> 2]);
        }
        if constexpr (ks == 5 || ks == 7 || ks == 11 - 0 || ks == 13) {
          constexpr int g = ks == 5 ? 0 : ks == 7 ? 1 : ks == 11 ? 2 : 3;
          srs[g & 1] = *(const LAS f32x4*)(stl + 8 * g + 4 * h2); smn[g & 1] = *(const LAS f32x4*)(stl + 32 + 8 * g + 4 * h2);
        }
        if constexpr (ks == 17) { hst(rs, voff + 2048, kvo, vo[0]); hst(rs, voff + 3072, kvo, vo[1]); }
        if constexpr (ks == 18) { LAS char* kvl = aux + (8 + w) * 4096 + lane * 16; lds_wr(kvl, 0, ko[0]); lds_wr(kvl, 1024, ko[1]); lds_wr(kvl, 2048, vo[0]); lds_wr(kvl, 3072, vo[1]); }
        if constexpr (ks >= 20 && ks < 24) cq[ks - 20] = *(const LAS f32x4*)(csl + 64 + 8 * (ks - 20) + 4 * h2);
        SB();
      });
      wait_vm_bar<0>();
      FS(24);
      const float qs = st.rs * (0.18898223650461362f * 1.4426950408889634f);
#pragma unroll
      for (int g = 0; g < 4; ++g) {
        float v[4];
#pragma unroll
        for (int e = 0; e < 4; ++e) { v[e] = aq[4 * g + e] - st.mean * cq[g][e]; PIN(v[e]); v[e] *= qs; PIN(v[e]); }
        qf[g >> 1][(g & 1) * 2] = pk2(v[0], v[1]); qf[g >> 1][(g & 1) * 2 + 1] = pk2(v[2], v[3]);
      }
      bar_lgkm();
      FS(25);
    }
    {
      IDS();
      const float NEG = -1e30f;
      f32x16 cd, co;
#pragma unroll
      for (int i = 0; i < 16; ++i) { const int key = (i & 3) + 8 * (i >> 2) + 4 * h2; cd[i] = key <= r ? 0.f : NEG; co[i] = key > r ? 0.f : NEG; }
      PIN(cd); PIN(co);
      const LAS char* kw = opq((const LAS char*)aux + lane * 16 + w * 4096);
      bar();
      FS(29);
      float m = NEG;
      f32x16 o = {};
      if (rb >= 2) {
        u32x4 kf[9][2];
#pragma unroll
        for (int it = 0; it < 9; ++it) { kf[it][0] = lds_rd(kw, it * 4096); kf[it][1] = lds_rd(kw, it * 4096 + 1024); }
        SB();
        {
          const f32x16 z = {};
          f32x16 s[3];
          static_for<11>([&](auto IT) {
            constexpr int it = IT;
            if constexpr (it < 9) { s[it % 3] = mfma(kf[it][0], qf[0], it == 0 ? co : it == 8 ? cd : z); s[it % 3] = mfma(kf[it][1], qf[1], s[it % 3]); PIN(s[it % 3]); }
            if constexpr (it >= 2) {
              const f32x16& t = s[(it - 2) % 3];
              float ma = fmaxf(fmaxf(t[0], t[1]), t[2]), mb = fmaxf(fmaxf(t[3], t[4]), t[5]);
              ma = fmaxf(fmaxf(ma, t[6]), t[7]); mb = fmaxf(fmaxf(mb, t[8]), t[9]);
              ma = fmaxf(fmaxf(ma, t[10]), t[11]); mb = fmaxf(fmaxf(mb, t[12]), t[13]);
              ma = fmaxf(fmaxf(ma, t[14]), t[15]);
              m = fmaxf(fmaxf(m, ma), mb);
              PIN(m);
            }
            SB();
          });
        }
        m = -max32(m);
        PIN(m);
        FS(30);
        f32x16 cm;
#pragma unroll
        for (int i = 0; i < 16; ++i) { cm[i] = m; co[i] = co[i] + m; }
        u32x4 vf[2][2];
        vf[0][0] = lds_rd(kw, 2048); vf[0][1] = lds_rd(kw, 3072);
        f32x16 s[3];
        s[0] = mfma(kf[0][0], qf[0], co); s[0] = mfma(kf[0][1], qf[1], s[0]);
        s[1] = mfma(kf[1][0], qf[0], cm); s[1] = mfma(kf[1][1], qf[1], s[1]);
#pragma unroll
        for (int i = 0; i < 16; ++i) cd[i] = cd[i] + m;
        SB();
        static_for<9>([&](auto IT) {
          constexpr int it = IT;
          if constexpr (it < 8) { vf[(it + 1) & 1][0] = lds_rd(kw, (it + 1) * 4096 + 2048); vf[(it + 1) & 1][1] = lds_rd(kw, (it + 1) * 4096 + 3072); }
          u32x4 p[2];
          const f32x16& c = s[it % 3];
          if constexpr (it + 2 < 9) { s[(it + 2) % 3] = mfma(kf[it + 2][0], qf[0], it + 2 == 8 ? cd : cm); }
#pragma unroll
          for (int q = 0; q < 4; ++q) p[0][q] = pk2(__builtin_amdgcn_exp2f(c[2 * q]), __builtin_amdgcn_exp2f(c[2 * q + 1]));
          PIN(p[0]);
          SB();
          if constexpr (it + 2 < 9) { s[(it + 2) % 3] = mfma(kf[it + 2][1], qf[1], s[(it + 2) % 3]); PIN(s[(it + 2) % 3]); }
          o = mfma(vf[it & 1][0], p[0], o);
#pragma unroll
          for (int q = 0; q < 4; ++q) p[1][q] = pk2(__builtin_amdgcn_exp2f(c[8 + 2 * q]), __builtin_amdgcn_exp2f(c[8 + 2 * q + 1]));
          PIN(p[1]);
          SB();
          o = mfma(vf[it & 1][1], p[1], o); PIN(o);
          SB();
        });
      } else {
        const LAS char* kq = kw;
        const f32x16 z = {};
#pragma unroll
        for (int it = 0; it < 9; ++it) {
          if (tt - 8 + it >= 0) {
            f32x16 s = mfma(lds_rd(kq, it * 4096), qf[0], it == 0 ? co : it == 8 ? cd : z);
            s = mfma(lds_rd(kq, it * 4096 + 1024), qf[1], s);
#pragma unroll
            for (int i = 0; i < 16; i += 2) m = fmaxf(fmaxf(m, s[i]), s[i + 1]);
          }
        }
        m = -max32(m);
        f32x16 cm;
#pragma unroll
        for (int i = 0; i < 16; ++i) { cd[i] += m; cm[i] = m; }
#pragma unroll
        for (int it = 0; it < 9; ++it) {
          if (it >= 1 && tt - 8 + it >= 0) {
            f32x16 s = mfma(lds_rd(kq, it * 4096), qf[0], it == 8 ? cd : cm);
            s = mfma(lds_rd(kq, it * 4096 + 1024), qf[1], s);
            u32x4 p[2];
            PACK16(p, pk2(__builtin_amdgcn_exp2f(s[i0]), __builtin_amdgcn_exp2f(s[i1])));
            o = mfma(lds_rd(kq, it * 4096 + 2048), p[0], o);
            o = mfma(lds_rd(kq, it * 4096 + 3072), p[1], o);
          }
        }
      }
      FS(31);
      const unsigned lu = __float_as_uint(o[12]);
      auto rr = __builtin_amdgcn_permlane32_swap(lu, lu, false, false);
      const float inv = __builtin_amdgcn_rcpf(__uint_as_float(rr[1]));
      u32x4 ob[2];
      PACK16(ob, pk2(o[i0] * inv, o[i1] * inv));
      const unsigned oo = OFF_OA + l * SZ_OA + (tt * KSO + 2 * j) * 1024;
      hst(rs, voff, oo, ob[0]); hst(rs, voff + 1024, oo, ob[1]);
      FS(32);
      wait_vm_bar<0>();
      FS(33);
    }
    auto proj_init = [&](int xin) { IDS(); const unsigned xo = OFF_XA + xin * SZ_XA + (ttp * KSD + 4 * fs + 2 * kh) * 1024;
      u32x4 xr[2]; xr[0] = hld(rs, voff, xo); xr[1] = hld(rs, voff + 1024, xo);
      f32x16 a;
#pragma unroll
      for (int q = 0; q < 8; ++q) { const unsigned xv = xr[q >> 2][q & 3]; a[2 * q] = bflo(xv); a[2 * q + 1] = bfhi(xv); }
      PIN(a);
      return a; };
    auto proj_finish = [&](f32x16 own, const f32x16& snd, int xout, bool final_) {
      IDS();
      u32x4 xres[2];
      LAS f32x4* xs = (LAS f32x4*)(aux) + (tt2 * 2 + kh) * 256 + lane;
      LAS f32x4* xrv = (LAS f32x4*)(aux) + (tt2 * 2 + (kh ^ 1)) * 256 + lane;
#pragma unroll
      for (int g = 0; g < 4; ++g) { f32x4 v = {snd[4 * g], snd[4 * g + 1], snd[4 * g + 2], snd[4 * g + 3]}; xs[g * 64] = v; }
      bar_lgkm();
#pragma unroll
      for (int g = 0; g < 4; ++g) { const f32x4 v = xrv[g * 64]; own[4 * g] += v.x; own[4 * g + 1] += v.y; own[4 * g + 2] += v.z; own[4 * g + 3] += v.w; }
#pragma unroll
      for (int q = 0; q < 8; ++q) xres[q >> 2][q & 3] = pk2(own[2 * q], own[2 * q + 1]);
      if (final_) {
        unsigned short* orow = out + (size_t)(ttp * 32 + r) * D_ + 64 * fs + 32 * kh + 4 * h2;
#pragma unroll
        for (int g = 0; g < 4; ++g) { u32x2 v = {xres[g >> 1][(g & 1) * 2], xres[g >> 1][(g & 1) * 2 + 1]}; *(GAS u32x2*)(orow + 8 * g) = v; }
      } else {
        const unsigned xo = OFF_XA + xout * SZ_XA + (ttp * KSD + 4 * fs + 2 * kh) * 1024;
        hst(rs, voff, xo, xres[0]); hst(rs, voff + 1024, xo, xres[1]);
        float s1[4], s2[4];
#pragma unroll
        for (int g = 0; g < 4; ++g) {
          s1[g] = (own[4 * g] + own[4 * g + 1]) + (own[4 * g + 2] + own[4 * g + 3]);
          s2[g] = __builtin_fmaf(own[4 * g], own[4 * g], own[4 * g + 1] * own[4 * g + 1]) + __builtin_fmaf(own[4 * g + 2], own[4 * g + 2], own[4 * g + 3] * own[4 * g + 3]);
        }
        const float t1 = sum32((s1[0] + s1[1]) + (s1[2] + s1[3])), t2 = sum32((s2[0] + s2[1]) + (s2[2] + s2[3]));
        if (h2 == 0) { u32x2 v = {__float_as_uint(t1), __float_as_uint(t2)}; __builtin_amdgcn_raw_buffer_store_b64(v, rs, r * 8, OFF_ST + xout * SZ_ST + (ttp * 14 + 2 * fs + kh) * 256, 16); }
      }
    };
    if (active) {
      IDS();
      f32x16 own = proj_init(2 * l), snd = {};
      bar();
      FS(34);
      u32x4 af[16];
      const unsigned ao = OFF_OA + l * SZ_OA + (ttp * KSO + kh * 16) * 1024;
      constexpr int AH = 10;
      static_for<AH>([&](auto I) { constexpr int i = I; af[i] = hld(rs, voff + i * 1024, ao); });
      const LAS char* wbo[3] = {opq(ring + 3 * CHB + kh * 3072), opq(ring + 0 * CHB + kh * 3072), opq((const LAS char*)aux + WOT_L + lane * 16 + kh * 3072)};
      const LAS char* wbs[3] = {opq(ring + 3 * CHB + kh * 2048 + (kh ^ 1) * 1024), opq(ring + 0 * CHB + kh * 2048 + (kh ^ 1) * 1024), opq((const LAS char*)aux + WOT_L + lane * 16 + kh * 2048 + (kh ^ 1) * 1024)};
      constexpr int PF = 2;
      u32x4 wo_[PF], wsn[PF];
#pragma unroll
      for (int i = 0; i < PF; ++i) { wo_[i] = lds_rd(wbo[0], i * 4096); wsn[i] = lds_rd(wbs[0], i * 4096); }
      SB();
      static_for<16>([&](auto I) {
        constexpr int i = I, n = i + PF;
        own = mfma(wo_[i % PF], af[i], own); PIN(own);
        if constexpr (n < 16) wo_[i % PF] = lds_rd(wbo[n / 7], (n % 7) * 4096);
        if constexpr (i + AH < 16) af[i + AH] = hld(rs, voff + (i + AH) * 1024, ao);
        SB();
        snd = mfma(wsn[i % PF], af[i], snd); PIN(snd);
        if constexpr (n < 16) wsn[i % PF] = lds_rd(wbs[n / 7], (n % 7) * 4096);
        SB();
      });
      FS(35);
      proj_finish(own, snd, 2 * l + 1, false);
      FS(38);
      wait_vm_bar<0>();
      FS(39);
    }
    {
      IDS();
      bar();
      FS(0);
      constexpr int AH = 10;
      u32x4 af[KSD];
      const unsigned xo = OFF_XA + (2 * l + 1) * SZ_XA + tt * (KSD * 1024);
      StatLd sl; stats_issue(sl, rs, OFF_ST + (2 * l + 1) * SZ_ST + tt * 3584, lane);
      Stats st;
      static_for<AH>([&](auto I) { constexpr int ks = I; af[ks] = hld(rs, voff + ks * 1024, xo); });
      const LAS float* csl = (const LAS float*)(aux + CSG_L) + 4 * h2;
      const unsigned ho = OFF_HA + l * SZ_HA + (tt * KSF + 7 * j) * 1024;
      f32x16 prev = {}, acc = {};
      u32x4 wr[4];
      {
        const LAS char* w0 = slot(5); const LAS char* w1 = slot(6);
        constexpr int PF = 3;
        u32x4 r0[PF], r1[PF];
#pragma unroll
        for (int i = 0; i < PF; ++i) { r0[i] = lds_rd(w0, i * 1024); r1[i] = lds_rd(w1, i * 1024); }
        SB();
        FS(2);
        static_for<KSD>([&](auto I) {
          constexpr int ks = I;
          prev = mfma(r0[ks % PF], af[ks], prev); PIN(prev);
          if constexpr (ks + PF < KSD) r0[ks % PF] = lds_rd(w0, (ks + PF) * 1024);
          if constexpr (ks + AH < KSD) af[ks + AH] = hld(rs, voff + (ks + AH) * 1024, xo);
          if constexpr (ks == 20) { st = stats_finish(sl); PIN(st.mean); PIN(st.rs); }
          SB();
          acc = mfma(r1[ks % PF], af[ks], acc); PIN(acc);
          if constexpr (ks + PF < KSD) r1[ks % PF] = lds_rd(w1, (ks + PF) * 1024);
          SB();
        });
      }
      FS(3);
      const float c1 = -1.4426950408889634f * st.rs, vk = __builtin_amdgcn_rcpf(st.rs * st.rs);
      u32x4 ov;
      f32x4 cc[4];
      float ea[8], eb[8], ex[8];
#define GU_STAGE(P, v, S) do { \
        if constexpr ((S) == 0) { ea[v] = P[v] - st.mean * cc[(v) >> 2][(v) & 3]; PIN(ea[v]); eb[v] = P[(v) + 8] - st.mean * cc[2 + ((v) >> 2)][(v) & 3]; PIN(eb[v]); } \
        else if constexpr ((S) == 1) { ex[v] = c1 * ea[v]; PIN(ex[v]); eb[v] = ea[v] * eb[v]; PIN(eb[v]); } \
        else if constexpr ((S) == 2) { ex[v] = __builtin_amdgcn_exp2f(ex[v]); PIN(ex[v]); } \
        else if constexpr ((S) == 3) { ex[v] = __builtin_fmaf(ex[v], vk, vk); PIN(ex[v]); } \
        else if constexpr ((S) == 4) { ex[v] = __builtin_amdgcn_rcpf(ex[v]); PIN(ex[v]); } \
        else { eb[v] = eb[v] * ex[v]; PIN(eb[v]); if constexpr ((v) & 1) { ov[(v) >> 1] = pk2(eb[(v) - 1], eb[v]); PIN(ov); } } } while (0)
#define GU_GAP(P, g, base, sp) static_for<8>([&](auto V_) { constexpr int v_ = V_; constexpr int s_ = (g) - (base) - (sp) * v_; if constexpr (s_ >= 0 && s_ < 6) GU_STAGE(P, v_, s_); })
#define GU_CC(t) _Pragma("unroll") for (int g = 0; g < 4; ++g) cc[g] = *(const LAS f32x4*)(csl + (t) * 32 + 8 * g)
      GU_CC(0);
      bar();
      { const LAS char* w2 = slot(7);
#pragma unroll
        for (int i = 0; i < 4; ++i) wr[i] = lds_rd(w2, i * 1024); }
      SB();
      FS(4);
      f32x16 t1 = acc;
      static_for<5>([&](auto T) {
        constexpr int t = T + 2;
        const LAS char* wl = slot(5 + t);
        const LAS char* wn = slot(6 + t);
        acc = f32x16{};
        static_for<KSD>([&](auto I) {
          constexpr int ks = I;
          if constexpr (ks == 14) {
            if constexpr (t < 6) bar();
            else asm volatile("s_waitcnt vmcnt(0)\n\ts_barrier" ::: "memory");
            SB();
          }
          acc = mfma(wr[ks & 3], af[ks], acc); PIN(acc);
          if constexpr (ks + 4 < KSD) wr[ks & 3] = lds_rd(wl, (ks + 4) * 1024);
          else if constexpr (t < 6) wr[ks & 3] = lds_rd(wn, (ks + 4 - KSD) * 1024);
          if constexpr (t == 2) {
            if constexpr (ks < 14) { GU_GAP(prev, ks, 0, 1); }
            if constexpr (ks == 12) { GU_CC(1); }
            if constexpr (ks == 13) hst(rs, voff, ho, ov);
            if constexpr (ks >= 14) { GU_GAP(t1, ks, 15, 1); }
            if constexpr (ks == 27) { hst(rs, voff + 1024, ho, ov); GU_CC(2); }
          } else {
            GU_GAP(prev, ks, 1, 3);
            if constexpr (ks == 27) { hst(rs, voff + (t - 1) * 1024, ho, ov); GU_CC(t); }
          }
          SB();
        });
        prev = acc;
        FS(3 + t);
      });
      static_for<13>([&](auto G) { constexpr int g = G; GU_GAP(prev, g, 0, 1); });
      hst(rs, voff + 6 * 1024, ho, ov);
      FS(10);
      wait_vm_bar<0>();
      FS(11);
    }
    if (active) {
      IDS();
      f32x16 own = proj_init(2 * l + 1), snd = {};
      bar();
      FS(41);
      constexpr int WIN = DN_WIN;
      u32x4 af[WIN];
      const unsigned ao = OFF_HA + l * SZ_HA + (ttp * KSF + kh * 56) * 1024;
      static_for<12>([&](auto I) { constexpr int i = I; af[i] = hld(rs, voff + kk_of(i) * 1024, ao); });
      const int oo_ = kh * 3072, so_ = kh * 2048 + (kh ^ 1) * 1024;
      constexpr int PF = 2;
      u32x4 wo_[PF], wsn[PF];
      const LAS char* po = opq(ring + 0 * CHB + oo_); const LAS char* ps = opq(ring + 0 * CHB + so_);
#pragma unroll
      for (int i = 0; i < PF; ++i) { wo_[i] = lds_rd(po, i * 4096); wsn[i] = lds_rd(ps, i * 4096); }
      SB();
      FS(42);
      const LAS char* pon = po; const LAS char* psn = ps;
      static_for<56>([&](auto I) {
        constexpr int i = I, c = i / 7, s = i % 7, n = s + PF;
        if constexpr (s == 0) { po = pon; ps = psn; if constexpr (c < 7) { pon = opq(ring + ((13 + c) & 3) * CHB + oo_); psn = opq(ring + ((13 + c) & 3) * CHB + so_); } }
        if constexpr (i == 1) FS(43);
        if constexpr (i == 28) FS(44);
        if constexpr (s == 3) { bar(); SB(); }
        const u32x4 a = af[i % WIN];
        own = mfma(wo_[i % PF], a, own); PIN(own);
        if constexpr (n < 7) wo_[i % PF] = lds_rd(po, n * 4096); else if constexpr (c < 7) wo_[i % PF] = lds_rd(pon, (n - 7) * 4096);
        if constexpr (i < WIN - 12) af[12 + i] = hld(rs, voff + kk_of(12 + i) * 1024, ao);
        SB();
        snd = mfma(wsn[i % PF], a, snd); PIN(snd);
        if constexpr (n < 7) wsn[i % PF] = lds_rd(ps, n * 4096); else if constexpr (c < 7) wsn[i % PF] = lds_rd(psn, (n - 7) * 4096);
        if constexpr (i + WIN < 56) af[i % WIN] = hld(rs, voff + kk_of(i + WIN) * 1024, ao);
        SB();
      });
      FS(45);
      proj_finish(own, snd, 2, l == 1);
      FS(46);
      wait_vm_bar<0>();
      FS(47);
    }
    if (l == 0) FS(61);
  }
}

}

extern "C" size_t stamp_offset() { return OFF_STAMP; }

extern "C" void kernel_launch(void* const* d_in, const int* in_sizes, int n_in, void* d_out, int out_size, void* d_ws, size_t ws_size,
                              hipStream_t stream) {
  const unsigned short* x = (const unsigned short*)d_in[0];
  const unsigned short* wqkv = (const unsigned short*)d_in[1];
  const unsigned short* wo = (const unsigned short*)d_in[2];
  const unsigned short* w1 = (const unsigned short*)d_in[4];
  const unsigned short* w3 = (const unsigned short*)d_in[5];
  const unsigned short* w2 = (const unsigned short*)d_in[6];
  char* ws = (char*)d_ws;
  (void)hipFuncSetAttribute((const void*)k_main, hipFuncAttributeMaxDynamicSharedMemorySize, LDS_BYTES);
  k_prep<<<PREP_BLOCKS, 256, PREP_LDS, stream>>>(x, wqkv, wo, w1, w3, w2, ws);
  k_main<<<256, 512, LDS_BYTES, stream>>>(ws, (unsigned short*)d_out);
}
```

```cpp
#include <hip/hip_runtime.h>
#include <stdint.h>
#include <utility>

typedef __attribute__((ext_vector_type(8))) __bf16 bf16x8;
typedef __attribute__((ext_vector_type(2))) __bf16 bf16x2;
typedef __attribute__((ext_vector_type(2))) float f32x2;
typedef __attribute__((ext_vector_type(4))) float f32x4;
typedef __attribute__((ext_vector_type(16))) float f32x16;
typedef __attribute__((ext_vector_type(4))) unsigned u32x4;
typedef __attribute__((ext_vector_type(2))) unsigned u32x2;
#define DEV __device__ __forceinline__
#define LAS __attribute__((address_space(3)))
#define GAS __attribute__((address_space(1)))
typedef GAS unsigned gu32;
#define RLX __ATOMIC_RELAXED, __HIP_MEMORY_SCOPE_AGENT

namespace {

constexpr int D_ = 448, F_ = 1792;
constexpr int NTT = 64;
constexpr int KSD = 28;
constexpr int KSO = 32;
constexpr int KSF = 112;
constexpr int CHB = 28672;

constexpr unsigned OFF_FLAG = 0;
constexpr int FL_KV = 0;
constexpr int FL_O = FL_KV + 512;
constexpr int FL_X1 = FL_O + 32;
constexpr int FL_H = FL_X1 + 32;
constexpr int FL_X2 = FL_H + 64;
constexpr int FL_TMO = FL_X2 + 16;
constexpr int NFLAG = FL_TMO + 1;
constexpr unsigned OFF_WQKV = 96 * 1024;
constexpr unsigned OFF_WO   = OFF_WQKV + 16 * 3 * CHB;
constexpr unsigned OFF_WGU  = OFF_WO + 7 * 64 * 1024;
constexpr unsigned OFF_WDN  = OFF_WGU + 16 * 7 * CHB;
constexpr unsigned OFF_CSQ  = OFF_WDN + 7 * 224 * 1024;
constexpr unsigned OFF_CSG  = OFF_CSQ + 16 * 3 * 32 * 4;
constexpr unsigned SZ_XA    = NTT * KSD * 1024;
constexpr unsigned OFF_XA   = OFF_CSG + 16 * 7 * 32 * 4;
constexpr unsigned SZ_ST    = NTT * 14 * 32 * 8;
constexpr unsigned OFF_ST   = OFF_XA + 4 * SZ_XA;
constexpr unsigned SZ_KV    = NTT * 16 * 4096;
constexpr unsigned OFF_KV   = OFF_ST + 4 * SZ_ST;
constexpr unsigned SZ_OA    = NTT * KSO * 1024;
constexpr unsigned OFF_OA   = OFF_KV + 2 * SZ_KV;
constexpr unsigned SZ_HA    = NTT * KSF * 1024;
constexpr unsigned OFF_HA   = OFF_OA + 2 * SZ_OA;
constexpr unsigned WS_END   = OFF_HA + 2 * SZ_HA;
constexpr unsigned OFF_STAMP = (WS_END + 4095) / 4096 * 4096;
static_assert(NFLAG * 128 <= OFF_WQKV, "flags");
static_assert(OFF_XA % 128 == 0 && OFF_ST % 128 == 0 && OFF_KV % 128 == 0 && OFF_OA % 128 == 0 && OFF_HA % 128 == 0, "lines");

#define STAMP(i) do {} while (0)
#define FS(i) do {} while (0)

template <class F, int... I> DEV void static_for_impl(F&& f, std::integer_sequence<int, I...>) { (f(std::integral_constant<int, I>{}), ...); }
template <int N, class F> DEV void static_for(F&& f) { static_for_impl(f, std::make_integer_sequence<int, N>{}); }

DEV int lane_id() { int l; asm volatile("v_mbcnt_lo_u32_b32 %0, -1, 0\n\tv_mbcnt_hi_u32_b32 %0, -1, %0" : "=v"(l)); return l; }
DEV unsigned pk2(float a, float b) { f32x2 v = {a, b}; return __builtin_bit_cast(unsigned, __builtin_convertvector(v, bf16x2)); }
DEV f32x16 mfma(u32x4 a, u32x4 b, f32x16 c) {
  return __builtin_amdgcn_mfma_f32_32x32x16_bf16(__builtin_bit_cast(bf16x8, a), __builtin_bit_cast(bf16x8, b), c, 0, 0, 0);
}
DEV float bflo(unsigned u) { return __uint_as_float(u << 16); }
DEV float bfhi(unsigned u) { return __uint_as_float(u & 0xffff0000u); }
DEV float swap32(float v) {
  const unsigned u = __float_as_uint(v);
  auto rr = __builtin_amdgcn_permlane32_swap(u, u, false, false);
  return __uint_as_float((threadIdx.x & 32) ? rr[0] : rr[1]);
}
DEV float sum32(float v) {
  const unsigned u = __float_as_uint(v);
  auto rr = __builtin_amdgcn_permlane32_swap(u, u, false, false);
  return __uint_as_float(rr[0]) + __uint_as_float(rr[1]);
}
DEV float max32(float v) {
  const unsigned u = __float_as_uint(v);
  auto rr = __builtin_amdgcn_permlane32_swap(u, u, false, false);
  return fmaxf(__uint_as_float(rr[0]), __uint_as_float(rr[1]));
}
DEV void dma7(const char* g, unsigned voff, unsigned lds) {
  unsigned keep;
  const char* g2 = g + 4096;
  lds = __builtin_amdgcn_readfirstlane(lds);
  asm volatile("s_mov_b32 %0, m0\n\ts_mov_b32 m0, %2\n\ts_nop 0\n\t"
               "global_load_lds_dwordx4 %1, %3 offset:0\n\t"
               "global_load_lds_dwordx4 %1, %3 offset:1024\n\t"
               "global_load_lds_dwordx4 %1, %3 offset:2048\n\t"
               "global_load_lds_dwordx4 %1, %3 offset:3072\n\t"
               "s_add_u32 m0, m0, 0x1000\n\ts_nop 0\n\t"
               "global_load_lds_dwordx4 %1, %4 offset:0\n\t"
               "global_load_lds_dwordx4 %1, %4 offset:1024\n\t"
               "global_load_lds_dwordx4 %1, %4 offset:2048\n\t"
               "s_mov_b32 m0, %0"
               : "=&s"(keep) : "v"(voff), "s"(lds), "s"(g), "s"(g2) : "memory", "scc");
}
template <int N> DEV void wait_vm() { asm volatile("s_waitcnt vmcnt(%0)" ::"n"(N) : "memory"); }
template <int N> DEV void wait_vm_bar() { asm volatile("s_waitcnt vmcnt(%0) lgkmcnt(0)\n\ts_barrier" ::"n"(N) : "memory"); }
DEV void bar_lgkm() { asm volatile("s_waitcnt lgkmcnt(0)\n\ts_barrier" ::: "memory"); }
DEV u32x4 lds_rd(const LAS char* base, int off) { return *(const LAS u32x4*)(base + off); }
DEV void lds_wr(LAS char* base, int off, u32x4 v) { *(LAS u32x4*)(base + off) = v; }
DEV u32x4 gld(const char* p) { return *(const GAS u32x4*)(p); }
DEV void gst(char* p, u32x4 v) { *(GAS u32x4*)(p) = v; }
typedef __amdgpu_buffer_rsrc_t rsrc_t;
DEV u32x4 hld(rsrc_t rs, unsigned voff, unsigned soff) { return __builtin_amdgcn_raw_buffer_load_b128(rs, voff, soff, 16); }
DEV void hst(rsrc_t rs, unsigned voff, unsigned soff, u32x4 v) { __builtin_amdgcn_raw_buffer_store_b128(v, rs, voff, soff, 16); }

struct Poll { unsigned v0, v1, v2, v3; };
DEV void poll_ge(Poll& q, char* ws, int flag, unsigned target, int on) {
  const char* c = ws + OFF_FLAG + (size_t)flag * 128;
  unsigned cnt, tmp;
  on = __builtin_amdgcn_readfirstlane(on);
  asm volatile(
      "s_mov_b32 %4, 0\n\t"
      "s_cmp_eq_u32 %9, 0\n\t"
      "s_cbranch_scc1 2f\n"
      "1:\n\t"
      "global_load_dword %0, %6, %7 sc1\n\t"
      "s_waitcnt vmcnt(0)\n\tv_readfirstlane_b32 %5, %0\n\ts_nop 0\n\ts_cmp_ge_u32 %5, %8\n\ts_cbranch_scc1 2f\n\ts_sleep 1\n\t"
      "s_add_u32 %4, %4, 1\n\ts_cmp_lt_u32 %4, 0x8000\n\ts_cbranch_scc1 1b\n"
      "2:"
      : "+v"(q.v0), "+v"(q.v1), "+v"(q.v2), "+v"(q.v3), "=&s"(cnt), "=&s"(tmp)
      : "v"(0u), "s"(c), "s"(target), "s"(on)
      : "memory", "scc");
  if (cnt >= 0x8000u && lane_id() == 0) __hip_atomic_store((gu32*)(ws + OFF_FLAG + (size_t)FL_TMO * 128), 1u, RLX);
}
DEV void poll_done(Poll& q, int on) {
  on = __builtin_amdgcn_readfirstlane(on);
  asm volatile("s_cmp_eq_u32 %4, 0\n\ts_cbranch_scc1 3f\n\ts_waitcnt vmcnt(0)\n3:" : "+v"(q.v0), "+v"(q.v1), "+v"(q.v2), "+v"(q.v3) : "s"(on) : "scc");
}
DEV void sig_add(char* ws, int flag) { (void)__hip_atomic_fetch_add((gu32*)(ws + OFF_FLAG + (size_t)flag * 128), 1u, RLX); }

struct Stats { float mean, rs; };
DEV Stats load_stats(rsrc_t rs, unsigned off_st, int tt, int lane) {
  const int r = lane & 31, h2 = lane >> 5;
  float s1 = 0.f, s2 = 0.f;
  u32x2 v[7];
#pragma unroll
  for (int i = 0; i < 7; ++i) v[i] = __builtin_amdgcn_raw_buffer_load_b64(rs, h2 * 1792 + r * 8 + i * 256, off_st + tt * 3584, 16);
#pragma unroll
  for (int i = 0; i < 7; ++i) { s1 += __uint_as_float(v[i].x); s2 += __uint_as_float(v[i].y); }
  s1 = sum32(s1); s2 = sum32(s2);
  Stats s; s.mean = s1 * (1.0f / D_);
  const float var = fmaxf(s2 * (1.0f / D_) - s.mean * s.mean, 0.f);
  s.rs = __builtin_amdgcn_rsqf(var + 1e-5f);
  return s;
}
constexpr int G1T = 5, G1S = 8 * G1T;
__host__ __device__ constexpr int kk_of(int i) { return i < G1S ? 7 * (i / G1T) + i % G1T : 7 * ((i - G1S) / (7 - G1T)) + G1T + (i - G1S) % (7 - G1T); }

typedef short s16x4 __attribute__((ext_vector_type(4)));
DEV u32x2 tr_read(const LAS char* p) { return __builtin_bit_cast(u32x2, __builtin_amdgcn_ds_read_tr16_b64_v4i16((LAS s16x4*)p)); }
template <int KIND>
DEV void prep_unit(const unsigned short* __restrict__ src, const unsigned short* __restrict__ src2, int ld, int col, char* __restrict__ dst, float* cs, int a1, int a2, char* smem) {
  const int tid = threadIdx.x, lane = tid & 63, w = tid >> 6, r = lane & 31, h2 = lane >> 5;
  LAS char* img = (LAS char*)smem;
  if (KIND == 0) {
    u32x2 v[14];
    const int p = tid & 7, row0 = tid >> 3;
#pragma unroll
    for (int n = 0; n < 14; ++n) v[n] = p < 7 ? *(const GAS u32x2*)(src + (size_t)(row0 + 32 * n) * ld + col + 4 * p) : u32x2{0u, 0u};
#pragma unroll
    for (int n = 0; n < 14; ++n) *(LAS u32x2*)(img + (row0 + 32 * n) * 64 + p * 8) = v[n];
  } else {
    u32x4 v[7];
    const int p = tid & 3, row0 = tid >> 2;
    const unsigned short* sp = KIND == 1 ? (p < 2 ? src + col + 8 * p : src2 + col + 8 * (p - 2)) : src + col + 8 * p;
#pragma unroll
    for (int n = 0; n < 7; ++n) v[n] = *(const GAS u32x4*)(sp + (size_t)((KIND == 3 ? 448 * a2 : 0) + row0 + 64 * n) * ld);
#pragma unroll
    for (int n = 0; n < 7; ++n) {
      const int row = row0 + 64 * n;
      const int irow = KIND == 2 ? 32 * (row / 28) + row % 28 : row;
      *(LAS u32x4*)(img + irow * 64 + p * 16) = v[n];
    }
    if (KIND == 2) { const int head = tid >> 4, d = 28 + ((tid >> 2) & 3); *(LAS u32x4*)(img + (32 * head + d) * 64 + p * 16) = u32x4{0u, 0u, 0u, 0u}; }
  }
  __syncthreads();
  constexpr int NKS = KIND == 2 ? 32 : 28;
  const LAS char* rd = img + (4 * h2 + ((lane & 15) >> 2)) * 64 + ((lane >> 4) & 1) * 32 + (lane & 3) * 8;
  float sum = 0.f;
#pragma unroll
  for (int n = 0; n < NKS / 4; ++n) {
    const int i = w + 4 * n;
    const u32x2 lo = tr_read(rd + i * 1024), hi = tr_read(rd + i * 1024 + 512);
    const u32x4 v = {lo.x, lo.y, hi.x, hi.y};
    size_t doff;
    if (KIND <= 1) doff = (size_t)i * 1024;
    else if (KIND == 2) doff = (size_t)(((i & 15) * 2 + (i >> 4)) * 2 + a1) * 1024;
    else {
      const int ks = 28 * a2 + i, kh = ks / 56, kk = ks % 56, j8 = kk / 7, t = kk % 7;
      const int step = t < G1T ? G1T * j8 + t : G1S + (7 - G1T) * j8 + (t - G1T);
      doff = (size_t)((step * 2 + kh) * 2 + a1) * 1024;
    }
    gst(dst + doff + lane * 16, v);
    if (KIND <= 1) {
#pragma unroll
      for (int q = 0; q < 4; ++q) sum += bflo(v[q]) + bfhi(v[q]);
    }
  }
  if (KIND <= 1) {
    sum = sum32(sum);
    __syncthreads();
    LAS float* red = (LAS float*)img;
    if (h2 == 0) red[w * 32 + r] = sum;
    __syncthreads();
    if (tid < 32) cs[tid] = (red[tid] + red[32 + tid]) + (red[64 + tid] + red[96 + tid]);
  }
}
constexpr int PREP_LDS = 32768;
constexpr int PREP_BLOCKS = 64 + 112 + 48 + 56 + 14;
__global__ __launch_bounds__(256) void k_prep(const unsigned short* __restrict__ x, const unsigned short* __restrict__ wqkv,
                                              const unsigned short* __restrict__ wo, const unsigned short* __restrict__ w1,
                                              const unsigned short* __restrict__ w3, const unsigned short* __restrict__ w2, char* __restrict__ ws) {
  extern __shared__ __attribute__((aligned(16))) char smem[];
  const int b = blockIdx.x, tid = threadIdx.x, lane = tid & 63, w = tid >> 6, r = lane & 31, h2 = lane >> 5;
  if (b >= PREP_BLOCKS - 64) {
    const int tt = b - (PREP_BLOCKS - 64);
    if (tt < 3) { const int f = tt * 256 + tid; if (f < NFLAG) *(GAS unsigned*)(ws + OFF_FLAG + (size_t)f * 128) = 0u; }
    LAS char* img = (LAS char*)smem;
    u32x4 v[7];
#pragma unroll
    for (int n = 0; n < 7; ++n) { const int c = tid + 256 * n; v[n] = *(const GAS u32x4*)(x + (size_t)tt * 32 * D_ + c * 8); }
#pragma unroll
    for (int n = 0; n < 7; ++n) { const int c = tid + 256 * n; *(LAS u32x4*)(img + (c / 56) * 912 + (c % 56) * 16) = v[n]; }
    __syncthreads();
    for (int ft = w; ft < 14; ft += 4) {
      float s1 = 0.f, s2 = 0.f;
#pragma unroll
      for (int s = 0; s < 2; ++s) {
        const int ks = 2 * ft + s;
        const u32x2 a = *(const LAS u32x2*)(img + r * 912 + ks * 32 + 8 * h2), c = *(const LAS u32x2*)(img + r * 912 + ks * 32 + 16 + 8 * h2);
        const u32x4 o = {a.x, a.y, c.x, c.y};
        gst(ws + OFF_XA + ((size_t)tt * KSD + ks) * 1024 + lane * 16, o);
#pragma unroll
        for (int q = 0; q < 4; ++q) { const float lo = bflo(o[q]), hi = bfhi(o[q]); s1 += lo + hi; s2 += lo * lo + hi * hi; }
      }
      s1 = sum32(s1); s2 = sum32(s2);
      if (h2 == 0) { f32x2 o = {s1, s2}; *(GAS f32x2*)(ws + OFF_ST + ((size_t)tt * 14 + ft) * 256 + r * 8) = o; }
    }
    return;
  }
  int u = b;
  if (u < 48) {
    const int j = u / 3, p = u % 3;
    prep_unit<0>(wqkv, nullptr, 3 * D_, (p == 2 ? 0 : p == 0 ? D_ : 2 * D_) + 28 * j, ws + OFF_WQKV + (size_t)u * CHB, (float*)(ws + OFF_CSQ) + u * 32, 0, 0, smem);
  } else if (u < 48 + 112) {
    u -= 48; const int j = u / 7, t = u % 7;
    prep_unit<1>(w1, w3, F_, 112 * j + 16 * t, ws + OFF_WGU + (size_t)u * CHB, (float*)(ws + OFF_CSG) + u * 32, 0, 0, smem);
  } else if (u < 48 + 112 + 14) {
    u -= 160; const int fs = u >> 1, tile = u & 1;
    prep_unit<2>(wo, nullptr, D_, 64 * fs + 32 * tile, ws + OFF_WO + (size_t)fs * 64 * 1024, nullptr, tile, 0, smem);
  } else {
    u -= 174; const int fs = u >> 3, tile = (u >> 2) & 1, q = u & 3;
    prep_unit<3>(w2, nullptr, D_, 64 * fs + 32 * tile, ws + OFF_WDN + (size_t)fs * 224 * 1024, nullptr, tile, q, smem);
  }
}

constexpr int AUX = 4 * CHB;
constexpr int LDS_BYTES = AUX + 49152;
#define SB() __builtin_amdgcn_sched_barrier(0)
#define PIN(x) asm volatile("" : "+v"(x))
#define PACK16(o, expr) _Pragma("unroll") for (int q_ = 0; q_ < 8; ++q_) { const int i0 = 2 * q_, i1 = 2 * q_ + 1; (void)i0; (void)i1; o[q_ >> 2][q_ & 3] = expr; }
template <class T> DEV void opaque_s(T& v) { asm volatile("" : "+s"(v)); }

struct StatLd { u32x2 v[7]; };
DEV void stats_issue(StatLd& s, rsrc_t rs, unsigned soff, int lane) {
  const unsigned vo = (lane >> 5) * 1792 + (lane & 31) * 8;
#pragma unroll
  for (int i = 0; i < 7; ++i) s.v[i] = __builtin_amdgcn_raw_buffer_load_b64(rs, vo + i * 256, soff, 16);
}
DEV Stats stats_finish(const StatLd& s) {
  float s1 = 0.f, s2 = 0.f;
#pragma unroll
  for (int i = 0; i < 7; ++i) { s1 += __uint_as_float(s.v[i].x); s2 += __uint_as_float(s.v[i].y); }
  s1 = sum32(s1); s2 = sum32(s2);
  Stats o; o.mean = s1 * (1.0f / D_);
  const float var = fmaxf(s2 * (1.0f / D_) - o.mean * o.mean, 0.f);
  o.rs = __builtin_amdgcn_rsqf(var + 1e-5f);
  return o;
}

template <int N> DEV void dma_n(rsrc_t rs, unsigned voff, unsigned lds, unsigned soff) {
  if constexpr (N == 4)
    asm volatile("s_mov_b32 m0, %0\n\ts_nop 0\n\tbuffer_load_dwordx4 %1, %2, %3 offen lds\n\tbuffer_load_dwordx4 %1, %2, %3 offen offset:1024 lds\n\t"
                 "buffer_load_dwordx4 %1, %2, %3 offen offset:2048 lds\n\tbuffer_load_dwordx4 %1, %2, %3 offen offset:3072 lds" ::"s"(lds), "v"(voff), "s"(rs), "s"(soff) : "memory");
  else if constexpr (N == 3)
    asm volatile("s_mov_b32 m0, %0\n\ts_nop 0\n\tbuffer_load_dwordx4 %1, %2, %3 offen lds\n\tbuffer_load_dwordx4 %1, %2, %3 offen offset:1024 lds\n\t"
                 "buffer_load_dwordx4 %1, %2, %3 offen offset:2048 lds" ::"s"(lds), "v"(voff), "s"(rs), "s"(soff) : "memory");
  else
    asm volatile("s_mov_b32 m0, %0\n\ts_nop 0\n\tbuffer_load_dwordx4 %1, %2, %3 offen lds\n\tbuffer_load_dwordx4 %1, %2, %3 offen offset:1024 lds" ::"s"(lds), "v"(voff), "s"(rs), "s"(soff) : "memory");
}
DEV void dma_chunk(rsrc_t rs, unsigned voff, unsigned lds, unsigned soff, int w) {
  const unsigned o = (w - 1) * 9216;
  dma_n<4>(rs, voff, lds + o, soff + o); dma_n<4>(rs, voff, lds + o + 4096, soff + o + 4096);
  if (w == 3) dma_n<2>(rs, voff, lds + o + 8192, soff + o + 8192);
  else asm volatile("s_mov_b32 m0, %0\n\ts_nop 0\n\tbuffer_load_dwordx4 %1, %2, %3 offen lds" ::"s"(lds + o + 8192), "v"(voff), "s"(rs), "s"(soff + o + 8192) : "memory");
}
DEV void dma2(rsrc_t rs, unsigned voff, unsigned lds, unsigned soff) {
  asm volatile("s_mov_b32 m0, %0\n\ts_nop 0\n\t"
               "buffer_load_dwordx4 %1, %2, %3 offen lds\n\t"
               "buffer_load_dwordx4 %1, %2, %3 offen offset:1024 lds"
               ::"s"(lds), "v"(voff), "s"(rs), "s"(soff) : "memory");
}
DEV void bar() { asm volatile("s_barrier" ::: "memory"); }
DEV const LAS char* opq(const LAS char* p) { asm volatile("" : "+v"(p)); return p; }

constexpr int DN_WIN = 23;

__global__ __launch_bounds__(512, 2) void k_main(char* __restrict__ ws, unsigned short* __restrict__ out) {
  extern __shared__ __attribute__((aligned(16))) char smem[];
  const int w8_ = __builtin_amdgcn_readfirstlane(threadIdx.x >> 6);
  const bool helper = w8_ >= 4;
  const int w_ = w8_ & 3;
  const int b = blockIdx.x, idx = b >> 3;
#define MAPA 1
  const int x_ = b & 7;
  const int rb_ = MAPA == 0 ? 2 * x_ + (idx >> 4) : MAPA == 1 ? 4 * (x_ & 3) + (idx >> 3) : 8 * (x_ & 1) + (idx >> 2);
  const int j_ = MAPA == 0 ? (idx & 15) : MAPA == 1 ? 8 * (x_ >> 2) + (idx & 7) : 4 * (x_ >> 1) + (idx & 3);
  const int rp_ = 2 * x_ + (idx >> 4), m_ = idx & 15;
  const bool active = m_ < 14;
  const unsigned lds0 = __builtin_amdgcn_readfirstlane((unsigned)(uintptr_t)smem);
  const rsrc_t rs = __builtin_amdgcn_make_buffer_rsrc(ws, 0, WS_END, 0x00020000);
#define IDS() \
  int w = w_, rb = rb_, j = j_, rp = rp_, mm = m_; asm volatile("" : "+s"(w), "+s"(rb), "+s"(j), "+s"(rp), "+s"(mm)); \
  const int lane = lane_id(), r = lane & 31, h2 = lane >> 5; \
  const bool t0 = w == 0 && lane == 0; (void)t0; \
  const int th = mm >= 7 ? 1 : 0, fs = mm - 7 * th, tt2 = w & 1, kh = w >> 1; \
  const int tt = 4 * rb + w, ttp = 4 * rp + 2 * th + tt2; \
  const unsigned voff = lane * 16; \
  const LAS char* ring = (const LAS char*)smem + lane * 16; \
  LAS char* aux = (LAS char*)smem + AUX; \
  (void)r; (void)h2; (void)fs; (void)kh; (void)tt; (void)ttp; (void)voff; (void)ring; (void)aux; (void)tt2; (void)rp;
#define slot(c) opq(ring + ((c) & 3) * CHB)
  constexpr int CSQ_L = 0;
  constexpr int CSG_L = 16384;
  constexpr int WOT_L = 24576;

  if (helper) {
    auto issue = [&](int c, int l) {
      const int sl = c & 3;
      if (c >= 20) { c -= 20; ++l; }
      const bool proj = (c >= 3 && c < 5) || c >= 12;
      int w = w_, j = j_, mm = m_; asm volatile("" : "+s"(w), "+s"(j), "+s"(mm));
      const int fs = mm >= 7 ? mm - 7 : mm;
      unsigned src;
      if (c < 3) src = OFF_WQKV + (j * 3 + c) * CHB;
      else if (c < 5) src = OFF_WO + fs * 65536 + (c - 3) * CHB;
      else if (c < 12) src = OFF_WGU + (j * 7 + c - 5) * CHB;
      else src = OFF_WDN + fs * 229376 + (c - 12) * CHB;
      if (!(l < 2 && (active || !proj))) src = WS_END;
      if (w != 0) dma_chunk(rs, lane_id() * 16, lds0 + sl * CHB, src, w);
    };
    auto stage_csq = [&]() { IDS(); if (w == 3 && lane < 24) *(LAS u32x4*)(aux + CSQ_L + lane * 16) = gld(ws + OFF_CSQ + j * 384 + lane * 16); };
    stage_csq();
    issue(0, 0); issue(1, 0); issue(2, 0);
    Poll pq = {0u, 0u, 0u, 0u};
#pragma unroll 1
    for (int l = 0; l < 2; ++l) {
      IDS();
      const int pw = w == 0 ? 1 : 0;
      poll_ge(pq, ws, FL_X2 + rb, 14, l == 1 ? pw : 0);
      if (w != 0) wait_vm<0>();
      bar_lgkm();
      poll_done(pq, pw);
      bar();
      bar();
      if (w == 1 && lane == 0) sig_add(ws, FL_KV + (l * 16 + rb) * 16 + j);
      if (rb >= 1) {
        poll_ge(pq, ws, FL_KV + (l * 16 + rb - 1) * 16 + j, 1, 1);
        poll_done(pq, 1);
        poll_ge(pq, ws, FL_KV + (l * 16 + rb - 2) * 16 + j, 1, rb >= 2 ? 1 : 0);
        poll_done(pq, 1);
        u32x4 hv[8];
#pragma unroll
        for (int n = 0; n < 2; ++n) {
          const int rel = 2 * w + n, kt = max(4 * rb - 8 + rel, 0);
#pragma unroll
          for (int q = 0; q < 4; ++q) hv[n * 4 + q] = hld(rs, voff + q * 1024, OFF_KV + l * SZ_KV + (kt * 16 + j) * 4096);
        }
        SB();
#pragma unroll
        for (int n = 0; n < 8; ++n) lds_wr(aux + lane * 16, (2 * w) * 4096 + n * 1024, hv[n]);
      }
      bar_lgkm();
      issue(3, l); issue(4, l); issue(5, l); issue(6, l);
      bar();
      if (w == 1 && lane == 0) sig_add(ws, FL_O + l * 16 + rb);
      if (w == 3 && lane < 56) *(LAS u32x4*)(aux + CSG_L + lane * 16) = gld(ws + OFF_CSG + j * 896 + lane * 16);
      if (active) {
        if (w == 1 || w == 2) dma_n<4>(rs, voff, lds0 + AUX + WOT_L + (w - 1) * 4096, OFF_WO + fs * 65536 + 2 * CHB + (w - 1) * 4096);
        poll_ge(pq, ws, FL_O + l * 16 + rp, 16, pw);
        if (w != 0) wait_vm<0>();
        bar_lgkm();
        poll_done(pq, pw);
        bar();
        bar();
        if (w == 1 && lane == 0) sig_add(ws, FL_X1 + l * 16 + rp);
      }
      issue(7, l); issue(8, l);
      poll_ge(pq, ws, FL_X1 + l * 16 + rb, 14, pw);
      if (w != 0) wait_vm<18>();
      bar_lgkm();
      poll_done(pq, pw);
      wait_vm_bar<9>();
      issue(9, l); issue(10, l);
      wait_vm_bar<18>();
      wait_vm_bar<9>(); issue(11, l);
      wait_vm_bar<9>(); issue(12, l);
      wait_vm_bar<9>(); issue(13, l);
      bar();
      if (w == 1 && lane == 0) sig_add(ws, FL_H + l * 16 + rb);
      issue(14, l);
      bar();
      if (w == 1 && lane == 0) sig_add(ws, FL_H + 32 + l * 16 + rb);
      if (active) {
        issue(15, l);
        poll_ge(pq, ws, FL_H + l * 16 + rp, 16, pw);
        if (w != 0) wait_vm<18>();
        bar();
        poll_done(pq, pw);
        bar();
        wait_vm_bar<9>(); issue(16, l);
        poll_ge(pq, ws, FL_H + 32 + l * 16 + rp, 16, pw);
        if (w != 0) wait_vm<9>();
        bar(); issue(17, l);
        poll_done(pq, pw);
        wait_vm_bar<9>(); issue(18, l);
        wait_vm_bar<9>(); issue(19, l);
        wait_vm_bar<9>();
        wait_vm_bar<0>();
        bar();
        bar();
        bar();
        if (l == 0 && w == 1 && lane == 0) sig_add(ws, FL_X2 + rp);
      }
      if (l == 0) { stage_csq(); issue(20, l); issue(21, l); issue(22, l); }
    }
    return;
  }

  FS(60);
#pragma unroll 1
  for (int l = 0; l < 2; ++l) {
    u32x4 qf[2];
    {
      IDS();
      bar();
      FS(20);
      constexpr int AH = 10;
      u32x4 af[KSD];
      const unsigned xo = OFF_XA + 2 * l * SZ_XA + tt * (KSD * 1024);
      StatLd sl; stats_issue(sl, rs, OFF_ST + 2 * l * SZ_ST + tt * 3584, lane);
      Stats st;
      static_for<AH>([&](auto I) { constexpr int ks = I; af[ks] = hld(rs, voff + ks * 1024, xo); });
      const LAS char* wk = slot(0); const LAS char* wv = slot(1); const LAS char* wq = slot(2);
      f32x16 ak = {}, av = {};
      constexpr int PF = 3;
      u32x4 rk[PF], rv[PF];
#pragma unroll
      for (int i = 0; i < PF; ++i) { rk[i] = lds_rd(wk, i * 1024); rv[i] = lds_rd(wv, i * 1024); }
      SB();
      FS(21);
      u32x4 rq[4];
      static_for<KSD>([&](auto I) {
        constexpr int ks = I;
        ak = mfma(rk[ks % PF], af[ks], ak); PIN(ak);
        if constexpr (ks + PF < KSD) rk[ks % PF] = lds_rd(wk, (ks + PF) * 1024);
        if constexpr (ks + AH < KSD) af[ks + AH] = hld(rs, voff + (ks + AH) * 1024, xo);
        if constexpr (ks == 20) { st = stats_finish(sl); PIN(st.mean); PIN(st.rs); }
        SB();
        av = mfma(af[ks], rv[ks % PF], av); PIN(av);
        if constexpr (ks + PF < KSD) rv[ks % PF] = lds_rd(wv, (ks + PF) * 1024);
        if constexpr (ks >= KSD - 4) rq[ks - (KSD - 4)] = lds_rd(wq, (ks - (KSD - 4)) * 1024);
        SB();
      });
      FS(22);
      const LAS float* csl = (const LAS float*)(aux + CSQ_L);
      f32x4 ck[4];
#pragma unroll
      for (int g = 0; g < 4; ++g) ck[g] = *(const LAS f32x4*)(csl + 8 * g + 4 * h2);
      const float csv = csl[32 + r];
      LAS float* stl = (LAS float*)(aux + 8 * 4096 + w * 4096);
      if (h2 == 0) stl[r] = st.rs; else stl[32 + r] = st.mean;
      bar();
      FS(23);
      f32x16 aq = {};
      u32x4 ko[2], vo[2];
      f32x4 srs[2], smn[2];
      f32x4 cq[4];
      float e0, e1;
      const unsigned kvo = OFF_KV + l * SZ_KV + (tt * 16 + j) * 4096;
      static_for<KSD>([&](auto I) {
        constexpr int ks = I;
        aq = mfma(rq[ks & 3], af[ks], aq); PIN(aq);
        if constexpr (ks + 4 < KSD) rq[ks & 3] = lds_rd(wq, (ks + 4) * 1024);
        if constexpr (ks == 0) { srs[0] = *(const LAS f32x4*)(stl + 4 * h2); smn[0] = *(const LAS f32x4*)(stl + 32 + 4 * h2); srs[1] = *(const LAS f32x4*)(stl + 8 + 4 * h2); smn[1] = *(const LAS f32x4*)(stl + 40 + 4 * h2); }
        if constexpr (ks >= 1 && ks < 5) {
          constexpr int g = ks - 1;
          float v[4];
#pragma unroll
          for (int e = 0; e < 4; ++e) { v[e] = ak[4 * g + e] - st.mean * ck[g][e]; PIN(v[e]); }
#pragma unroll
          for (int e = 0; e < 4; ++e) { v[e] *= st.rs; PIN(v[e]); }
          ko[g >> 1][(g & 1) * 2] = pk2(v[0], v[1]); ko[g >> 1][(g & 1) * 2 + 1] = pk2(v[2], v[3]);
          PIN(ko[g >> 1]);
        }
        if constexpr (ks == 5) { hst(rs, voff, kvo, ko[0]); hst(rs, voff + 1024, kvo, ko[1]); }
        if constexpr (ks >= 5 && ks < 13) {
          constexpr int q = ks - 5, g = q >> 1, e = (q & 1) * 2;
          if constexpr ((q & 1) == 0) { PIN(srs[g & 1]); PIN(smn[g & 1]); }
          e0 = av[2 * q] - smn[g & 1][e] * csv; e1 = av[2 * q + 1] - smn[g & 1][e + 1] * csv; PIN(e0); PIN(e1);
          e0 *= srs[g & 1][e]; e1 *= srs[g & 1][e + 1]; PIN(e0); PIN(e1);
          const unsigned pv = pk2(e0, e1);
          vo[q >> 2][q & 3] = r == 28 ? 0x3f803f80u : pv;
          PIN(vo[q >> 2]);
          if constexpr (q == 1 || q == 3) {
            srs[g & 1] = *(const LAS f32x4*)(stl + 8 * (g + 2) + 4 * h2); smn[g & 1] = *(const LAS f32x4*)(stl + 32 + 8 * (g + 2) + 4 * h2);
          }
        }
        if constexpr (ks == 13) { hst(rs, voff + 2048, kvo, vo[0]); hst(rs, voff + 3072, kvo, vo[1]); }
        if constexpr (ks == 14) { LAS char* kvl = aux + (8 + w) * 4096 + lane * 16; lds_wr(kvl, 0, ko[0]); lds_wr(kvl, 1024, ko[1]); lds_wr(kvl, 2048, vo[0]); lds_wr(kvl, 3072, vo[1]); }
        if constexpr (ks >= 16 && ks < 20) cq[ks - 16] = *(const LAS f32x4*)(csl + 64 + 8 * (ks - 16) + 4 * h2);
        if constexpr (ks == 25) { wait_vm_bar<0>(); }
        SB();
      });
      FS(24);
      const float qs = st.rs * (0.18898223650461362f * 1.4426950408889634f);
#pragma unroll
      for (int g = 0; g < 4; ++g) {
        float v[4];
#pragma unroll
        for (int e = 0; e < 4; ++e) { v[e] = aq[4 * g + e] - st.mean * cq[g][e]; PIN(v[e]); v[e] *= qs; PIN(v[e]); }
        qf[g >> 1][(g & 1) * 2] = pk2(v[0], v[1]); qf[g >> 1][(g & 1) * 2 + 1] = pk2(v[2], v[3]);
      }
      FS(25);
    }
    {
      IDS();
      const float NEG = -1e30f;
      f32x16 cd, co;
#pragma unroll
      for (int i = 0; i < 16; ++i) { const int key = (i & 3) + 8 * (i >> 2) + 4 * h2; cd[i] = key <= r ? 0.f : NEG; co[i] = key > r ? 0.f : NEG; }
      PIN(cd); PIN(co);
      const LAS char* kw = opq((const LAS char*)aux + lane * 16 + w * 4096);
      bar();
      FS(29);
      float m = NEG;
      f32x16 o = {};
      if (rb >= 2) {
        u32x4 kf[9][2];
#pragma unroll
        for (int it = 0; it < 9; ++it) { kf[it][0] = lds_rd(kw, it * 4096); kf[it][1] = lds_rd(kw, it * 4096 + 1024); }
        SB();
        {
          const f32x16 z = {};
          f32x16 s[3];
          static_for<11>([&](auto IT) {
            constexpr int it = IT;
            if constexpr (it < 9) { s[it % 3] = mfma(kf[it][0], qf[0], it == 0 ? co : it == 8 ? cd : z); s[it % 3] = mfma(kf[it][1], qf[1], s[it % 3]); PIN(s[it % 3]); }
            if constexpr (it >= 2) {
              const f32x16& t = s[(it - 2) % 3];
              float ma = fmaxf(fmaxf(t[0], t[1]), t[2]), mb = fmaxf(fmaxf(t[3], t[4]), t[5]);
              ma = fmaxf(fmaxf(ma, t[6]), t[7]); mb = fmaxf(fmaxf(mb, t[8]), t[9]);
              ma = fmaxf(fmaxf(ma, t[10]), t[11]); mb = fmaxf(fmaxf(mb, t[12]), t[13]);
              ma = fmaxf(fmaxf(ma, t[14]), t[15]);
              m = fmaxf(fmaxf(m, ma), mb);
              PIN(m);
            }
            SB();
          });
        }
        m = -max32(m);
        PIN(m);
        FS(30);
        f32x16 cm;
#pragma unroll
        for (int i = 0; i < 16; ++i) { cm[i] = m; co[i] = co[i] + m; }
        u32x4 vf[2][2];
        vf[0][0] = lds_rd(kw, 2048); vf[0][1] = lds_rd(kw, 3072);
        f32x16 s[3];
        s[0] = mfma(kf[0][0], qf[0], co); s[0] = mfma(kf[0][1], qf[1], s[0]);
        s[1] = mfma(kf[1][0], qf[0], cm); s[1] = mfma(kf[1][1], qf[1], s[1]);
#pragma unroll
        for (int i = 0; i < 16; ++i) cd[i] = cd[i] + m;
        SB();
        static_for<9>([&](auto IT) {
          constexpr int it = IT;
          if constexpr (it < 8) { vf[(it + 1) & 1][0] = lds_rd(kw, (it + 1) * 4096 + 2048); vf[(it + 1) & 1][1] = lds_rd(kw, (it + 1) * 4096 + 3072); }
          u32x4 p[2];
          const f32x16& c = s[it % 3];
          if constexpr (it + 2 < 9) { s[(it + 2) % 3] = mfma(kf[it + 2][0], qf[0], it + 2 == 8 ? cd : cm); }
#pragma unroll
          for (int q = 0; q < 4; ++q) p[0][q] = pk2(__builtin_amdgcn_exp2f(c[2 * q]), __builtin_amdgcn_exp2f(c[2 * q + 1]));
          PIN(p[0]);
          SB();
          if constexpr (it + 2 < 9) { s[(it + 2) % 3] = mfma(kf[it + 2][1], qf[1], s[(it + 2) % 3]); PIN(s[(it + 2) % 3]); }
          o = mfma(vf[it & 1][0], p[0], o);
#pragma unroll
          for (int q = 0; q < 4; ++q) p[1][q] = pk2(__builtin_amdgcn_exp2f(c[8 + 2 * q]), __builtin_amdgcn_exp2f(c[8 + 2 * q + 1]));
          PIN(p[1]);
          SB();
          o = mfma(vf[it & 1][1], p[1], o); PIN(o);
          SB();
        });
      } else {
        const LAS char* kq = kw;
        const f32x16 z = {};
#pragma unroll
        for (int it = 0; it < 9; ++it) {
          if (tt - 8 + it >= 0) {
            f32x16 s = mfma(lds_rd(kq, it * 4096), qf[0], it == 0 ? co : it == 8 ? cd : z);
            s = mfma(lds_rd(kq, it * 4096 + 1024), qf[1], s);
#pragma unroll
            for (int i = 0; i < 16; i += 2) m = fmaxf(fmaxf(m, s[i]), s[i + 1]);
          }
        }
        m = -max32(m);
        f32x16 cm;
#pragma unroll
        for (int i = 0; i < 16; ++i) { cd[i] += m; cm[i] = m; }
#pragma unroll
        for (int it = 0; it < 9; ++it) {
          if (it >= 1 && tt - 8 + it >= 0) {
            f32x16 s = mfma(lds_rd(kq, it * 4096), qf[0], it == 8 ? cd : cm);
            s = mfma(lds_rd(kq, it * 4096 + 1024), qf[1], s);
            u32x4 p[2];
            PACK16(p, pk2(__builtin_amdgcn_exp2f(s[i0]), __builtin_amdgcn_exp2f(s[i1])));
            o = mfma(lds_rd(kq, it * 4096 + 2048), p[0], o);
            o = mfma(lds_rd(kq, it * 4096 + 3072), p[1], o);
          }
        }
      }
      FS(31);
      const unsigned lu = __float_as_uint(o[12]);
      auto rr = __builtin_amdgcn_permlane32_swap(lu, lu, false, false);
      const float inv = __builtin_amdgcn_rcpf(__uint_as_float(rr[1]));
      u32x4 ob[2];
      PACK16(ob, pk2(o[i0] * inv, o[i1] * inv));
      const unsigned oo = OFF_OA + l * SZ_OA + (tt * KSO + 2 * j) * 1024;
      hst(rs, voff, oo, ob[0]); hst(rs, voff + 1024, oo, ob[1]);
      FS(32);
      wait_vm_bar<0>();
      FS(33);
    }
    auto proj_init = [&](int xin) { IDS(); const unsigned xo = OFF_XA + xin * SZ_XA + (ttp * KSD + 4 * fs + 2 * kh) * 1024;
      u32x4 xr[2]; xr[0] = hld(rs, voff, xo); xr[1] = hld(rs, voff + 1024, xo);
      f32x16 a;
#pragma unroll
      for (int q = 0; q < 8; ++q) { const unsigned xv = xr[q >> 2][q & 3]; a[2 * q] = bflo(xv); a[2 * q + 1] = bfhi(xv); }
      PIN(a);
      return a; };
    auto proj_finish = [&](f32x16 own, const f32x16& snd, int xout, bool final_) {
      IDS();
      u32x4 xres[2];
      LAS f32x4* xs = (LAS f32x4*)(aux) + (tt2 * 2 + kh) * 256 + lane;
      LAS f32x4* xrv = (LAS f32x4*)(aux) + (tt2 * 2 + (kh ^ 1)) * 256 + lane;
#pragma unroll
      for (int g = 0; g < 4; ++g) { f32x4 v = {snd[4 * g], snd[4 * g + 1], snd[4 * g + 2], snd[4 * g + 3]}; xs[g * 64] = v; }
      bar_lgkm();
#pragma unroll
      for (int g = 0; g < 4; ++g) { const f32x4 v = xrv[g * 64]; own[4 * g] += v.x; own[4 * g + 1] += v.y; own[4 * g + 2] += v.z; own[4 * g + 3] += v.w; }
#pragma unroll
      for (int q = 0; q < 8; ++q) xres[q >> 2][q & 3] = pk2(own[2 * q], own[2 * q + 1]);
      if (final_) {
        unsigned short* orow = out + (size_t)(ttp * 32 + r) * D_ + 64 * fs + 32 * kh + 4 * h2;
#pragma unroll
        for (int g = 0; g < 4; ++g) { u32x2 v = {xres[g >> 1][(g & 1) * 2], xres[g >> 1][(g & 1) * 2 + 1]}; *(GAS u32x2*)(orow + 8 * g) = v; }
      } else {
        const unsigned xo = OFF_XA + xout * SZ_XA + (ttp * KSD + 4 * fs + 2 * kh) * 1024;
        hst(rs, voff, xo, xres[0]); hst(rs, voff + 1024, xo, xres[1]);
        float s1[4], s2[4];
#pragma unroll
        for (int g = 0; g < 4; ++g) {
          s1[g] = (own[4 * g] + own[4 * g + 1]) + (own[4 * g + 2] + own[4 * g + 3]);
          s2[g] = __builtin_fmaf(own[4 * g], own[4 * g], own[4 * g + 1] * own[4 * g + 1]) + __builtin_fmaf(own[4 * g + 2], own[4 * g + 2], own[4 * g + 3] * own[4 * g + 3]);
        }
        const float t1 = sum32((s1[0] + s1[1]) + (s1[2] + s1[3])), t2 = sum32((s2[0] + s2[1]) + (s2[2] + s2[3]));
        if (h2 == 0) { u32x2 v = {__float_as_uint(t1), __float_as_uint(t2)}; __builtin_amdgcn_raw_buffer_store_b64(v, rs, r * 8, OFF_ST + xout * SZ_ST + (ttp * 14 + 2 * fs + kh) * 256, 16); }
      }
    };
    if (active) {
      IDS();
      f32x16 own = proj_init(2 * l), snd = {};
      bar();
      FS(34);
      u32x4 af[16];
      const unsigned ao = OFF_OA + l * SZ_OA + (ttp * KSO + kh * 16) * 1024;
      constexpr int AH = 10;
      static_for<AH>([&](auto I) { constexpr int i = I; af[i] = hld(rs, voff + i * 1024, ao); });
      const LAS char* wbo[3] = {opq(ring + 3 * CHB + kh * 3072), opq(ring + 0 * CHB + kh * 3072), opq((const LAS char*)aux + WOT_L + lane * 16 + kh * 3072)};
      const LAS char* wbs[3] = {opq(ring + 3 * CHB + kh * 2048 + (kh ^ 1) * 1024), opq(ring + 0 * CHB + kh * 2048 + (kh ^ 1) * 1024), opq((const LAS char*)aux + WOT_L + lane * 16 + kh * 2048 + (kh ^ 1) * 1024)};
      constexpr int PF = 2;
      u32x4 wo_[PF], wsn[PF];
#pragma unroll
      for (int i = 0; i < PF; ++i) { wo_[i] = lds_rd(wbo[0], i * 4096); wsn[i] = lds_rd(wbs[0], i * 4096); }
      SB();
      static_for<16>([&](auto I) {
        constexpr int i = I, n = i + PF;
        own = mfma(wo_[i % PF], af[i], own); PIN(own);
        if constexpr (n < 16) wo_[i % PF] = lds_rd(wbo[n / 7], (n % 7) * 4096);
        if constexpr (i + AH < 16) af[i + AH] = hld(rs, voff + (i + AH) * 1024, ao);
        SB();
        snd = mfma(wsn[i % PF], af[i], snd); PIN(snd);
        if constexpr (n < 16) wsn[i % PF] = lds_rd(wbs[n / 7], (n % 7) * 4096);
        SB();
      });
      FS(35);
      proj_finish(own, snd, 2 * l + 1, false);
      FS(38);
      wait_vm_bar<0>();
      FS(39);
    }
    {
      IDS();
      bar();
      FS(0);
      constexpr int AH = 10;
      u32x4 af[KSD];
      const unsigned xo = OFF_XA + (2 * l + 1) * SZ_XA + tt * (KSD * 1024);
      StatLd sl; stats_issue(sl, rs, OFF_ST + (2 * l + 1) * SZ_ST + tt * 3584, lane);
      Stats st;
      static_for<AH>([&](auto I) { constexpr int ks = I; af[ks] = hld(rs, voff + ks * 1024, xo); });
      const LAS float* csl = (const LAS float*)(aux + CSG_L) + 4 * h2;
      const unsigned ho = OFF_HA + l * SZ_HA + (tt * KSF + 7 * j) * 1024;
      f32x16 prev = {}, acc = {};
      u32x4 wr[4];
      {
        const LAS char* w0 = slot(5); const LAS char* w1 = slot(6);
        constexpr int PF = 3;
        u32x4 r0[PF], r1[PF];
#pragma unroll
        for (int i = 0; i < PF; ++i) { r0[i] = lds_rd(w0, i * 1024); r1[i] = lds_rd(w1, i * 1024); }
        SB();
        FS(2);
        static_for<KSD>([&](auto I) {
          constexpr int ks = I;
          prev = mfma(r0[ks % PF], af[ks], prev); PIN(prev);
          if constexpr (ks + PF < KSD) r0[ks % PF] = lds_rd(w0, (ks + PF) * 1024);
          if constexpr (ks + AH < KSD) af[ks + AH] = hld(rs, voff + (ks + AH) * 1024, xo);
          if constexpr (ks == 20) { st = stats_finish(sl); PIN(st.mean); PIN(st.rs); }
          SB();
          acc = mfma(r1[ks % PF], af[ks], acc); PIN(acc);
          if constexpr (ks + PF < KSD) r1[ks % PF] = lds_rd(w1, (ks + PF) * 1024);
          SB();
        });
      }
      FS(3);
      const float c1 = -1.4426950408889634f * st.rs, vk = __builtin_amdgcn_rcpf(st.rs * st.rs);
      u32x4 ov;
      f32x4 cc[4];
      float ea[8], eb[8], ex[8];
#define GU_STAGE(P, v, S) do { \
        if constexpr ((S) == 0) { ea[v] = P[v] - st.mean * cc[(v) >> 2][(v) & 3]; PIN(ea[v]); eb[v] = P[(v) + 8] - st.mean * cc[2 + ((v) >> 2)][(v) & 3]; PIN(eb[v]); } \
        else if constexpr ((S) == 1) { ex[v] = c1 * ea[v]; PIN(ex[v]); eb[v] = ea[v] * eb[v]; PIN(eb[v]); } \
        else if constexpr ((S) == 2) { ex[v] = __builtin_amdgcn_exp2f(ex[v]); PIN(ex[v]); } \
        else if constexpr ((S) == 3) { ex[v] = __builtin_fmaf(ex[v], vk, vk); PIN(ex[v]); } \
        else if constexpr ((S) == 4) { ex[v] = __builtin_amdgcn_rcpf(ex[v]); PIN(ex[v]); } \
        else { eb[v] = eb[v] * ex[v]; PIN(eb[v]); if constexpr ((v) & 1) { ov[(v) >> 1] = pk2(eb[(v) - 1], eb[v]); PIN(ov); } } } while (0)
#define GU_GAP(P, g, base, sp) static_for<8>([&](auto V_) { constexpr int v_ = V_; constexpr int s_ = (g) - (base) - (sp) * v_; if constexpr (s_ >= 0 && s_ < 6) GU_STAGE(P, v_, s_); })
#define GU_CC(t) _Pragma("unroll") for (int g = 0; g < 4; ++g) cc[g] = *(const LAS f32x4*)(csl + (t) * 32 + 8 * g)
      GU_CC(0);
      bar();
      { const LAS char* w2 = slot(7);
#pragma unroll
        for (int i = 0; i < 4; ++i) wr[i] = lds_rd(w2, i * 1024); }
      SB();
      FS(4);
      f32x16 t1 = acc;
      static_for<5>([&](auto T) {
        constexpr int t = T + 2;
        const LAS char* wl = slot(5 + t);
        const LAS char* wn = slot(6 + t);
        acc = f32x16{};
        static_for<KSD>([&](auto I) {
          constexpr int ks = I;
          if constexpr (ks == 14) {
            if constexpr (t < 6) bar();
            else asm volatile("s_waitcnt vmcnt(0)\n\ts_barrier" ::: "memory");
            SB();
          }
          acc = mfma(wr[ks & 3], af[ks], acc); PIN(acc);
          if constexpr (ks + 4 < KSD) wr[ks & 3] = lds_rd(wl, (ks + 4) * 1024);
          else if constexpr (t < 6) wr[ks & 3] = lds_rd(wn, (ks + 4 - KSD) * 1024);
          if constexpr (t == 2) {
            if constexpr (ks < 14) { GU_GAP(prev, ks, 0, 1); }
            if constexpr (ks == 12) { GU_CC(1); }
            if constexpr (ks == 13) hst(rs, voff, ho, ov);
            if constexpr (ks >= 14) { GU_GAP(t1, ks, 15, 1); }
            if constexpr (ks == 27) { hst(rs, voff + 1024, ho, ov); GU_CC(2); }
          } else {
            GU_GAP(prev, ks, 1, 3);
            if constexpr (ks == 27) { hst(rs, voff + (t - 1) * 1024, ho, ov); GU_CC(t); }
          }
          SB();
        });
        prev = acc;
        FS(3 + t);
      });
      static_for<13>([&](auto G) { constexpr int g = G; GU_GAP(prev, g, 0, 1); });
      hst(rs, voff + 6 * 1024, ho, ov);
      FS(10);
      wait_vm_bar<0>();
      FS(11);
    }
    if (active) {
      IDS();
      f32x16 own = proj_init(2 * l + 1), snd = {};
      bar();
      FS(41);
      constexpr int WIN = DN_WIN;
      u32x4 af[WIN];
      const unsigned ao = OFF_HA + l * SZ_HA + (ttp * KSF + kh * 56) * 1024;
      static_for<12>([&](auto I) { constexpr int i = I; af[i] = hld(rs, voff + kk_of(i) * 1024, ao); });
      const int oo_ = kh * 3072, so_ = kh * 2048 + (kh ^ 1) * 1024;
      constexpr int PF = 2;
      u32x4 wo_[PF], wsn[PF];
      const LAS char* po = opq(ring + 0 * CHB + oo_); const LAS char* ps = opq(ring + 0 * CHB + so_);
#pragma unroll
      for (int i = 0; i < PF; ++i) { wo_[i] = lds_rd(po, i * 4096); wsn[i] = lds_rd(ps, i * 4096); }
      SB();
      FS(42);
      const LAS char* pon = po; const LAS char* psn = ps;
      static_for<56>([&](auto I) {
        constexpr int i = I, c = i / 7, s = i % 7, n = s + PF;
        if constexpr (s == 0) { po = pon; ps = psn; if constexpr (c < 7) { pon = opq(ring + ((13 + c) & 3) * CHB + oo_); psn = opq(ring + ((13 + c) & 3) * CHB + so_); } }
        if constexpr (i == 1) FS(43);
        if constexpr (i == 28) FS(44);
        if constexpr (s == 3) { bar(); SB(); }
        const u32x4 a = af[i % WIN];
        own = mfma(wo_[i % PF], a, own); PIN(own);
        if constexpr (n < 7) wo_[i % PF] = lds_rd(po, n * 4096); else if constexpr (c < 7) wo_[i % PF] = lds_rd(pon, (n - 7) * 4096);
        if constexpr (i < WIN - 12) af[12 + i] = hld(rs, voff + kk_of(12 + i) * 1024, ao);
        SB();
        snd = mfma(wsn[i % PF], a, snd); PIN(snd);
        if constexpr (n < 7) wsn[i % PF] = lds_rd(ps, n * 4096); else if constexpr (c < 7) wsn[i % PF] = lds_rd(psn, (n - 7) * 4096);
        if constexpr (i + WIN < 56) af[i % WIN] = hld(rs, voff + kk_of(i + WIN) * 1024, ao);
        SB();
      });
      FS(45);
      proj_finish(own, snd, 2, l == 1);
      FS(46);
      wait_vm_bar<0>();
      FS(47);
    }
    if (l == 0) FS(61);
  }
}

}

extern "C" size_t stamp_offset() { return OFF_STAMP; }

extern "C" void kernel_launch(void* const* d_in, const int* in_sizes, int n_in, void* d_out, int out_size, void* d_ws, size_t ws_size,
                              hipStream_t stream) {
  const unsigned short* x = (const unsigned short*)d_in[0];
  const unsigned short* wqkv = (const unsigned short*)d_in[1];
  const unsigned short* wo = (const unsigned short*)d_in[2];
  const unsigned short* w1 = (const unsigned short*)d_in[4];
  const unsigned short* w3 = (const unsigned short*)d_in[5];
  const unsigned short* w2 = (const unsigned short*)d_in[6];
  char* ws = (char*)d_ws;
  (void)hipFuncSetAttribute((const void*)k_main, hipFuncAttributeMaxDynamicSharedMemorySize, LDS_BYTES);
  k_prep<<<PREP_BLOCKS, 256, PREP_LDS, stream>>>(x, wqkv, wo, w1, w3, w2, ws);
  k_main<<<256, 512, LDS_BYTES, stream>>>(ws, (unsigned short*)d_out);
}
```

```cpp
#include <hip/hip_runtime.h>
#include <stdint.h>
#include <utility>

typedef __attribute__((ext_vector_type(8))) __bf16 bf16x8;
typedef __attribute__((ext_vector_type(2))) __bf16 bf16x2;
typedef __attribute__((ext_vector_type(2))) float f32x2;
typedef __attribute__((ext_vector_type(4))) float f32x4;
typedef __attribute__((ext_vector_type(16))) float f32x16;
typedef __attribute__((ext_vector_type(4))) unsigned u32x4;
typedef __attribute__((ext_vector_type(2))) unsigned u32x2;
#define DEV __device__ __forceinline__
#define LAS __attribute__((address_space(3)))
#define GAS __attribute__((address_space(1)))
typedef GAS unsigned gu32;
#define RLX __ATOMIC_RELAXED, __HIP_MEMORY_SCOPE_AGENT

namespace {

constexpr int D_ = 448, F_ = 1792;
constexpr int NTT = 64;
constexpr int KSD = 28;
constexpr int KSO = 32;
constexpr int KSF = 112;
constexpr int CHB = 28672;

constexpr unsigned OFF_FLAG = 0;
constexpr int FL_KV = 0;
constexpr int FL_O = FL_KV + 512;
constexpr int FL_X1 = FL_O + 32;
constexpr int FL_H = FL_X1 + 32;
constexpr int FL_X2 = FL_H + 64;
constexpr int FL_TMO = FL_X2 + 16;
constexpr int NFLAG = FL_TMO + 1;
constexpr unsigned OFF_WQKV = 96 * 1024;
constexpr unsigned OFF_WO   = OFF_WQKV + 16 * 3 * CHB;
constexpr unsigned OFF_WGU  = OFF_WO + 7 * 64 * 1024;
constexpr unsigned OFF_WDN  = OFF_WGU + 16 * 7 * CHB;
constexpr unsigned OFF_CSQ  = OFF_WDN + 7 * 224 * 1024;
constexpr unsigned OFF_CSG  = OFF_CSQ + 16 * 3 * 32 * 4;
constexpr unsigned SZ_XA    = NTT * KSD * 1024;
constexpr unsigned OFF_XA   = OFF_CSG + 16 * 7 * 32 * 4;
constexpr unsigned SZ_ST    = NTT * 14 * 32 * 8;
constexpr unsigned OFF_ST   = OFF_XA + 4 * SZ_XA;
constexpr unsigned SZ_KV    = NTT * 16 * 4096;
constexpr unsigned OFF_KV   = OFF_ST + 4 * SZ_ST;
constexpr unsigned SZ_OA    = NTT * KSO * 1024;
constexpr unsigned OFF_OA   = OFF_KV + 2 * SZ_KV;
constexpr unsigned SZ_HA    = NTT * KSF * 1024;
constexpr unsigned OFF_HA   = OFF_OA + 2 * SZ_OA;
constexpr unsigned WS_END   = OFF_HA + 2 * SZ_HA;
constexpr unsigned OFF_STAMP = (WS_END + 4095) / 4096 * 4096;
static_assert(NFLAG * 128 <= OFF_WQKV, "flags");
static_assert(OFF_XA % 128 == 0 && OFF_ST % 128 == 0 && OFF_KV % 128 == 0 && OFF_OA % 128 == 0 && OFF_HA % 128 == 0, "lines");

#define STAMP(i) do {} while (0)
#define FS(i) do {} while (0)

template <class F, int... I> DEV void static_for_impl(F&& f, std::integer_sequence<int, I...>) { (f(std::integral_constant<int, I>{}), ...); }
template <int N, class F> DEV void static_for(F&& f) { static_for_impl(f, std::make_integer_sequence<int, N>{}); }

DEV int lane_id() { int l; asm volatile("v_mbcnt_lo_u32_b32 %0, -1, 0\n\tv_mbcnt_hi_u32_b32 %0, -1, %0" : "=v"(l)); return l; }
DEV unsigned pk2(float a, float b) { f32x2 v = {a, b}; return __builtin_bit_cast(unsigned, __builtin_convertvector(v, bf16x2)); }
DEV f32x16 mfma(u32x4 a, u32x4 b, f32x16 c) {
  return __builtin_amdgcn_mfma_f32_32x32x16_bf16(__builtin_bit_cast(bf16x8, a), __builtin_bit_cast(bf16x8, b), c, 0, 0, 0);
}
DEV float bflo(unsigned u) { return __uint_as_float(u << 16); }
DEV float bfhi(unsigned u) { return __uint_as_float(u & 0xffff0000u); }
DEV float swap32(float v) {
  const unsigned u = __float_as_uint(v);
  auto rr = __builtin_amdgcn_permlane32_swap(u, u, false, false);
  return __uint_as_float((threadIdx.x & 32) ? rr[0] : rr[1]);
}
DEV float sum32(float v) {
  const unsigned u = __float_as_uint(v);
  auto rr = __builtin_amdgcn_permlane32_swap(u, u, false, false);
  return __uint_as_float(rr[0]) + __uint_as_float(rr[1]);
}
DEV float max32(float v) {
  const unsigned u = __float_as_uint(v);
  auto rr = __builtin_amdgcn_permlane32_swap(u, u, false, false);
  return fmaxf(__uint_as_float(rr[0]), __uint_as_float(rr[1]));
}
DEV void dma7(const char* g, unsigned voff, unsigned lds) {
  unsigned keep;
  const char* g2 = g + 4096;
  lds = __builtin_amdgcn_readfirstlane(lds);
  asm volatile("s_mov_b32 %0, m0\n\ts_mov_b32 m0, %2\n\ts_nop 0\n\t"
               "global_load_lds_dwordx4 %1, %3 offset:0\n\t"
               "global_load_lds_dwordx4 %1, %3 offset:1024\n\t"
               "global_load_lds_dwordx4 %1, %3 offset:2048\n\t"
               "global_load_lds_dwordx4 %1, %3 offset:3072\n\t"
               "s_add_u32 m0, m0, 0x1000\n\ts_nop 0\n\t"
               "global_load_lds_dwordx4 %1, %4 offset:0\n\t"
               "global_load_lds_dwordx4 %1, %4 offset:1024\n\t"
               "global_load_lds_dwordx4 %1, %4 offset:2048\n\t"
               "s_mov_b32 m0, %0"
               : "=&s"(keep) : "v"(voff), "s"(lds), "s"(g), "s"(g2) : "memory", "scc");
}
template <int N> DEV void wait_vm() { asm volatile("s_waitcnt vmcnt(%0)" ::"n"(N) : "memory"); }
template <int N> DEV void wait_vm_bar() { asm volatile("s_waitcnt vmcnt(%0) lgkmcnt(0)\n\ts_barrier" ::"n"(N) : "memory"); }
DEV void bar_lgkm() { asm volatile("s_waitcnt lgkmcnt(0)\n\ts_barrier" ::: "memory"); }
DEV u32x4 lds_rd(const LAS char* base, int off) { return *(const LAS u32x4*)(base + off); }
DEV void lds_wr(LAS char* base, int off, u32x4 v) { *(LAS u32x4*)(base + off) = v; }
DEV u32x4 gld(const char* p) { return *(const GAS u32x4*)(p); }
DEV void gst(char* p, u32x4 v) { *(GAS u32x4*)(p) = v; }
typedef __amdgpu_buffer_rsrc_t rsrc_t;
DEV u32x4 hld(rsrc_t rs, unsigned voff, unsigned soff) { return __builtin_amdgcn_raw_buffer_load_b128(rs, voff, soff, 16); }
DEV void hst(rsrc_t rs, unsigned voff, unsigned soff, u32x4 v) { __builtin_amdgcn_raw_buffer_store_b128(v, rs, voff, soff, 16); }

struct Poll { unsigned v0, v1, v2, v3; };
DEV void poll_ge(Poll& q, char* ws, int flag, unsigned target, int on) {
  const char* c = ws + OFF_FLAG + (size_t)flag * 128;
  unsigned cnt, tmp;
  on = __builtin_amdgcn_readfirstlane(on);
  asm volatile(
      "s_mov_b32 %4, 0\n\t"
      "s_cmp_eq_u32 %9, 0\n\t"
      "s_cbranch_scc1 2f\n"
      "1:\n\t"
      "global_load_dword %0, %6, %7 sc1\n\t"
      "s_waitcnt vmcnt(0)\n\tv_readfirstlane_b32 %5, %0\n\ts_nop 0\n\ts_cmp_ge_u32 %5, %8\n\ts_cbranch_scc1 2f\n\ts_sleep 1\n\t"
      "s_add_u32 %4, %4, 1\n\ts_cmp_lt_u32 %4, 0x8000\n\ts_cbranch_scc1 1b\n"
      "2:"
      : "+v"(q.v0), "+v"(q.v1), "+v"(q.v2), "+v"(q.v3), "=&s"(cnt), "=&s"(tmp)
      : "v"(0u), "s"(c), "s"(target), "s"(on)
      : "memory", "scc");
  if (cnt >= 0x8000u && lane_id() == 0) __hip_atomic_store((gu32*)(ws + OFF_FLAG + (size_t)FL_TMO * 128), 1u, RLX);
}
DEV void poll_done(Poll& q, int on) {
  on = __builtin_amdgcn_readfirstlane(on);
  asm volatile("s_cmp_eq_u32 %4, 0\n\ts_cbranch_scc1 3f\n\ts_waitcnt vmcnt(0)\n3:" : "+v"(q.v0), "+v"(q.v1), "+v"(q.v2), "+v"(q.v3) : "s"(on) : "scc");
}
DEV void sig_add(char* ws, int flag) { (void)__hip_atomic_fetch_add((gu32*)(ws + OFF_FLAG + (size_t)flag * 128), 1u, RLX); }

struct Stats { float mean, rs; };
DEV Stats load_stats(rsrc_t rs, unsigned off_st, int tt, int lane) {
  const int r = lane & 31, h2 = lane >> 5;
  float s1 = 0.f, s2 = 0.f;
  u32x2 v[7];
#pragma unroll
  for (int i = 0; i < 7; ++i) v[i] = __builtin_amdgcn_raw_buffer_load_b64(rs, h2 * 1792 + r * 8 + i * 256, off_st + tt * 3584, 16);
#pragma unroll
  for (int i = 0; i < 7; ++i) { s1 += __uint_as_float(v[i].x); s2 += __uint_as_float(v[i].y); }
  s1 = sum32(s1); s2 = sum32(s2);
  Stats s; s.mean = s1 * (1.0f / D_);
  const float var = fmaxf(s2 * (1.0f / D_) - s.mean * s.mean, 0.f);
  s.rs = __builtin_amdgcn_rsqf(var + 1e-5f);
  return s;
}
constexpr int G1T = 5, G1S = 8 * G1T;
__host__ __device__ constexpr int kk_of(int i) { return i < G1S ? 7 * (i / G1T) + i % G1T : 7 * ((i - G1S) / (7 - G1T)) + G1T + (i - G1S) % (7 - G1T); }

typedef short s16x4 __attribute__((ext_vector_type(4)));
DEV u32x2 tr_read(const LAS char* p) { return __builtin_bit_cast(u32x2, __builtin_amdgcn_ds_read_tr16_b64_v4i16((LAS s16x4*)p)); }
template <int KIND>
DEV void prep_unit(const unsigned short* __restrict__ src, const unsigned short* __restrict__ src2, int ld, int col, char* __restrict__ dst, float* cs, int a1, int a2, char* smem) {
  const int tid = threadIdx.x, lane = tid & 63, w = tid >> 6, r = lane & 31, h2 = lane >> 5;
  LAS char* img = (LAS char*)smem;
  if (KIND == 0) {
    u32x2 v[14];
    const int p = tid & 7, row0 = tid >> 3;
#pragma unroll
    for (int n = 0; n < 14; ++n) v[n] = p < 7 ? *(const GAS u32x2*)(src + (size_t)(row0 + 32 * n) * ld + col + 4 * p) : u32x2{0u, 0u};
#pragma unroll
    for (int n = 0; n < 14; ++n) *(LAS u32x2*)(img + (row0 + 32 * n) * 64 + p * 8) = v[n];
  } else {
    u32x4 v[7];
    const int p = tid & 3, row0 = tid >> 2;
    const unsigned short* sp = KIND == 1 ? (p < 2 ? src + col + 8 * p : src2 + col + 8 * (p - 2)) : src + col + 8 * p;
#pragma unroll
    for (int n = 0; n < 7; ++n) v[n] = *(const GAS u32x4*)(sp + (size_t)((KIND == 3 ? 448 * a2 : 0) + row0 + 64 * n) * ld);
#pragma unroll
    for (int n = 0; n < 7; ++n) {
      const int row = row0 + 64 * n;
      const int irow = KIND == 2 ? 32 * (row / 28) + row % 28 : row;
      *(LAS u32x4*)(img + irow * 64 + p * 16) = v[n];
    }
    if (KIND == 2) { const int head = tid >> 4, d = 28 + ((tid >> 2) & 3); *(LAS u32x4*)(img + (32 * head + d) * 64 + p * 16) = u32x4{0u, 0u, 0u, 0u}; }
  }
  __syncthreads();
  constexpr int NKS = KIND == 2 ? 32 : 28;
  const LAS char* rd = img + (4 * h2 + ((lane & 15) >> 2)) * 64 + ((lane >> 4) & 1) * 32 + (lane & 3) * 8;
  float sum = 0.f;
#pragma unroll
  for (int n = 0; n < NKS / 4; ++n) {
    const int i = w + 4 * n;
    const u32x2 lo = tr_read(rd + i * 1024), hi = tr_read(rd + i * 1024 + 512);
    const u32x4 v = {lo.x, lo.y, hi.x, hi.y};
    size_t doff;
    if (KIND <= 1) doff = (size_t)i * 1024;
    else if (KIND == 2) doff = (size_t)(((i & 15) * 2 + (i >> 4)) * 2 + a1) * 1024;
    else {
      const int ks = 28 * a2 + i, kh = ks / 56, kk = ks % 56, j8 = kk / 7, t = kk % 7;
      const int step = t < G1T ? G1T * j8 + t : G1S + (7 - G1T) * j8 + (t - G1T);
      doff = (size_t)((step * 2 + kh) * 2 + a1) * 1024;
    }
    gst(dst + doff + lane * 16, v);
    if (KIND <= 1) {
#pragma unroll
      for (int q = 0; q < 4; ++q) sum += bflo(v[q]) + bfhi(v[q]);
    }
  }
  if (KIND <= 1) {
    sum = sum32(sum);
    __syncthreads();
    LAS float* red = (LAS float*)img;
    if (h2 == 0) red[w * 32 + r] = sum;
    __syncthreads();
    if (tid < 32) cs[tid] = (red[tid] + red[32 + tid]) + (red[64 + tid] + red[96 + tid]);
  }
}
constexpr int PREP_LDS = 32768;
constexpr int PREP_BLOCKS = 8 * 38;
__global__ __launch_bounds__(256) void k_prep(const unsigned short* __restrict__ x, const unsigned short* __restrict__ wqkv,
                                              const unsigned short* __restrict__ wo, const unsigned short* __restrict__ w1,
                                              const unsigned short* __restrict__ w3, const unsigned short* __restrict__ w2, char* __restrict__ ws) {
  extern __shared__ __attribute__((aligned(16))) char smem[];
  const int b = blockIdx.x, tid = threadIdx.x, lane = tid & 63, w = tid >> 6, r = lane & 31, h2 = lane >> 5;
  const int xg = b & 7, k = b >> 3;
  if (k >= 30) {
    const int tt = xg * 8 + (k - 30);
    if (tt < 3) { const int f = tt * 256 + tid; if (f < NFLAG) *(GAS unsigned*)(ws + OFF_FLAG + (size_t)f * 128) = 0u; }
    LAS char* img = (LAS char*)smem;
    u32x4 v[7];
#pragma unroll
    for (int n = 0; n < 7; ++n) { const int c = tid + 256 * n; v[n] = *(const GAS u32x4*)(x + (size_t)tt * 32 * D_ + c * 8); }
#pragma unroll
    for (int n = 0; n < 7; ++n) { const int c = tid + 256 * n; *(LAS u32x4*)(img + (c / 56) * 912 + (c % 56) * 16) = v[n]; }
    __syncthreads();
    for (int ft = w; ft < 14; ft += 4) {
      float s1 = 0.f, s2 = 0.f;
#pragma unroll
      for (int s = 0; s < 2; ++s) {
        const int ks = 2 * ft + s;
        const u32x2 a = *(const LAS u32x2*)(img + r * 912 + ks * 32 + 8 * h2), c = *(const LAS u32x2*)(img + r * 912 + ks * 32 + 16 + 8 * h2);
        const u32x4 o = {a.x, a.y, c.x, c.y};
        gst(ws + OFF_XA + ((size_t)tt * KSD + ks) * 1024 + lane * 16, o);
#pragma unroll
        for (int q = 0; q < 4; ++q) { const float lo = bflo(o[q]), hi = bfhi(o[q]); s1 += lo + hi; s2 += lo * lo + hi * hi; }
      }
      s1 = sum32(s1); s2 = sum32(s2);
      if (h2 == 0) { f32x2 o = {s1, s2}; *(GAS f32x2*)(ws + OFF_ST + ((size_t)tt * 14 + ft) * 256 + r * 8) = o; }
    }
    return;
  }
  if (k < 14) {
    const int u = xg * 14 + k, j = u / 7, t = u % 7;
    prep_unit<1>(w1, w3, F_, 112 * j + 16 * t, ws + OFF_WGU + (size_t)u * CHB, (float*)(ws + OFF_CSG) + u * 32, 0, 0, smem);
  } else if (k < 20) {
    const int u = xg * 6 + (k - 14), j = u / 3, p = u % 3;
    prep_unit<0>(wqkv, nullptr, 3 * D_, (p == 2 ? 0 : p == 0 ? D_ : 2 * D_) + 28 * j, ws + OFF_WQKV + (size_t)u * CHB, (float*)(ws + OFF_CSQ) + u * 32, 0, 0, smem);
  } else if (xg < 7) {
    if (k < 28) { const int fs = xg, tile = (k - 20) & 1, q = (k - 20) >> 1;
      prep_unit<3>(w2, nullptr, D_, 64 * fs + 32 * tile, ws + OFF_WDN + (size_t)fs * 224 * 1024, nullptr, tile, q, smem);
    } else { const int fs = xg, tile = k - 28;
      prep_unit<2>(wo, nullptr, D_, 64 * fs + 32 * tile, ws + OFF_WO + (size_t)fs * 64 * 1024, nullptr, tile, 0, smem);
    }
  }
}

constexpr int AUX = 4 * CHB;
constexpr int LDS_BYTES = AUX + 49152;
#define SB() __builtin_amdgcn_sched_barrier(0)
#define PIN(x) asm volatile("" : "+v"(x))
#define PACK16(o, expr) _Pragma("unroll") for (int q_ = 0; q_ < 8; ++q_) { const int i0 = 2 * q_, i1 = 2 * q_ + 1; (void)i0; (void)i1; o[q_ >> 2][q_ & 3] = expr; }
template <class T> DEV void opaque_s(T& v) { asm volatile("" : "+s"(v)); }

struct StatLd { u32x2 v[7]; };
DEV void stats_issue(StatLd& s, rsrc_t rs, unsigned soff, int lane) {
  const unsigned vo = (lane >> 5) * 1792 + (lane & 31) * 8;
#pragma unroll
  for (int i = 0; i < 7; ++i) s.v[i] = __builtin_amdgcn_raw_buffer_load_b64(rs, vo + i * 256, soff, 16);
}
DEV Stats stats_finish(const StatLd& s) {
  float s1 = 0.f, s2 = 0.f;
#pragma unroll
  for (int i = 0; i < 7; ++i) { s1 += __uint_as_float(s.v[i].x); s2 += __uint_as_float(s.v[i].y); }
  s1 = sum32(s1); s2 = sum32(s2);
  Stats o; o.mean = s1 * (1.0f / D_);
  const float var = fmaxf(s2 * (1.0f / D_) - o.mean * o.mean, 0.f);
  o.rs = __builtin_amdgcn_rsqf(var + 1e-5f);
  return o;
}

template <int N> DEV void dma_n(rsrc_t rs, unsigned voff, unsigned lds, unsigned soff) {
  if constexpr (N == 4)
    asm volatile("s_mov_b32 m0, %0\n\ts_nop 0\n\tbuffer_load_dwordx4 %1, %2, %3 offen lds\n\tbuffer_load_dwordx4 %1, %2, %3 offen offset:1024 lds\n\t"
                 "buffer_load_dwordx4 %1, %2, %3 offen offset:2048 lds\n\tbuffer_load_dwordx4 %1, %2, %3 offen offset:3072 lds" ::"s"(lds), "v"(voff), "s"(rs), "s"(soff) : "memory");
  else if constexpr (N == 3)
    asm volatile("s_mov_b32 m0, %0\n\ts_nop 0\n\tbuffer_load_dwordx4 %1, %2, %3 offen lds\n\tbuffer_load_dwordx4 %1, %2, %3 offen offset:1024 lds\n\t"
                 "buffer_load_dwordx4 %1, %2, %3 offen offset:2048 lds" ::"s"(lds), "v"(voff), "s"(rs), "s"(soff) : "memory");
  else
    asm volatile("s_mov_b32 m0, %0\n\ts_nop 0\n\tbuffer_load_dwordx4 %1, %2, %3 offen lds\n\tbuffer_load_dwordx4 %1, %2, %3 offen offset:1024 lds" ::"s"(lds), "v"(voff), "s"(rs), "s"(soff) : "memory");
}
DEV void dma_chunk(rsrc_t rs, unsigned voff, unsigned lds, unsigned soff, int w) {
  const unsigned o = (w - 1) * 9216;
  dma_n<4>(rs, voff, lds + o, soff + o); dma_n<4>(rs, voff, lds + o + 4096, soff + o + 4096);
  if (w == 3) dma_n<2>(rs, voff, lds + o + 8192, soff + o + 8192);
  else asm volatile("s_mov_b32 m0, %0\n\ts_nop 0\n\tbuffer_load_dwordx4 %1, %2, %3 offen lds" ::"s"(lds + o + 8192), "v"(voff), "s"(rs), "s"(soff + o + 8192) : "memory");
}
DEV void dma2(rsrc_t rs, unsigned voff, unsigned lds, unsigned soff) {
  asm volatile("s_mov_b32 m0, %0\n\ts_nop 0\n\t"
               "buffer_load_dwordx4 %1, %2, %3 offen lds\n\t"
               "buffer_load_dwordx4 %1, %2, %3 offen offset:1024 lds"
               ::"s"(lds), "v"(voff), "s"(rs), "s"(soff) : "memory");
}
DEV void bar() { asm volatile("s_barrier" ::: "memory"); }
DEV const LAS char* opq(const LAS char* p) { asm volatile("" : "+v"(p)); return p; }

constexpr int DN_WIN = 23;

__global__ __launch_bounds__(512, 2) void k_main(char* __restrict__ ws, unsigned short* __restrict__ out) {
  extern __shared__ __attribute__((aligned(16))) char smem[];
  const int w8_ = __builtin_amdgcn_readfirstlane(threadIdx.x >> 6);
  const bool helper = w8_ >= 4;
  const int w_ = w8_ & 3;
  const int b = blockIdx.x, idx = b >> 3;
#define MAPA 1
  const int x_ = b & 7;
  const int rb_ = MAPA == 0 ? 2 * x_ + (idx >> 4) : MAPA == 1 ? 4 * (x_ & 3) + (idx >> 3) : 8 * (x_ & 1) + (idx >> 2);
  const int j_ = MAPA == 0 ? (idx & 15) : MAPA == 1 ? 8 * (x_ >> 2) + (idx & 7) : 4 * (x_ >> 1) + (idx & 3);
  const int rp_ = 2 * x_ + (idx >> 4), m_ = idx & 15;
  const bool active = m_ < 14;
  const unsigned lds0 = __builtin_amdgcn_readfirstlane((unsigned)(uintptr_t)smem);
  const rsrc_t rs = __builtin_amdgcn_make_buffer_rsrc(ws, 0, WS_END, 0x00020000);
#define IDS() \
  int w = w_, rb = rb_, j = j_, rp = rp_, mm = m_; asm volatile("" : "+s"(w), "+s"(rb), "+s"(j), "+s"(rp), "+s"(mm)); \
  const int lane = lane_id(), r = lane & 31, h2 = lane >> 5; \
  const bool t0 = w == 0 && lane == 0; (void)t0; \
  const int th = mm >= 7 ? 1 : 0, fs = mm - 7 * th, tt2 = w & 1, kh = w >> 1; \
  const int tt = 4 * rb + w, ttp = 4 * rp + 2 * th + tt2; \
  const unsigned voff = lane * 16; \
  const LAS char* ring = (const LAS char*)smem + lane * 16; \
  LAS char* aux = (LAS char*)smem + AUX; \
  (void)r; (void)h2; (void)fs; (void)kh; (void)tt; (void)ttp; (void)voff; (void)ring; (void)aux; (void)tt2; (void)rp;
#define slot(c) opq(ring + ((c) & 3) * CHB)
  constexpr int CSQ_L = 0;
  constexpr int CSG_L = 16384;
  constexpr int WOT_L = 24576;

  if (helper) {
    auto issue = [&](int c, int l) {
      const int sl = c & 3;
      if (c >= 20) { c -= 20; ++l; }
      const bool proj = (c >= 3 && c < 5) || c >= 12;
      int w = w_, j = j_, mm = m_; asm volatile("" : "+s"(w), "+s"(j), "+s"(mm));
      const int fs = mm >= 7 ? mm - 7 : mm;
      unsigned src;
      if (c < 3) src = OFF_WQKV + (j * 3 + c) * CHB;
      else if (c < 5) src = OFF_WO + fs * 65536 + (c - 3) * CHB;
      else if (c < 12) src = OFF_WGU + (j * 7 + c - 5) * CHB;
      else src = OFF_WDN + fs * 229376 + (c - 12) * CHB;
      if (!(l < 2 && (active || !proj))) src = WS_END;
      if (w != 0) dma_chunk(rs, lane_id() * 16, lds0 + sl * CHB, src, w);
    };
    auto stage_csq = [&]() { IDS(); if (w == 3 && lane < 24) *(LAS u32x4*)(aux + CSQ_L + lane * 16) = gld(ws + OFF_CSQ + j * 384 + lane * 16); };
    stage_csq();
    issue(0, 0); issue(1, 0); issue(2, 0);
    Poll pq = {0u, 0u, 0u, 0u};
#pragma unroll 1
    for (int l = 0; l < 2; ++l) {
      IDS();
      const int pw = w == 0 ? 1 : 0;
      poll_ge(pq, ws, FL_X2 + rb, 14, l == 1 ? pw : 0);
      if (w != 0) wait_vm<0>();
      bar_lgkm();
      poll_done(pq, pw);
      bar();
      bar();
      if (w == 1 && lane == 0) sig_add(ws, FL_KV + (l * 16 + rb) * 16 + j);
      if (rb >= 1) {
        poll_ge(pq, ws, FL_KV + (l * 16 + rb - 1) * 16 + j, 1, 1);
        poll_done(pq, 1);
        poll_ge(pq, ws, FL_KV + (l * 16 + rb - 2) * 16 + j, 1, rb >= 2 ? 1 : 0);
        poll_done(pq, 1);
        u32x4 hv[8];
#pragma unroll
        for (int n = 0; n < 2; ++n) {
          const int rel = 2 * w + n, kt = max(4 * rb - 8 + rel, 0);
#pragma unroll
          for (int q = 0; q < 4; ++q) hv[n * 4 + q] = hld(rs, voff + q * 1024, OFF_KV + l * SZ_KV + (kt * 16 + j) * 4096);
        }
        SB();
#pragma unroll
        for (int n = 0; n < 8; ++n) lds_wr(aux + lane * 16, (2 * w) * 4096 + n * 1024, hv[n]);
      }
      bar_lgkm();
      issue(3, l); issue(4, l); issue(5, l); issue(6, l);
      bar();
      if (w == 1 && lane == 0) sig_add(ws, FL_O + l * 16 + rb);
      if (w == 3 && lane < 56) *(LAS u32x4*)(aux + CSG_L + lane * 16) = gld(ws + OFF_CSG + j * 896 + lane * 16);
      if (active) {
        if (w == 1 || w == 2) dma_n<4>(rs, voff, lds0 + AUX + WOT_L + (w - 1) * 4096, OFF_WO + fs * 65536 + 2 * CHB + (w - 1) * 4096);
        poll_ge(pq, ws, FL_O + l * 16 + rp, 16, pw);
        if (w != 0) wait_vm<0>();
        bar_lgkm();
        poll_done(pq, pw);
        bar();
        bar();
        if (w == 1 && lane == 0) sig_add(ws, FL_X1 + l * 16 + rp);
      }
      issue(7, l); issue(8, l);
      poll_ge(pq, ws, FL_X1 + l * 16 + rb, 14, pw);
      if (w != 0) wait_vm<18>();
      bar_lgkm();
      poll_done(pq, pw);
      wait_vm_bar<9>();
      issue(9, l); issue(10, l);
      wait_vm_bar<18>();
      wait_vm_bar<9>(); issue(11, l);
      wait_vm_bar<9>(); issue(12, l);
      wait_vm_bar<9>(); issue(13, l);
      bar();
      if (w == 1 && lane == 0) sig_add(ws, FL_H + l * 16 + rb);
      issue(14, l);
      bar();
      if (w == 1 && lane == 0) sig_add(ws, FL_H + 32 + l * 16 + rb);
      if (active) {
        issue(15, l);
        poll_ge(pq, ws, FL_H + l * 16 + rp, 16, pw);
        if (w != 0) wait_vm<18>();
        bar();
        poll_done(pq, pw);
        bar();
        wait_vm_bar<9>(); issue(16, l);
        poll_ge(pq, ws, FL_H + 32 + l * 16 + rp, 16, pw);
        if (w != 0) wait_vm<9>();
        bar(); issue(17, l);
        poll_done(pq, pw);
        wait_vm_bar<9>(); issue(18, l);
        wait_vm_bar<9>(); issue(19, l);
        wait_vm_bar<9>();
        wait_vm_bar<0>();
        bar();
        bar();
        bar();
        if (l == 0 && w == 1 && lane == 0) sig_add(ws, FL_X2 + rp);
      }
      if (l == 0) { stage_csq(); issue(20, l); issue(21, l); issue(22, l); }
    }
    return;
  }

  { const int l = 0; (void)l; FS(60); }
#pragma unroll 1
  for (int l = 0; l < 2; ++l) {
    u32x4 qf[2];
    {
      IDS();
      bar();
      FS(20);
      constexpr int AH = 10;
      u32x4 af[KSD];
      const unsigned xo = OFF_XA + 2 * l * SZ_XA + tt * (KSD * 1024);
      StatLd sl; stats_issue(sl, rs, OFF_ST + 2 * l * SZ_ST + tt * 3584, lane);
      Stats st;
      static_for<AH>([&](auto I) { constexpr int ks = I; af[ks] = hld(rs, voff + ks * 1024, xo); });
      const LAS char* wk = slot(0); const LAS char* wv = slot(1); const LAS char* wq = slot(2);
      f32x16 ak = {}, av = {};
      constexpr int PF = 3;
      u32x4 rk[PF], rv[PF];
#pragma unroll
      for (int i = 0; i < PF; ++i) { rk[i] = lds_rd(wk, i * 1024); rv[i] = lds_rd(wv, i * 1024); }
      SB();
      FS(21);
      u32x4 rq[4];
      static_for<KSD>([&](auto I) {
        constexpr int ks = I;
        ak = mfma(rk[ks % PF], af[ks], ak); PIN(ak);
        if constexpr (ks + PF < KSD) rk[ks % PF] = lds_rd(wk, (ks + PF) * 1024);
        if constexpr (ks + AH < KSD) af[ks + AH] = hld(rs, voff + (ks + AH) * 1024, xo);
        if constexpr (ks == 20) { st = stats_finish(sl); PIN(st.mean); PIN(st.rs); }
        SB();
        av = mfma(af[ks], rv[ks % PF], av); PIN(av);
        if constexpr (ks + PF < KSD) rv[ks % PF] = lds_rd(wv, (ks + PF) * 1024);
        if constexpr (ks >= KSD - 4) rq[ks - (KSD - 4)] = lds_rd(wq, (ks - (KSD - 4)) * 1024);
        SB();
      });
      FS(22);
      const LAS float* csl = (const LAS float*)(aux + CSQ_L);
      f32x4 ck[4];
#pragma unroll
      for (int g = 0; g < 4; ++g) ck[g] = *(const LAS f32x4*)(csl + 8 * g + 4 * h2);
      const float csv = csl[32 + r];
      LAS float* stl = (LAS float*)(aux + 8 * 4096 + w * 4096);
      if (h2 == 0) stl[r] = st.rs; else stl[32 + r] = st.mean;
      bar();
      FS(23);
      f32x16 aq = {};
      u32x4 ko[2], vo[2];
      f32x4 srs[2], smn[2];
      f32x4 cq[4];
      float e0, e1;
      const unsigned kvo = OFF_KV + l * SZ_KV + (tt * 16 + j) * 4096;
      static_for<KSD>([&](auto I) {
        constexpr int ks = I;
        aq = mfma(rq[ks & 3], af[ks], aq); PIN(aq);
        if constexpr (ks + 4 < KSD) rq[ks & 3] = lds_rd(wq, (ks + 4) * 1024);
        if constexpr (ks == 0) { srs[0] = *(const LAS f32x4*)(stl + 4 * h2); smn[0] = *(const LAS f32x4*)(stl + 32 + 4 * h2); srs[1] = *(const LAS f32x4*)(stl + 8 + 4 * h2); smn[1] = *(const LAS f32x4*)(stl + 40 + 4 * h2); }
        if constexpr (ks >= 1 && ks < 5) {
          constexpr int g = ks - 1;
          float v[4];
#pragma unroll
          for (int e = 0; e < 4; ++e) { v[e] = ak[4 * g + e] - st.mean * ck[g][e]; PIN(v[e]); }
#pragma unroll
          for (int e = 0; e < 4; ++e) { v[e] *= st.rs; PIN(v[e]); }
          ko[g >> 1][(g & 1) * 2] = pk2(v[0], v[1]); ko[g >> 1][(g & 1) * 2 + 1] = pk2(v[2], v[3]);
          PIN(ko[g >> 1]);
        }
        if constexpr (ks == 5) { hst(rs, voff, kvo, ko[0]); hst(rs, voff + 1024, kvo, ko[1]); }
        if constexpr (ks >= 5 && ks < 13) {
          constexpr int q = ks - 5, g = q >> 1, e = (q & 1) * 2;
          if constexpr ((q & 1) == 0) { PIN(srs[g & 1]); PIN(smn[g & 1]); }
          e0 = av[2 * q] - smn[g & 1][e] * csv; e1 = av[2 * q + 1] - smn[g & 1][e + 1] * csv; PIN(e0); PIN(e1);
          e0 *= srs[g & 1][e]; e1 *= srs[g & 1][e + 1]; PIN(e0); PIN(e1);
          const unsigned pv = pk2(e0, e1);
          vo[q >> 2][q & 3] = r == 28 ? 0x3f803f80u : pv;
          PIN(vo[q >> 2]);
          if constexpr (q == 1 || q == 3) {
            srs[g & 1] = *(const LAS f32x4*)(stl + 8 * (g + 2) + 4 * h2); smn[g & 1] = *(const LAS f32x4*)(stl + 32 + 8 * (g + 2) + 4 * h2);
          }
        }
        if constexpr (ks == 13) { hst(rs, voff + 2048, kvo, vo[0]); hst(rs, voff + 3072, kvo, vo[1]); }
        if constexpr (ks == 14) { LAS char* kvl = aux + (8 + w) * 4096 + lane * 16; lds_wr(kvl, 0, ko[0]); lds_wr(kvl, 1024, ko[1]); lds_wr(kvl, 2048, vo[0]); lds_wr(kvl, 3072, vo[1]); }
        if constexpr (ks >= 16 && ks < 20) cq[ks - 16] = *(const LAS f32x4*)(csl + 64 + 8 * (ks - 16) + 4 * h2);
        if constexpr (ks == 25) { wait_vm_bar<0>(); }
        SB();
      });
      FS(24);
      const float qs = st.rs * (0.18898223650461362f * 1.4426950408889634f);
#pragma unroll
      for (int g = 0; g < 4; ++g) {
        float v[4];
#pragma unroll
        for (int e = 0; e < 4; ++e) { v[e] = aq[4 * g + e] - st.mean * cq[g][e]; PIN(v[e]); v[e] *= qs; PIN(v[e]); }
        qf[g >> 1][(g & 1) * 2] = pk2(v[0], v[1]); qf[g >> 1][(g & 1) * 2 + 1] = pk2(v[2], v[3]);
      }
      FS(25);
    }
    {
      IDS();
      const float NEG = -1e30f;
      f32x16 cd, co;
#pragma unroll
      for (int i = 0; i < 16; ++i) { const int key = (i & 3) + 8 * (i >> 2) + 4 * h2; cd[i] = key <= r ? 0.f : NEG; co[i] = key > r ? 0.f : NEG; }
      PIN(cd); PIN(co);
      const LAS char* kw = opq((const LAS char*)aux + lane * 16 + w * 4096);
      bar();
      FS(29);
      float m = NEG;
      f32x16 o = {};
      if (rb >= 2) {
        u32x4 kf[9][2];
#pragma unroll
        for (int it = 0; it < 9; ++it) { kf[it][0] = lds_rd(kw, it * 4096); kf[it][1] = lds_rd(kw, it * 4096 + 1024); }
        SB();
        {
          const f32x16 z = {};
          f32x16 s[3];
          static_for<11>([&](auto IT) {
            constexpr int it = IT;
            if constexpr (it < 9) { s[it % 3] = mfma(kf[it][0], qf[0], it == 0 ? co : it == 8 ? cd : z); s[it % 3] = mfma(kf[it][1], qf[1], s[it % 3]); PIN(s[it % 3]); }
            if constexpr (it >= 2) {
              const f32x16& t = s[(it - 2) % 3];
              float ma = fmaxf(fmaxf(t[0], t[1]), t[2]), mb = fmaxf(fmaxf(t[3], t[4]), t[5]);
              ma = fmaxf(fmaxf(ma, t[6]), t[7]); mb = fmaxf(fmaxf(mb, t[8]), t[9]);
              ma = fmaxf(fmaxf(ma, t[10]), t[11]); mb = fmaxf(fmaxf(mb, t[12]), t[13]);
              ma = fmaxf(fmaxf(ma, t[14]), t[15]);
              m = fmaxf(fmaxf(m, ma), mb);
              PIN(m);
            }
            SB();
          });
        }
        m = -max32(m);
        PIN(m);
        FS(30);
        f32x16 cm;
#pragma unroll
        for (int i = 0; i < 16; ++i) { cm[i] = m; co[i] = co[i] + m; }
        u32x4 vf[2][2];
        vf[0][0] = lds_rd(kw, 2048); vf[0][1] = lds_rd(kw, 3072);
        f32x16 s[3];
        s[0] = mfma(kf[0][0], qf[0], co); s[0] = mfma(kf[0][1], qf[1], s[0]);
        s[1] = mfma(kf[1][0], qf[0], cm); s[1] = mfma(kf[1][1], qf[1], s[1]);
#pragma unroll
        for (int i = 0; i < 16; ++i) cd[i] = cd[i] + m;
        SB();
        static_for<9>([&](auto IT) {
          constexpr int it = IT;
          if constexpr (it < 8) { vf[(it + 1) & 1][0] = lds_rd(kw, (it + 1) * 4096 + 2048); vf[(it + 1) & 1][1] = lds_rd(kw, (it + 1) * 4096 + 3072); }
          u32x4 p[2];
          const f32x16& c = s[it % 3];
          if constexpr (it + 2 < 9) { s[(it + 2) % 3] = mfma(kf[it + 2][0], qf[0], it + 2 == 8 ? cd : cm); }
#pragma unroll
          for (int q = 0; q < 4; ++q) p[0][q] = pk2(__builtin_amdgcn_exp2f(c[2 * q]), __builtin_amdgcn_exp2f(c[2 * q + 1]));
          PIN(p[0]);
          SB();
          if constexpr (it + 2 < 9) { s[(it + 2) % 3] = mfma(kf[it + 2][1], qf[1], s[(it + 2) % 3]); PIN(s[(it + 2) % 3]); }
          o = mfma(vf[it & 1][0], p[0], o);
#pragma unroll
          for (int q = 0; q < 4; ++q) p[1][q] = pk2(__builtin_amdgcn_exp2f(c[8 + 2 * q]), __builtin_amdgcn_exp2f(c[8 + 2 * q + 1]));
          PIN(p[1]);
          SB();
          o = mfma(vf[it & 1][1], p[1], o); PIN(o);
          SB();
        });
      } else {
        const LAS char* kq = kw;
        const f32x16 z = {};
#pragma unroll
        for (int it = 0; it < 9; ++it) {
          if (tt - 8 + it >= 0) {
            f32x16 s = mfma(lds_rd(kq, it * 4096), qf[0], it == 0 ? co : it == 8 ? cd : z);
            s = mfma(lds_rd(kq, it * 4096 + 1024), qf[1], s);
#pragma unroll
            for (int i = 0; i < 16; i += 2) m = fmaxf(fmaxf(m, s[i]), s[i + 1]);
          }
        }
        m = -max32(m);
        f32x16 cm;
#pragma unroll
        for (int i = 0; i < 16; ++i) { cd[i] += m; cm[i] = m; }
#pragma unroll
        for (int it = 0; it < 9; ++it) {
          if (it >= 1 && tt - 8 + it >= 0) {
            f32x16 s = mfma(lds_rd(kq, it * 4096), qf[0], it == 8 ? cd : cm);
            s = mfma(lds_rd(kq, it * 4096 + 1024), qf[1], s);
            u32x4 p[2];
            PACK16(p, pk2(__builtin_amdgcn_exp2f(s[i0]), __builtin_amdgcn_exp2f(s[i1])));
            o = mfma(lds_rd(kq, it * 4096 + 2048), p[0], o);
            o = mfma(lds_rd(kq, it * 4096 + 3072), p[1], o);
          }
        }
      }
      FS(31);
      const unsigned lu = __float_as_uint(o[12]);
      auto rr = __builtin_amdgcn_permlane32_swap(lu, lu, false, false);
      const float inv = __builtin_amdgcn_rcpf(__uint_as_float(rr[1]));
      u32x4 ob[2];
      PACK16(ob, pk2(o[i0] * inv, o[i1] * inv));
      const unsigned oo = OFF_OA + l * SZ_OA + (tt * KSO + 2 * j) * 1024;
      hst(rs, voff, oo, ob[0]); hst(rs, voff + 1024, oo, ob[1]);
      FS(32);
      wait_vm_bar<0>();
      FS(33);
    }
    auto proj_init = [&](int xin) { IDS(); const unsigned xo = OFF_XA + xin * SZ_XA + (ttp * KSD + 4 * fs + 2 * kh) * 1024;
      u32x4 xr[2]; xr[0] = hld(rs, voff, xo); xr[1] = hld(rs, voff + 1024, xo);
      f32x16 a;
#pragma unroll
      for (int q = 0; q < 8; ++q) { const unsigned xv = xr[q >> 2][q & 3]; a[2 * q] = bflo(xv); a[2 * q + 1] = bfhi(xv); }
      PIN(a);
      return a; };
    auto proj_finish = [&](f32x16 own, const f32x16& snd, int xout, bool final_) {
      IDS();
      u32x4 xres[2];
      LAS f32x4* xs = (LAS f32x4*)(aux) + (tt2 * 2 + kh) * 256 + lane;
      LAS f32x4* xrv = (LAS f32x4*)(aux) + (tt2 * 2 + (kh ^ 1)) * 256 + lane;
#pragma unroll
      for (int g = 0; g < 4; ++g) { f32x4 v = {snd[4 * g], snd[4 * g + 1], snd[4 * g + 2], snd[4 * g + 3]}; xs[g * 64] = v; }
      bar_lgkm();
#pragma unroll
      for (int g = 0; g < 4; ++g) { const f32x4 v = xrv[g * 64]; own[4 * g] += v.x; own[4 * g + 1] += v.y; own[4 * g + 2] += v.z; own[4 * g + 3] += v.w; }
#pragma unroll
      for (int q = 0; q < 8; ++q) xres[q >> 2][q & 3] = pk2(own[2 * q], own[2 * q + 1]);
      if (final_) {
        unsigned short* orow = out + (size_t)(ttp * 32 + r) * D_ + 64 * fs + 32 * kh + 4 * h2;
#pragma unroll
        for (int g = 0; g < 4; ++g) { u32x2 v = {xres[g >> 1][(g & 1) * 2], xres[g >> 1][(g & 1) * 2 + 1]}; *(GAS u32x2*)(orow + 8 * g) = v; }
      } else {
        const unsigned xo = OFF_XA + xout * SZ_XA + (ttp * KSD + 4 * fs + 2 * kh) * 1024;
        hst(rs, voff, xo, xres[0]); hst(rs, voff + 1024, xo, xres[1]);
        float s1[4], s2[4];
#pragma unroll
        for (int g = 0; g < 4; ++g) {
          s1[g] = (own[4 * g] + own[4 * g + 1]) + (own[4 * g + 2] + own[4 * g + 3]);
          s2[g] = __builtin_fmaf(own[4 * g], own[4 * g], own[4 * g + 1] * own[4 * g + 1]) + __builtin_fmaf(own[4 * g + 2], own[4 * g + 2], own[4 * g + 3] * own[4 * g + 3]);
        }
        const float t1 = sum32((s1[0] + s1[1]) + (s1[2] + s1[3])), t2 = sum32((s2[0] + s2[1]) + (s2[2] + s2[3]));
        if (h2 == 0) { u32x2 v = {__float_as_uint(t1), __float_as_uint(t2)}; __builtin_amdgcn_raw_buffer_store_b64(v, rs, r * 8, OFF_ST + xout * SZ_ST + (ttp * 14 + 2 * fs + kh) * 256, 16); }
      }
    };
    if (active) {
      IDS();
      f32x16 own = proj_init(2 * l), snd = {};
      bar();
      FS(34);
      u32x4 af[16];
      const unsigned ao = OFF_OA + l * SZ_OA + (ttp * KSO + kh * 16) * 1024;
      constexpr int AH = 10;
      static_for<AH>([&](auto I) { constexpr int i = I; af[i] = hld(rs, voff + i * 1024, ao); });
      const LAS char* wbo[3] = {opq(ring + 3 * CHB + kh * 3072), opq(ring + 0 * CHB + kh * 3072), opq((const LAS char*)aux + WOT_L + lane * 16 + kh * 3072)};
      const LAS char* wbs[3] = {opq(ring + 3 * CHB + kh * 2048 + (kh ^ 1) * 1024), opq(ring + 0 * CHB + kh * 2048 + (kh ^ 1) * 1024), opq((const LAS char*)aux + WOT_L + lane * 16 + kh * 2048 + (kh ^ 1) * 1024)};
      constexpr int PF = 2;
      u32x4 wo_[PF], wsn[PF];
#pragma unroll
      for (int i = 0; i < PF; ++i) { wo_[i] = lds_rd(wbo[0], i * 4096); wsn[i] = lds_rd(wbs[0], i * 4096); }
      SB();
      static_for<16>([&](auto I) {
        constexpr int i = I, n = i + PF;
        own = mfma(wo_[i % PF], af[i], own); PIN(own);
        if constexpr (n < 16) wo_[i % PF] = lds_rd(wbo[n / 7], (n % 7) * 4096);
        if constexpr (i + AH < 16) af[i + AH] = hld(rs, voff + (i + AH) * 1024, ao);
        SB();
        snd = mfma(wsn[i % PF], af[i], snd); PIN(snd);
        if constexpr (n < 16) wsn[i % PF] = lds_rd(wbs[n / 7], (n % 7) * 4096);
        SB();
      });
      FS(35);
      proj_finish(own, snd, 2 * l + 1, false);
      FS(38);
      wait_vm_bar<0>();
      FS(39);
    }
    {
      IDS();
      bar();
      FS(0);
      constexpr int AH = 10;
      u32x4 af[KSD];
      const unsigned xo = OFF_XA + (2 * l + 1) * SZ_XA + tt * (KSD * 1024);
      StatLd sl; stats_issue(sl, rs, OFF_ST + (2 * l + 1) * SZ_ST + tt * 3584, lane);
      Stats st;
      static_for<AH>([&](auto I) { constexpr int ks = I; af[ks] = hld(rs, voff + ks * 1024, xo); });
      const LAS float* csl = (const LAS float*)(aux + CSG_L) + 4 * h2;
      const unsigned ho = OFF_HA + l * SZ_HA + (tt * KSF + 7 * j) * 1024;
      f32x16 prev = {}, acc = {};
      u32x4 wr[4];
      {
        const LAS char* w0 = slot(5); const LAS char* w1 = slot(6);
        constexpr int PF = 3;
        u32x4 r0[PF], r1[PF];
#pragma unroll
        for (int i = 0; i < PF; ++i) { r0[i] = lds_rd(w0, i * 1024); r1[i] = lds_rd(w1, i * 1024); }
        SB();
        FS(2);
        static_for<KSD>([&](auto I) {
          constexpr int ks = I;
          prev = mfma(r0[ks % PF], af[ks], prev); PIN(prev);
          if constexpr (ks + PF < KSD) r0[ks % PF] = lds_rd(w0, (ks + PF) * 1024);
          if constexpr (ks + AH < KSD) af[ks + AH] = hld(rs, voff + (ks + AH) * 1024, xo);
          if constexpr (ks == 20) { st = stats_finish(sl); PIN(st.mean); PIN(st.rs); }
          SB();
          acc = mfma(r1[ks % PF], af[ks], acc); PIN(acc);
          if constexpr (ks + PF < KSD) r1[ks % PF] = lds_rd(w1, (ks + PF) * 1024);
          SB();
        });
      }
      FS(3);
      const float c1 = -1.4426950408889634f * st.rs, vk = __builtin_amdgcn_rcpf(st.rs * st.rs);
      u32x4 ov;
      f32x4 cc[4];
      float ea[8], eb[8], ex[8];
#define GU_STAGE(P, v, S) do { \
        if constexpr ((S) == 0) { ea[v] = P[v] - st.mean * cc[(v) >> 2][(v) & 3]; PIN(ea[v]); eb[v] = P[(v) + 8] - st.mean * cc[2 + ((v) >> 2)][(v) & 3]; PIN(eb[v]); } \
        else if constexpr ((S) == 1) { ex[v] = c1 * ea[v]; PIN(ex[v]); eb[v] = ea[v] * eb[v]; PIN(eb[v]); } \
        else if constexpr ((S) == 2) { ex[v] = __builtin_amdgcn_exp2f(ex[v]); PIN(ex[v]); } \
        else if constexpr ((S) == 3) { ex[v] = __builtin_fmaf(ex[v], vk, vk); PIN(ex[v]); } \
        else if constexpr ((S) == 4) { ex[v] = __builtin_amdgcn_rcpf(ex[v]); PIN(ex[v]); } \
        else { eb[v] = eb[v] * ex[v]; PIN(eb[v]); if constexpr ((v) & 1) { ov[(v) >> 1] = pk2(eb[(v) - 1], eb[v]); PIN(ov); } } } while (0)
#define GU_GAP(P, g, base, sp) static_for<8>([&](auto V_) { constexpr int v_ = V_; constexpr int s_ = (g) - (base) - (sp) * v_; if constexpr (s_ >= 0 && s_ < 6) GU_STAGE(P, v_, s_); })
#define GU_CC(t) _Pragma("unroll") for (int g = 0; g < 4; ++g) cc[g] = *(const LAS f32x4*)(csl + (t) * 32 + 8 * g)
      GU_CC(0);
      bar();
      { const LAS char* w2 = slot(7);
#pragma unroll
        for (int i = 0; i < 4; ++i) wr[i] = lds_rd(w2, i * 1024); }
      SB();
      FS(4);
      f32x16 t1 = acc;
      static_for<5>([&](auto T) {
        constexpr int t = T + 2;
        const LAS char* wl = slot(5 + t);
        const LAS char* wn = slot(6 + t);
        acc = f32x16{};
        static_for<KSD>([&](auto I) {
          constexpr int ks = I;
          if constexpr (ks == 14) {
            if constexpr (t < 6) bar();
            else asm volatile("s_waitcnt vmcnt(0)\n\ts_barrier" ::: "memory");
            SB();
          }
          acc = mfma(wr[ks & 3], af[ks], acc); PIN(acc);
          if constexpr (ks + 4 < KSD) wr[ks & 3] = lds_rd(wl, (ks + 4) * 1024);
          else if constexpr (t < 6) wr[ks & 3] = lds_rd(wn, (ks + 4 - KSD) * 1024);
          if constexpr (t == 2) {
            if constexpr (ks < 14) { GU_GAP(prev, ks, 0, 1); }
            if constexpr (ks == 12) { GU_CC(1); }
            if constexpr (ks == 13) hst(rs, voff, ho, ov);
            if constexpr (ks >= 14) { GU_GAP(t1, ks, 15, 1); }
            if constexpr (ks == 27) { hst(rs, voff + 1024, ho, ov); GU_CC(2); }
          } else {
            GU_GAP(prev, ks, 1, 3);
            if constexpr (ks == 27) { hst(rs, voff + (t - 1) * 1024, ho, ov); GU_CC(t); }
          }
          SB();
        });
        prev = acc;
        FS(3 + t);
      });
      static_for<13>([&](auto G) { constexpr int g = G; GU_GAP(prev, g, 0, 1); });
      hst(rs, voff + 6 * 1024, ho, ov);
      FS(10);
      wait_vm_bar<0>();
      FS(11);
    }
    if (active) {
      IDS();
      f32x16 own = proj_init(2 * l + 1), snd = {};
      bar();
      FS(41);
      constexpr int WIN = DN_WIN;
      u32x4 af[WIN];
      const unsigned ao = OFF_HA + l * SZ_HA + (ttp * KSF + kh * 56) * 1024;
      static_for<12>([&](auto I) { constexpr int i = I; af[i] = hld(rs, voff + kk_of(i) * 1024, ao); });
      const int oo_ = kh * 3072, so_ = kh * 2048 + (kh ^ 1) * 1024;
      constexpr int PF = 2;
      u32x4 wo_[PF], wsn[PF];
      const LAS char* po = opq(ring + 0 * CHB + oo_); const LAS char* ps = opq(ring + 0 * CHB + so_);
#pragma unroll
      for (int i = 0; i < PF; ++i) { wo_[i] = lds_rd(po, i * 4096); wsn[i] = lds_rd(ps, i * 4096); }
      SB();
      FS(42);
      const LAS char* pon = po; const LAS char* psn = ps;
      static_for<56>([&](auto I) {
        constexpr int i = I, c = i / 7, s = i % 7, n = s + PF;
        if constexpr (s == 0) { po = pon; ps = psn; if constexpr (c < 7) { pon = opq(ring + ((13 + c) & 3) * CHB + oo_); psn = opq(ring + ((13 + c) & 3) * CHB + so_); } }
        if constexpr (i == 1) FS(43);
        if constexpr (i == 28) FS(44);
        if constexpr (s == 3) { bar(); SB(); }
        const u32x4 a = af[i % WIN];
        own = mfma(wo_[i % PF], a, own); PIN(own);
        if constexpr (n < 7) wo_[i % PF] = lds_rd(po, n * 4096); else if constexpr (c < 7) wo_[i % PF] = lds_rd(pon, (n - 7) * 4096);
        if constexpr (i < WIN - 12) af[12 + i] = hld(rs, voff + kk_of(12 + i) * 1024, ao);
        SB();
        snd = mfma(wsn[i % PF], a, snd); PIN(snd);
        if constexpr (n < 7) wsn[i % PF] = lds_rd(ps, n * 4096); else if constexpr (c < 7) wsn[i % PF] = lds_rd(psn, (n - 7) * 4096);
        if constexpr (i + WIN < 56) af[i % WIN] = hld(rs, voff + kk_of(i + WIN) * 1024, ao);
        SB();
      });
      FS(45);
      proj_finish(own, snd, 2, l == 1);
      FS(46);
      wait_vm_bar<0>();
      FS(47);
    }
    if (l == 0) FS(61);
  }
}

}

extern "C" size_t stamp_offset() { return OFF_STAMP; }

extern "C" void kernel_launch(void* const* d_in, const int* in_sizes, int n_in, void* d_out, int out_size, void* d_ws, size_t ws_size,
                              hipStream_t stream) {
  const unsigned short* x = (const unsigned short*)d_in[0];
  const unsigned short* wqkv = (const unsigned short*)d_in[1];
  const unsigned short* wo = (const unsigned short*)d_in[2];
  const unsigned short* w1 = (const unsigned short*)d_in[4];
  const unsigned short* w3 = (const unsigned short*)d_in[5];
  const unsigned short* w2 = (const unsigned short*)d_in[6];
  char* ws = (char*)d_ws;
  (void)hipFuncSetAttribute((const void*)k_main, hipFuncAttributeMaxDynamicSharedMemorySize, LDS_BYTES);
  k_prep<<<PREP_BLOCKS, 256, PREP_LDS, stream>>>(x, wqkv, wo, w1, w3, w2, ws);
  k_main<<<256, 512, LDS_BYTES, stream>>>(ws, (unsigned short*)d_out);
}
```

```cpp
#include <hip/hip_runtime.h>
#include <stdint.h>
#include <utility>

typedef __attribute__((ext_vector_type(8))) __bf16 bf16x8;
typedef __attribute__((ext_vector_type(2))) __bf16 bf16x2;
typedef __attribute__((ext_vector_type(2))) float f32x2;
typedef __attribute__((ext_vector_type(4))) float f32x4;
typedef __attribute__((ext_vector_type(16))) float f32x16;
typedef __attribute__((ext_vector_type(4))) unsigned u32x4;
typedef __attribute__((ext_vector_type(2))) unsigned u32x2;
#define DEV __device__ __forceinline__
#define LAS __attribute__((address_space(3)))
#define GAS __attribute__((address_space(1)))
typedef GAS unsigned gu32;
#define RLX __ATOMIC_RELAXED, __HIP_MEMORY_SCOPE_AGENT

namespace {

constexpr int D_ = 448, F_ = 1792;
constexpr int NTT = 64;
constexpr int KSD = 28;
constexpr int KSO = 32;
constexpr int KSF = 112;
constexpr int CHB = 28672;

constexpr unsigned OFF_FLAG = 0;
constexpr int FL_KV = 0;
constexpr int FL_O = FL_KV + 1024;
constexpr int FL_X1 = FL_O + 512;
constexpr int FL_H = FL_X1 + 512;
constexpr int FL_X2 = FL_H + 1024;
constexpr int FL_TMO = FL_X2 + 256;
constexpr int NFLAG = FL_TMO + 1;
constexpr unsigned OFF_WQKV = 448 * 1024;
constexpr unsigned OFF_WO   = OFF_WQKV + 16 * 3 * CHB;
constexpr unsigned OFF_WGU  = OFF_WO + 7 * 64 * 1024;
constexpr unsigned OFF_WDN  = OFF_WGU + 16 * 7 * CHB;
constexpr unsigned OFF_CSQ  = OFF_WDN + 7 * 224 * 1024;
constexpr unsigned OFF_CSG  = OFF_CSQ + 16 * 3 * 32 * 4;
constexpr unsigned SZ_XA    = NTT * KSD * 1024;
constexpr unsigned OFF_XA   = OFF_CSG + 16 * 7 * 32 * 4;
constexpr unsigned SZ_ST    = NTT * 14 * 32 * 8;
constexpr unsigned OFF_ST   = OFF_XA + 4 * SZ_XA;
constexpr unsigned SZ_KV    = NTT * 16 * 4096;
constexpr unsigned OFF_KV   = OFF_ST + 4 * SZ_ST;
constexpr unsigned SZ_OA    = NTT * KSO * 1024;
constexpr unsigned OFF_OA   = OFF_KV + 2 * SZ_KV;
#define HPAD 0
constexpr unsigned HSTR = KSF * 1024 + HPAD;
constexpr unsigned SZ_HA    = NTT * HSTR;
constexpr unsigned OFF_HA   = OFF_OA + 2 * SZ_OA;
constexpr unsigned WS_END   = OFF_HA + 2 * SZ_HA;
constexpr unsigned OFF_STAMP = (WS_END + 4095) / 4096 * 4096;
static_assert(NFLAG * 128 <= OFF_WQKV, "flags");
static_assert(OFF_XA % 128 == 0 && OFF_ST % 128 == 0 && OFF_KV % 128 == 0 && OFF_OA % 128 == 0 && OFF_HA % 128 == 0, "lines");

#define STAMP(i) do {} while (0)
#define FS(i) do {} while (0)

template <class F, int... I> DEV void static_for_impl(F&& f, std::integer_sequence<int, I...>) { (f(std::integral_constant<int, I>{}), ...); }
template <int N, class F> DEV void static_for(F&& f) { static_for_impl(f, std::make_integer_sequence<int, N>{}); }

DEV int lane_id() { int l; asm volatile("v_mbcnt_lo_u32_b32 %0, -1, 0\n\tv_mbcnt_hi_u32_b32 %0, -1, %0" : "=v"(l)); return l; }
DEV unsigned pk2(float a, float b) { f32x2 v = {a, b}; return __builtin_bit_cast(unsigned, __builtin_convertvector(v, bf16x2)); }
DEV f32x16 mfma(u32x4 a, u32x4 b, f32x16 c) {
  return __builtin_amdgcn_mfma_f32_32x32x16_bf16(__builtin_bit_cast(bf16x8, a), __builtin_bit_cast(bf16x8, b), c, 0, 0, 0);
}
DEV float bflo(unsigned u) { return __uint_as_float(u << 16); }
DEV float bfhi(unsigned u) { return __uint_as_float(u & 0xffff0000u); }
DEV float swap32(float v) {
  const unsigned u = __float_as_uint(v);
  auto rr = __builtin_amdgcn_permlane32_swap(u, u, false, false);
  return __uint_as_float((threadIdx.x & 32) ? rr[0] : rr[1]);
}
DEV float sum32(float v) {
  const unsigned u = __float_as_uint(v);
  auto rr = __builtin_amdgcn_permlane32_swap(u, u, false, false);
  return __uint_as_float(rr[0]) + __uint_as_float(rr[1]);
}
DEV float max32(float v) {
  const unsigned u = __float_as_uint(v);
  auto rr = __builtin_amdgcn_permlane32_swap(u, u, false, false);
  return fmaxf(__uint_as_float(rr[0]), __uint_as_float(rr[1]));
}
DEV void dma7(const char* g, unsigned voff, unsigned lds) {
  unsigned keep;
  const char* g2 = g + 4096;
  lds = __builtin_amdgcn_readfirstlane(lds);
  asm volatile("s_mov_b32 %0, m0\n\ts_mov_b32 m0, %2\n\ts_nop 0\n\t"
               "global_load_lds_dwordx4 %1, %3 offset:0\n\t"
               "global_load_lds_dwordx4 %1, %3 offset:1024\n\t"
               "global_load_lds_dwordx4 %1, %3 offset:2048\n\t"
               "global_load_lds_dwordx4 %1, %3 offset:3072\n\t"
               "s_add_u32 m0, m0, 0x1000\n\ts_nop 0\n\t"
               "global_load_lds_dwordx4 %1, %4 offset:0\n\t"
               "global_load_lds_dwordx4 %1, %4 offset:1024\n\t"
               "global_load_lds_dwordx4 %1, %4 offset:2048\n\t"
               "s_mov_b32 m0, %0"
               : "=&s"(keep) : "v"(voff), "s"(lds), "s"(g), "s"(g2) : "memory", "scc");
}
template <int N> DEV void wait_vm() { asm volatile("s_waitcnt vmcnt(%0)" ::"n"(N) : "memory"); }
template <int N> DEV void wait_vm_bar() { asm volatile("s_waitcnt vmcnt(%0) lgkmcnt(0)\n\ts_barrier" ::"n"(N) : "memory"); }
DEV void bar_lgkm() { asm volatile("s_waitcnt lgkmcnt(0)\n\ts_barrier" ::: "memory"); }
DEV u32x4 lds_rd(const LAS char* base, int off) { return *(const LAS u32x4*)(base + off); }
DEV void lds_wr(LAS char* base, int off, u32x4 v) { *(LAS u32x4*)(base + off) = v; }
DEV u32x4 gld(const char* p) { return *(const GAS u32x4*)(p); }
DEV void gst(char* p, u32x4 v) { *(GAS u32x4*)(p) = v; }
typedef __amdgpu_buffer_rsrc_t rsrc_t;
DEV u32x4 hld(rsrc_t rs, unsigned voff, unsigned soff) { return __builtin_amdgcn_raw_buffer_load_b128(rs, voff, soff, 16); }
DEV void hst(rsrc_t rs, unsigned voff, unsigned soff, u32x4 v) { __builtin_amdgcn_raw_buffer_store_b128(v, rs, voff, soff, 16); }

struct Poll { unsigned v0, v1, v2, v3; };
DEV void poll_ge(Poll& q, char* ws, int flag, unsigned target, int on) {
  const char* c = ws + OFF_FLAG + (size_t)flag * 128;
  unsigned cnt, tmp;
  on = __builtin_amdgcn_readfirstlane(on);
  asm volatile(
      "s_mov_b32 %4, 0\n\t"
      "s_cmp_eq_u32 %9, 0\n\t"
      "s_cbranch_scc1 2f\n"
      "1:\n\t"
      "global_load_dword %0, %6, %7 sc1\n\t"
      "s_waitcnt vmcnt(0)\n\tv_readfirstlane_b32 %5, %0\n\ts_nop 0\n\ts_cmp_ge_u32 %5, %8\n\ts_cbranch_scc1 2f\n\ts_sleep 1\n\t"
      "s_add_u32 %4, %4, 1\n\ts_cmp_lt_u32 %4, 0x8000\n\ts_cbranch_scc1 1b\n"
      "2:"
      : "+v"(q.v0), "+v"(q.v1), "+v"(q.v2), "+v"(q.v3), "=&s"(cnt), "=&s"(tmp)
      : "v"(0u), "s"(c), "s"(target), "s"(on)
      : "memory", "scc");
  if (cnt >= 0x8000u && lane_id() == 0) __hip_atomic_store((gu32*)(ws + OFF_FLAG + (size_t)FL_TMO * 128), 1u, RLX);
}
DEV void poll_two(char* ws, int fa, int fb) {
  const char* c = ws + OFF_FLAG + (size_t)fa * 128;
  const unsigned vo = (lane_id() & 1) * (unsigned)((fb - fa) * 128);
  unsigned cnt, v;
  asm volatile(
      "s_mov_b32 %1, 0\n"
      "1:\n\t"
      "global_load_dword %0, %2, %3 sc1\n\t"
      "s_waitcnt vmcnt(0)\n\tv_cmp_eq_u32 vcc, 0, %0\n\ts_nop 1\n\ts_cbranch_vccz 2f\n\ts_sleep 1\n\t"
      "s_add_u32 %1, %1, 1\n\ts_cmp_lt_u32 %1, 0x8000\n\ts_cbranch_scc1 1b\n"
      "2:"
      : "=&v"(v), "=&s"(cnt) : "v"(vo), "s"(c) : "memory", "scc", "vcc");
  if (cnt >= 0x8000u && lane_id() == 0) __hip_atomic_store((gu32*)(ws + OFF_FLAG + (size_t)FL_TMO * 128), 1u, RLX);
}
DEV void poll_done(Poll& q, int on) {
  on = __builtin_amdgcn_readfirstlane(on);
  asm volatile("s_cmp_eq_u32 %4, 0\n\ts_cbranch_scc1 3f\n\ts_waitcnt vmcnt(0)\n3:" : "+v"(q.v0), "+v"(q.v1), "+v"(q.v2), "+v"(q.v3) : "s"(on) : "scc");
}
DEV void sig_add16(char* ws, int flag, int lane) { if (lane < 16) (void)__hip_atomic_fetch_add((gu32*)(ws + OFF_FLAG + (size_t)(flag + lane) * 128), 1u, RLX); }
DEV void sig_add(char* ws, int flag) { (void)__hip_atomic_fetch_add((gu32*)(ws + OFF_FLAG + (size_t)flag * 128), 1u, RLX); }

struct Stats { float mean, rs; };
DEV Stats load_stats(rsrc_t rs, unsigned off_st, int tt, int lane) {
  const int r = lane & 31, h2 = lane >> 5;
  float s1 = 0.f, s2 = 0.f;
  u32x2 v[7];
#pragma unroll
  for (int i = 0; i < 7; ++i) v[i] = __builtin_amdgcn_raw_buffer_load_b64(rs, h2 * 1792 + r * 8 + i * 256, off_st + tt * 3584, 16);
#pragma unroll
  for (int i = 0; i < 7; ++i) { s1 += __uint_as_float(v[i].x); s2 += __uint_as_float(v[i].y); }
  s1 = sum32(s1); s2 = sum32(s2);
  Stats s; s.mean = s1 * (1.0f / D_);
  const float var = fmaxf(s2 * (1.0f / D_) - s.mean * s.mean, 0.f);
  s.rs = __builtin_amdgcn_rsqf(var + 1e-5f);
  return s;
}
constexpr int G1T = 5, G1S = 8 * G1T;
__host__ __device__ constexpr int kk_of(int i) { return i < G1S ? 7 * (i / G1T) + i % G1T : 7 * ((i - G1S) / (7 - G1T)) + G1T + (i - G1S) % (7 - G1T); }

#define ROTX 0
__host__ __device__ constexpr int rot_x(int j) { return ROTX ? (7 * (j & 7)) >> 1 : 0; }
typedef short s16x4 __attribute__((ext_vector_type(4)));
DEV u32x2 tr_read(const LAS char* p) { return __builtin_bit_cast(u32x2, __builtin_amdgcn_ds_read_tr16_b64_v4i16((LAS s16x4*)p)); }
template <int KIND>
DEV void prep_unit(const unsigned short* __restrict__ src, const unsigned short* __restrict__ src2, int ld, int col, char* __restrict__ dst, float* cs, int a1, int a2, char* smem) {
  const int tid = threadIdx.x, lane = tid & 63, w = tid >> 6, r = lane & 31, h2 = lane >> 5;
  LAS char* img = (LAS char*)smem;
  if (KIND == 0) {
    u32x2 v[14];
    const int p = tid & 7, row0 = tid >> 3;
#pragma unroll
    for (int n = 0; n < 14; ++n) v[n] = p < 7 ? *(const GAS u32x2*)(src + (size_t)(row0 + 32 * n) * ld + col + 4 * p) : u32x2{0u, 0u};
#pragma unroll
    for (int n = 0; n < 14; ++n) *(LAS u32x2*)(img + (row0 + 32 * n) * 64 + p * 8) = v[n];
  } else {
    u32x4 v[7];
    const int p = tid & 3, row0 = tid >> 2;
    const unsigned short* sp = KIND == 1 ? (p < 2 ? src + col + 8 * p : src2 + col + 8 * (p - 2)) : src + col + 8 * p;
#pragma unroll
    for (int n = 0; n < 7; ++n) v[n] = *(const GAS u32x4*)(sp + (size_t)((KIND == 3 ? 448 * (a2 & 3) : 0) + row0 + 64 * n) * ld);
#pragma unroll
    for (int n = 0; n < 7; ++n) {
      const int row = row0 + 64 * n;
      const int irow = KIND == 2 ? 32 * (row / 28) + row % 28 : row;
      *(LAS u32x4*)(img + irow * 64 + p * 16) = v[n];
    }
    if (KIND == 2) { const int head = tid >> 4, d = 28 + ((tid >> 2) & 3); *(LAS u32x4*)(img + (32 * head + d) * 64 + p * 16) = u32x4{0u, 0u, 0u, 0u}; }
  }
  __syncthreads();
  constexpr int NKS = KIND == 2 ? 32 : 28;
  const LAS char* rd = img + (4 * h2 + ((lane & 15) >> 2)) * 64 + ((lane >> 4) & 1) * 32 + (lane & 3) * 8;
  float sum = 0.f;
#pragma unroll
  for (int n = 0; n < NKS / 4; ++n) {
    const int i = w + 4 * n;
    const u32x2 lo = tr_read(rd + i * 1024), hi = tr_read(rd + i * 1024 + 512);
    const u32x4 v = {lo.x, lo.y, hi.x, hi.y};
    size_t doff;
    if (KIND <= 1) doff = (size_t)((i + 28 - a2) % 28) * 1024;
    else if (KIND == 2) doff = (size_t)((((i - 2 * a2) & 15) * 2 + (i >> 4)) * 2 + a1) * 1024;
    else {
      const int ks = 28 * (a2 & 3) + i, kh = ks / 56, kk = ks % 56, j8 = (kk / 7 - (a2 >> 2)) & 7, t = kk % 7;
      const int step = t < G1T ? G1T * j8 + t : G1S + (7 - G1T) * j8 + (t - G1T);
      doff = (size_t)((step * 2 + kh) * 2 + a1) * 1024;
    }
    gst(dst + doff + lane * 16, v);
    if (KIND <= 1) {
#pragma unroll
      for (int q = 0; q < 4; ++q) sum += bflo(v[q]) + bfhi(v[q]);
    }
  }
  if (KIND <= 1) {
    sum = sum32(sum);
    __syncthreads();
    LAS float* red = (LAS float*)img;
    if (h2 == 0) red[w * 32 + r] = sum;
    __syncthreads();
    if (tid < 32) cs[tid] = (red[tid] + red[32 + tid]) + (red[64 + tid] + red[96 + tid]);
  }
}
constexpr int PREP_LDS = 32768;
constexpr int PREP_BLOCKS = 8 * 38;
__global__ __launch_bounds__(256) void k_prep(const unsigned short* __restrict__ x, const unsigned short* __restrict__ wqkv,
                                              const unsigned short* __restrict__ wo, const unsigned short* __restrict__ w1,
                                              const unsigned short* __restrict__ w3, const unsigned short* __restrict__ w2, char* __restrict__ ws) {
  extern __shared__ __attribute__((aligned(16))) char smem[];
  const int b = blockIdx.x, tid = threadIdx.x, lane = tid & 63, w = tid >> 6, r = lane & 31, h2 = lane >> 5;
  const int xg = b & 7, k = b >> 3;
  if (k >= 30) {
    const int tt = xg * 8 + (k - 30);
    if (tt < 14) { const int f = tt * 256 + tid; if (f < NFLAG) *(GAS unsigned*)(ws + OFF_FLAG + (size_t)f * 128) = 0u; }
    LAS char* img = (LAS char*)smem;
    u32x4 v[7];
#pragma unroll
    for (int n = 0; n < 7; ++n) { const int c = tid + 256 * n; v[n] = *(const GAS u32x4*)(x + (size_t)tt * 32 * D_ + c * 8); }
#pragma unroll
    for (int n = 0; n < 7; ++n) { const int c = tid + 256 * n; *(LAS u32x4*)(img + (c / 56) * 912 + (c % 56) * 16) = v[n]; }
    __syncthreads();
    for (int ft = w; ft < 14; ft += 4) {
      float s1 = 0.f, s2 = 0.f;
#pragma unroll
      for (int s = 0; s < 2; ++s) {
        const int ks = 2 * ft + s;
        const u32x2 a = *(const LAS u32x2*)(img + r * 912 + ks * 32 + 8 * h2), c = *(const LAS u32x2*)(img + r * 912 + ks * 32 + 16 + 8 * h2);
        const u32x4 o = {a.x, a.y, c.x, c.y};
        gst(ws + OFF_XA + ((size_t)tt * KSD + ks) * 1024 + lane * 16, o);
#pragma unroll
        for (int q = 0; q < 4; ++q) { const float lo = bflo(o[q]), hi = bfhi(o[q]); s1 += lo + hi; s2 += lo * lo + hi * hi; }
      }
      s1 = sum32(s1); s2 = sum32(s2);
      if (h2 == 0) { f32x2 o = {s1, s2}; *(GAS f32x2*)(ws + OFF_ST + ((size_t)tt * 14 + ft) * 256 + r * 8) = o; }
    }
    return;
  }
  if (k < 14) {
    const int u = xg * 14 + k, j = u / 7, t = u % 7;
    prep_unit<1>(w1, w3, F_, 112 * j + 16 * t, ws + OFF_WGU + (size_t)u * CHB, (float*)(ws + OFF_CSG) + u * 32, 0, rot_x(j), smem);
  } else if (k < 20) {
    const int u = xg * 6 + (k - 14), j = u / 3, p = u % 3;
    prep_unit<0>(wqkv, nullptr, 3 * D_, (p == 2 ? 0 : p == 0 ? D_ : 2 * D_) + 28 * j, ws + OFF_WQKV + (size_t)u * CHB, (float*)(ws + OFF_CSQ) + u * 32, 0, rot_x(j), smem);
  } else if (xg < 7) {
    if (k < 28) { const int fs = xg, tile = (k - 20) & 1, q = (k - 20) >> 1;
      prep_unit<3>(w2, nullptr, D_, 64 * fs + 32 * tile, ws + OFF_WDN + (size_t)fs * 224 * 1024, nullptr, tile, q + 4 * fs, smem);
    } else { const int fs = xg, tile = k - 28;
      prep_unit<2>(wo, nullptr, D_, 64 * fs + 32 * tile, ws + OFF_WO + (size_t)fs * 64 * 1024, nullptr, tile, fs, smem);
    }
  }
}

constexpr int AUX = 4 * CHB;
constexpr int LDS_BYTES = AUX + 49152;
#define SB() __builtin_amdgcn_sched_barrier(0)
#define PIN(x) asm volatile("" : "+v"(x))
#define PACK16(o, expr) _Pragma("unroll") for (int q_ = 0; q_ < 8; ++q_) { const int i0 = 2 * q_, i1 = 2 * q_ + 1; (void)i0; (void)i1; o[q_ >> 2][q_ & 3] = expr; }
template <class T> DEV void opaque_s(T& v) { asm volatile("" : "+s"(v)); }

struct StatLd { u32x2 v[7]; };
DEV void stats_issue(StatLd& s, rsrc_t rs, unsigned soff, int lane) {
  const unsigned vo = (lane >> 5) * 1792 + (lane & 31) * 8;
#pragma unroll
  for (int i = 0; i < 7; ++i) s.v[i] = __builtin_amdgcn_raw_buffer_load_b64(rs, vo + i * 256, soff, 16);
}
DEV Stats stats_finish(const StatLd& s) {
  float s1 = 0.f, s2 = 0.f;
#pragma unroll
  for (int i = 0; i < 7; ++i) { s1 += __uint_as_float(s.v[i].x); s2 += __uint_as_float(s.v[i].y); }
  s1 = sum32(s1); s2 = sum32(s2);
  Stats o; o.mean = s1 * (1.0f / D_);
  const float var = fmaxf(s2 * (1.0f / D_) - o.mean * o.mean, 0.f);
  o.rs = __builtin_amdgcn_rsqf(var + 1e-5f);
  return o;
}

template <int N> DEV void dma_n(rsrc_t rs, unsigned voff, unsigned lds, unsigned soff) {
  if constexpr (N == 4)
    asm volatile("s_mov_b32 m0, %0\n\ts_nop 0\n\tbuffer_load_dwordx4 %1, %2, %3 offen lds\n\tbuffer_load_dwordx4 %1, %2, %3 offen offset:1024 lds\n\t"
                 "buffer_load_dwordx4 %1, %2, %3 offen offset:2048 lds\n\tbuffer_load_dwordx4 %1, %2, %3 offen offset:3072 lds" ::"s"(lds), "v"(voff), "s"(rs), "s"(soff) : "memory");
  else if constexpr (N == 3)
    asm volatile("s_mov_b32 m0, %0\n\ts_nop 0\n\tbuffer_load_dwordx4 %1, %2, %3 offen lds\n\tbuffer_load_dwordx4 %1, %2, %3 offen offset:1024 lds\n\t"
                 "buffer_load_dwordx4 %1, %2, %3 offen offset:2048 lds" ::"s"(lds), "v"(voff), "s"(rs), "s"(soff) : "memory");
  else
    asm volatile("s_mov_b32 m0, %0\n\ts_nop 0\n\tbuffer_load_dwordx4 %1, %2, %3 offen lds\n\tbuffer_load_dwordx4 %1, %2, %3 offen offset:1024 lds" ::"s"(lds), "v"(voff), "s"(rs), "s"(soff) : "memory");
}
DEV void dma_chunk(rsrc_t rs, unsigned voff, unsigned lds, unsigned soff, int w) {
  const unsigned o = (w - 1) * 9216;
  dma_n<4>(rs, voff, lds + o, soff + o); dma_n<4>(rs, voff, lds + o + 4096, soff + o + 4096);
  if (w == 3) dma_n<2>(rs, voff, lds + o + 8192, soff + o + 8192);
  else asm volatile("s_mov_b32 m0, %0\n\ts_nop 0\n\tbuffer_load_dwordx4 %1, %2, %3 offen lds" ::"s"(lds + o + 8192), "v"(voff), "s"(rs), "s"(soff + o + 8192) : "memory");
}
DEV void dma2(rsrc_t rs, unsigned voff, unsigned lds, unsigned soff) {
  asm volatile("s_mov_b32 m0, %0\n\ts_nop 0\n\t"
               "buffer_load_dwordx4 %1, %2, %3 offen lds\n\t"
               "buffer_load_dwordx4 %1, %2, %3 offen offset:1024 lds"
               ::"s"(lds), "v"(voff), "s"(rs), "s"(soff) : "memory");
}
DEV void bar() { asm volatile("s_barrier" ::: "memory"); }
DEV const LAS char* opq(const LAS char* p) { asm volatile("" : "+v"(p)); return p; }

#define DNW 14
constexpr int DN_WIN = DNW;

__global__ __launch_bounds__(512, 2) void k_main(char* __restrict__ ws, unsigned short* __restrict__ out) {
  extern __shared__ __attribute__((aligned(16))) char smem[];
  const int w8_ = __builtin_amdgcn_readfirstlane(threadIdx.x >> 6);
  const bool helper = w8_ >= 4;
  const int w_ = w8_ & 3;
  const int b = blockIdx.x, idx = b >> 3;
#define MAPA 1
  const int x_ = b & 7;
  const int rb_ = MAPA == 0 ? 2 * x_ + (idx >> 4) : MAPA == 1 ? 4 * (x_ & 3) + (idx >> 3) : 8 * (x_ & 1) + (idx >> 2);
  const int j_ = MAPA == 0 ? (idx & 15) : MAPA == 1 ? 8 * (x_ >> 2) + (idx & 7) : 4 * (x_ >> 1) + (idx & 3);
  const int rp_ = 2 * x_ + (idx >> 4), m_ = idx & 15;
  const bool active = m_ < 14;
  const unsigned lds0 = __builtin_amdgcn_readfirstlane((unsigned)(uintptr_t)smem);
  const rsrc_t rs = __builtin_amdgcn_make_buffer_rsrc(ws, 0, WS_END, 0x00020000);
#define IDS() \
  int w = w_, rb = rb_, j = j_, rp = rp_, mm = m_; asm volatile("" : "+s"(w), "+s"(rb), "+s"(j), "+s"(rp), "+s"(mm)); \
  const int lane = lane_id(), r = lane & 31, h2 = lane >> 5; \
  const bool t0 = w == 0 && lane == 0; (void)t0; \
  const int th = mm >= 7 ? 1 : 0, fs = mm - 7 * th, tt2 = w & 1, kh = w >> 1; \
  const int tt = 4 * rb + w, ttp = 4 * rp + 2 * th + tt2; \
  const unsigned voff = lane * 16; \
  const LAS char* ring = (const LAS char*)smem + lane * 16; \
  LAS char* aux = (LAS char*)smem + AUX; \
  (void)r; (void)h2; (void)fs; (void)kh; (void)tt; (void)ttp; (void)voff; (void)ring; (void)aux; (void)tt2; (void)rp;
#define slot(c) opq(ring + ((c) & 3) * CHB)
#define XLD(p) hld(rs, voff + (p) * 1024, (p) + rot < KSD ? xa : xb)
  constexpr int CSQ_L = 0;
  constexpr int CSG_L = 16384;
  constexpr int WOT_L = 24576;

  if (helper) {
    auto issue = [&](int c, int l) {
      const int sl = c & 3;
      if (c >= 20) { c -= 20; ++l; }
      const bool proj = (c >= 3 && c < 5) || c >= 12;
      int w = w_, j = j_, mm = m_; asm volatile("" : "+s"(w), "+s"(j), "+s"(mm));
      const int fs = mm >= 7 ? mm - 7 : mm;
      unsigned src;
      if (c < 3) src = OFF_WQKV + (j * 3 + c) * CHB;
      else if (c < 5) src = OFF_WO + fs * 65536 + (c - 3) * CHB;
      else if (c < 12) src = OFF_WGU + (j * 7 + c - 5) * CHB;
      else src = OFF_WDN + fs * 229376 + (c - 12) * CHB;
      if (!(l < 2 && (active || !proj))) src = WS_END;
      if (w != 0) dma_chunk(rs, lane_id() * 16, lds0 + sl * CHB, src, w);
    };
    auto stage_csq = [&]() { IDS(); if (w == 3 && lane < 24) *(LAS u32x4*)(aux + CSQ_L + lane * 16) = gld(ws + OFF_CSQ + j * 384 + lane * 16); };
    stage_csq();
    issue(0, 0); issue(1, 0); issue(2, 0);
    Poll pq = {0u, 0u, 0u, 0u};
    u32x4 hd[4][10];
#pragma unroll
    for (int c = 0; c < 4; ++c)
#pragma unroll
      for (int p = 0; p < 10; ++p) hd[c][p] = u32x4{0u, 0u, 0u, 0u};
    auto hold = [&](auto C) {
      constexpr int c = C;
      int w = w_, mm = m_; asm volatile("" : "+s"(w), "+s"(mm));
      if (w == 0 || !active) return;
      const int fs = mm >= 7 ? mm - 7 : mm;
      const char* src = ws + OFF_WDN + (size_t)fs * 229376 + (4 + c) * CHB + (w - 1) * 9216 + lane_id() * 16;
#pragma unroll
      for (int p = 0; p < 9; ++p) hd[c][p] = gld(src + p * 1024);
      if (w == 3) hd[c][9] = gld(src + 9 * 1024);
      SB();
    };
    auto drop = [&](auto C) {
      constexpr int c = C;
      int w = w_; asm volatile("" : "+s"(w));
      if (w == 0) return;
      LAS char* dst = (LAS char*)smem + (c & 3) * CHB + (w - 1) * 9216 + lane_id() * 16;
#pragma unroll
      for (int p = 0; p < 9; ++p) lds_wr(dst, p * 1024, hd[c][p]);
      if (w == 3) lds_wr(dst, 9 * 1024, hd[c][9]);
    };
    using I0 = std::integral_constant<int, 0>; using I1 = std::integral_constant<int, 1>; using I2 = std::integral_constant<int, 2>; using I3 = std::integral_constant<int, 3>;
#pragma unroll 1
    for (int l = 0; l < 2; ++l) {
      IDS();
      const int pw = w == 0 ? 1 : 0;
      poll_ge(pq, ws, FL_X2 + rb * 16 + j, 14, l == 1 ? pw : 0);
      if (w != 0) wait_vm<0>();
      bar_lgkm();
      poll_done(pq, pw);
      bar();
      bar();
      if (w == 1 && lane < 2) sig_add(ws, FL_KV + ((l * 16 + rb) * 16 + j) * 2 + lane);
      if (rb >= 1 && w == 0) poll_two(ws, FL_KV + (rb >= 2 ? ((l * 16 + rb - 2) * 16 + j) * 2 + 1 : ((l * 16 + rb - 1) * 16 + j) * 2), FL_KV + ((l * 16 + rb - 1) * 16 + j) * 2);
      bar();
      if (rb >= 1) {
        u32x4 hv[8];
#pragma unroll
        for (int n = 0; n < 2; ++n) {
          const int rel = 2 * w + n, kt = max(4 * rb - 8 + rel, 0);
#pragma unroll
          for (int q = 0; q < 4; ++q) hv[n * 4 + q] = hld(rs, voff + q * 1024, OFF_KV + l * SZ_KV + (kt * 16 + j) * 4096);
        }
        SB();
#pragma unroll
        for (int n = 0; n < 8; ++n) lds_wr(aux + lane * 16, (2 * w) * 4096 + n * 1024, hv[n]);
      }
      bar_lgkm();
      issue(3, l); issue(4, l); issue(5, l); issue(6, l);
      bar();
      if (w == 1) sig_add16(ws, FL_O + (l * 16 + rb) * 16, lane);
      if (w == 3 && lane < 56) *(LAS u32x4*)(aux + CSG_L + lane * 16) = gld(ws + OFF_CSG + j * 896 + lane * 16);
      if (active) {
        if (w == 1 || w == 2) dma_n<4>(rs, voff, lds0 + AUX + WOT_L + (w - 1) * 4096, OFF_WO + fs * 65536 + 2 * CHB + (w - 1) * 4096);
        poll_ge(pq, ws, FL_O + (l * 16 + rp) * 16 + mm, 16, pw);
        if (w != 0) wait_vm<0>();
        bar_lgkm();
        poll_done(pq, pw);
        bar();
        bar();
        if (w == 1) sig_add16(ws, FL_X1 + (l * 16 + rp) * 16, lane);
      }
      issue(7, l); issue(8, l);
      poll_ge(pq, ws, FL_X1 + (l * 16 + rb) * 16 + j, 14, pw);
      if (w != 0) wait_vm<18>();
      bar_lgkm();
      poll_done(pq, pw);
      wait_vm_bar<9>();
      issue(9, l); issue(10, l);
      wait_vm_bar<18>();
      const bool first = l == 0 && active;
      wait_vm_bar<9>(); issue(11, l);
      if (l == 0) hold(I0{});
      if (first) wait_vm<18>(); else wait_vm<9>();
      bar(); issue(12, l);
      if (l == 0) hold(I1{});
      if (first) wait_vm<27>(); else wait_vm<9>();
      bar(); issue(13, l);
      if (l == 0) hold(I2{});
      bar();
      if (w == 1) sig_add16(ws, FL_H + (l * 16 + rb) * 16, lane);
      issue(14, l);
      if (l == 0) hold(I3{});
      bar();
      if (w == 1) sig_add16(ws, FL_H + 512 + (l * 16 + rb) * 16, lane);
      if (active) {
        issue(15, l);
        poll_ge(pq, ws, FL_H + (l * 16 + rp) * 16 + mm, 16, pw);
        if (first) wait_vm<36>(); else wait_vm<18>();
        bar();
        poll_done(pq, pw);
        bar();
        if (first) wait_vm<18>(); else wait_vm<9>();
        bar();
        drop(I0{});
        poll_ge(pq, ws, FL_H + 512 + (l * 16 + rp) * 16 + mm, 16, pw);
        if (w != 0) wait_vm<0>();
        bar_lgkm();
        poll_done(pq, pw);
        drop(I1{});
        bar_lgkm();
        drop(I2{});
        bar_lgkm();
        drop(I3{});
        bar_lgkm();
        issue(20, l);
        bar_lgkm();
        issue(21, l);
        bar();
        issue(22, l);
        bar();
        bar();
        if (l == 0 && w == 1) sig_add16(ws, FL_X2 + rp * 16, lane);
        if (l == 0) stage_csq();
      } else if (l == 0) { stage_csq(); issue(20, l); issue(21, l); issue(22, l); }
    }
    return;
  }

  { const int l = 0; (void)l; FS(60); }
#pragma unroll 1
  for (int l = 0; l < 2; ++l) {
    u32x4 qf[2];
    {
      IDS();
      bar();
      FS(20);
      constexpr int AH = 10;
      u32x4 af[KSD];
      const unsigned xo = OFF_XA + 2 * l * SZ_XA + tt * (KSD * 1024);
      const int rot = rot_x(j);
      const unsigned xa = xo + rot * 1024, xb = xa - KSD * 1024;
      StatLd sl; stats_issue(sl, rs, OFF_ST + 2 * l * SZ_ST + tt * 3584, lane);
      Stats st;
      static_for<AH>([&](auto I) { constexpr int ks = I; af[ks] = XLD(ks); });
      const LAS char* wk = slot(0); const LAS char* wv = slot(1); const LAS char* wq = slot(2);
      f32x16 ak = {}, av = {};
      constexpr int PF = 3;
      u32x4 rk[PF], rv[PF];
#pragma unroll
      for (int i = 0; i < PF; ++i) { rk[i] = lds_rd(wk, i * 1024); rv[i] = lds_rd(wv, i * 1024); }
      SB();
      FS(21);
      u32x4 rq[4];
      static_for<KSD>([&](auto I) {
        constexpr int ks = I;
        ak = mfma(rk[ks % PF], af[ks], ak); PIN(ak);
        if constexpr (ks + PF < KSD) rk[ks % PF] = lds_rd(wk, (ks + PF) * 1024);
        if constexpr (ks + AH < KSD) af[ks + AH] = XLD(ks + AH);
        if constexpr (ks == 20) { st = stats_finish(sl); PIN(st.mean); PIN(st.rs); }
        SB();
        av = mfma(af[ks], rv[ks % PF], av); PIN(av);
        if constexpr (ks + PF < KSD) rv[ks % PF] = lds_rd(wv, (ks + PF) * 1024);
        if constexpr (ks >= KSD - 4) rq[ks - (KSD - 4)] = lds_rd(wq, (ks - (KSD - 4)) * 1024);
        SB();
      });
      FS(22);
      const LAS float* csl = (const LAS float*)(aux + CSQ_L);
      f32x4 ck[4];
#pragma unroll
      for (int g = 0; g < 4; ++g) ck[g] = *(const LAS f32x4*)(csl + 8 * g + 4 * h2);
      const float csv = csl[32 + r];
      LAS float* stl = (LAS float*)(aux + 8 * 4096 + w * 4096);
      if (h2 == 0) stl[r] = st.rs; else stl[32 + r] = st.mean;
      bar();
      FS(23);
      f32x16 aq = {};
      u32x4 ko[2], vo[2];
      f32x4 srs[2], smn[2];
      f32x4 cq[4];
      float e0, e1;
      const unsigned kvo = OFF_KV + l * SZ_KV + (tt * 16 + j) * 4096;
      static_for<KSD>([&](auto I) {
        constexpr int ks = I;
        aq = mfma(rq[ks & 3], af[ks], aq); PIN(aq);
        if constexpr (ks + 4 < KSD) rq[ks & 3] = lds_rd(wq, (ks + 4) * 1024);
        if constexpr (ks == 0) { srs[0] = *(const LAS f32x4*)(stl + 4 * h2); smn[0] = *(const LAS f32x4*)(stl + 32 + 4 * h2); srs[1] = *(const LAS f32x4*)(stl + 8 + 4 * h2); smn[1] = *(const LAS f32x4*)(stl + 40 + 4 * h2); }
        if constexpr (ks >= 1 && ks < 5) {
          constexpr int g = ks - 1;
          float v[4];
#pragma unroll
          for (int e = 0; e < 4; ++e) { v[e] = ak[4 * g + e] - st.mean * ck[g][e]; PIN(v[e]); }
#pragma unroll
          for (int e = 0; e < 4; ++e) { v[e] *= st.rs; PIN(v[e]); }
          ko[g >> 1][(g & 1) * 2] = pk2(v[0], v[1]); ko[g >> 1][(g & 1) * 2 + 1] = pk2(v[2], v[3]);
          PIN(ko[g >> 1]);
        }
        if constexpr (ks == 5) { hst(rs, voff, kvo, ko[0]); hst(rs, voff + 1024, kvo, ko[1]); }
        if constexpr (ks >= 5 && ks < 13) {
          constexpr int q = ks - 5, g = q >> 1, e = (q & 1) * 2;
          if constexpr ((q & 1) == 0) { PIN(srs[g & 1]); PIN(smn[g & 1]); }
          e0 = av[2 * q] - smn[g & 1][e] * csv; e1 = av[2 * q + 1] - smn[g & 1][e + 1] * csv; PIN(e0); PIN(e1);
          e0 *= srs[g & 1][e]; e1 *= srs[g & 1][e + 1]; PIN(e0); PIN(e1);
          const unsigned pv = pk2(e0, e1);
          vo[q >> 2][q & 3] = r == 28 ? 0x3f803f80u : pv;
          PIN(vo[q >> 2]);
          if constexpr (q == 1 || q == 3) {
            srs[g & 1] = *(const LAS f32x4*)(stl + 8 * (g + 2) + 4 * h2); smn[g & 1] = *(const LAS f32x4*)(stl + 32 + 8 * (g + 2) + 4 * h2);
          }
        }
        if constexpr (ks == 13) { hst(rs, voff + 2048, kvo, vo[0]); hst(rs, voff + 3072, kvo, vo[1]); }
        if constexpr (ks == 14) { LAS char* kvl = aux + (8 + w) * 4096 + lane * 16; lds_wr(kvl, 0, ko[0]); lds_wr(kvl, 1024, ko[1]); lds_wr(kvl, 2048, vo[0]); lds_wr(kvl, 3072, vo[1]); }
        if constexpr (ks >= 16 && ks < 20) cq[ks - 16] = *(const LAS f32x4*)(csl + 64 + 8 * (ks - 16) + 4 * h2);
        if constexpr (ks == 25) { wait_vm_bar<0>(); }
        SB();
      });
      FS(24);
      const float qs = st.rs * (0.18898223650461362f * 1.4426950408889634f);
#pragma unroll
      for (int g = 0; g < 4; ++g) {
        float v[4];
#pragma unroll
        for (int e = 0; e < 4; ++e) { v[e] = aq[4 * g + e] - st.mean * cq[g][e]; PIN(v[e]); v[e] *= qs; PIN(v[e]); }
        qf[g >> 1][(g & 1) * 2] = pk2(v[0], v[1]); qf[g >> 1][(g & 1) * 2 + 1] = pk2(v[2], v[3]);
      }
      FS(25);
    }
    {
      IDS();
      const float NEG = -1e30f;
      f32x16 cd, co;
#pragma unroll
      for (int i = 0; i < 16; ++i) { const int key = (i & 3) + 8 * (i >> 2) + 4 * h2; cd[i] = key <= r ? 0.f : NEG; co[i] = key > r ? 0.f : NEG; }
      PIN(cd); PIN(co);
      bar();
      const LAS char* kw = opq((const LAS char*)aux + lane * 16 + w * 4096);
      bar();
      FS(29);
      float m = NEG;
      f32x16 o = {};
      if (rb >= 2) {
        u32x4 kf[9][2];
#pragma unroll
        for (int it = 0; it < 9; ++it) { kf[it][0] = lds_rd(kw, it * 4096); kf[it][1] = lds_rd(kw, it * 4096 + 1024); }
        SB();
        {
          const f32x16 z = {};
          f32x16 s[3];
          static_for<11>([&](auto IT) {
            constexpr int it = IT;
            if constexpr (it < 9) { s[it % 3] = mfma(kf[it][0], qf[0], it == 0 ? co : it == 8 ? cd : z); s[it % 3] = mfma(kf[it][1], qf[1], s[it % 3]); PIN(s[it % 3]); }
            if constexpr (it >= 2) {
              const f32x16& t = s[(it - 2) % 3];
              float ma = fmaxf(fmaxf(t[0], t[1]), t[2]), mb = fmaxf(fmaxf(t[3], t[4]), t[5]);
              ma = fmaxf(fmaxf(ma, t[6]), t[7]); mb = fmaxf(fmaxf(mb, t[8]), t[9]);
              ma = fmaxf(fmaxf(ma, t[10]), t[11]); mb = fmaxf(fmaxf(mb, t[12]), t[13]);
              ma = fmaxf(fmaxf(ma, t[14]), t[15]);
              m = fmaxf(fmaxf(m, ma), mb);
              PIN(m);
            }
            SB();
          });
        }
        m = -max32(m);
        PIN(m);
        FS(30);
        f32x16 cm;
#pragma unroll
        for (int i = 0; i < 16; ++i) { cm[i] = m; co[i] = co[i] + m; }
        u32x4 vf[2][2];
        vf[0][0] = lds_rd(kw, 2048); vf[0][1] = lds_rd(kw, 3072);
        f32x16 s[3];
        s[0] = mfma(kf[0][0], qf[0], co); s[0] = mfma(kf[0][1], qf[1], s[0]);
        s[1] = mfma(kf[1][0], qf[0], cm); s[1] = mfma(kf[1][1], qf[1], s[1]);
#pragma unroll
        for (int i = 0; i < 16; ++i) cd[i] = cd[i] + m;
        SB();
        static_for<9>([&](auto IT) {
          constexpr int it = IT;
          if constexpr (it < 8) { vf[(it + 1) & 1][0] = lds_rd(kw, (it + 1) * 4096 + 2048); vf[(it + 1) & 1][1] = lds_rd(kw, (it + 1) * 4096 + 3072); }
          u32x4 p[2];
          const f32x16& c = s[it % 3];
          if constexpr (it + 2 < 9) { s[(it + 2) % 3] = mfma(kf[it + 2][0], qf[0], it + 2 == 8 ? cd : cm); }
#pragma unroll
          for (int q = 0; q < 4; ++q) p[0][q] = pk2(__builtin_amdgcn_exp2f(c[2 * q]), __builtin_amdgcn_exp2f(c[2 * q + 1]));
          PIN(p[0]);
          SB();
          if constexpr (it + 2 < 9) { s[(it + 2) % 3] = mfma(kf[it + 2][1], qf[1], s[(it + 2) % 3]); PIN(s[(it + 2) % 3]); }
          o = mfma(vf[it & 1][0], p[0], o);
#pragma unroll
          for (int q = 0; q < 4; ++q) p[1][q] = pk2(__builtin_amdgcn_exp2f(c[8 + 2 * q]), __builtin_amdgcn_exp2f(c[8 + 2 * q + 1]));
          PIN(p[1]);
          SB();
          o = mfma(vf[it & 1][1], p[1], o); PIN(o);
          SB();
        });
      } else {
        const LAS char* kq = kw;
        const f32x16 z = {};
#pragma unroll
        for (int it = 0; it < 9; ++it) {
          if (tt - 8 + it >= 0) {
            f32x16 s = mfma(lds_rd(kq, it * 4096), qf[0], it == 0 ? co : it == 8 ? cd : z);
            s = mfma(lds_rd(kq, it * 4096 + 1024), qf[1], s);
#pragma unroll
            for (int i = 0; i < 16; i += 2) m = fmaxf(fmaxf(m, s[i]), s[i + 1]);
          }
        }
        m = -max32(m);
        f32x16 cm;
#pragma unroll
        for (int i = 0; i < 16; ++i) { cd[i] += m; cm[i] = m; }
#pragma unroll
        for (int it = 0; it < 9; ++it) {
          if (it >= 1 && tt - 8 + it >= 0) {
            f32x16 s = mfma(lds_rd(kq, it * 4096), qf[0], it == 8 ? cd : cm);
            s = mfma(lds_rd(kq, it * 4096 + 1024), qf[1], s);
            u32x4 p[2];
            PACK16(p, pk2(__builtin_amdgcn_exp2f(s[i0]), __builtin_amdgcn_exp2f(s[i1])));
            o = mfma(lds_rd(kq, it * 4096 + 2048), p[0], o);
            o = mfma(lds_rd(kq, it * 4096 + 3072), p[1], o);
          }
        }
      }
      FS(31);
      const unsigned lu = __float_as_uint(o[12]);
      auto rr = __builtin_amdgcn_permlane32_swap(lu, lu, false, false);
      const float inv = __builtin_amdgcn_rcpf(__uint_as_float(rr[1]));
      u32x4 ob[2];
      PACK16(ob, pk2(o[i0] * inv, o[i1] * inv));
      const unsigned oo = OFF_OA + l * SZ_OA + (tt * KSO + 2 * j) * 1024;
      hst(rs, voff, oo, ob[0]); hst(rs, voff + 1024, oo, ob[1]);
      FS(32);
      wait_vm_bar<0>();
      FS(33);
    }
    auto proj_init = [&](int xin) { IDS(); const unsigned xo = OFF_XA + xin * SZ_XA + (ttp * KSD + 4 * fs + 2 * kh) * 1024;
      u32x4 xr[2]; xr[0] = hld(rs, voff, xo); xr[1] = hld(rs, voff + 1024, xo);
      f32x16 a;
#pragma unroll
      for (int q = 0; q < 8; ++q) { const unsigned xv = xr[q >> 2][q & 3]; a[2 * q] = bflo(xv); a[2 * q + 1] = bfhi(xv); }
      PIN(a);
      return a; };
    auto proj_finish = [&](f32x16 own, const f32x16& snd, int xout, bool final_) {
      IDS();
      u32x4 xres[2];
      LAS f32x4* xs = (LAS f32x4*)(aux) + (tt2 * 2 + kh) * 256 + lane;
      LAS f32x4* xrv = (LAS f32x4*)(aux) + (tt2 * 2 + (kh ^ 1)) * 256 + lane;
#pragma unroll
      for (int g = 0; g < 4; ++g) { f32x4 v = {snd[4 * g], snd[4 * g + 1], snd[4 * g + 2], snd[4 * g + 3]}; xs[g * 64] = v; }
      bar_lgkm();
#pragma unroll
      for (int g = 0; g < 4; ++g) { const f32x4 v = xrv[g * 64]; own[4 * g] += v.x; own[4 * g + 1] += v.y; own[4 * g + 2] += v.z; own[4 * g + 3] += v.w; }
#pragma unroll
      for (int q = 0; q < 8; ++q) xres[q >> 2][q & 3] = pk2(own[2 * q], own[2 * q + 1]);
      if (final_) {
        unsigned short* orow = out + (size_t)(ttp * 32 + r) * D_ + 64 * fs + 32 * kh + 4 * h2;
#pragma unroll
        for (int g = 0; g < 4; ++g) { u32x2 v = {xres[g >> 1][(g & 1) * 2], xres[g >> 1][(g & 1) * 2 + 1]}; *(GAS u32x2*)(orow + 8 * g) = v; }
      } else {
        const unsigned xo = OFF_XA + xout * SZ_XA + (ttp * KSD + 4 * fs + 2 * kh) * 1024;
        hst(rs, voff, xo, xres[0]); hst(rs, voff + 1024, xo, xres[1]);
        float s1[4], s2[4];
#pragma unroll
        for (int g = 0; g < 4; ++g) {
          s1[g] = (own[4 * g] + own[4 * g + 1]) + (own[4 * g + 2] + own[4 * g + 3]);
          s2[g] = __builtin_fmaf(own[4 * g], own[4 * g], own[4 * g + 1] * own[4 * g + 1]) + __builtin_fmaf(own[4 * g + 2], own[4 * g + 2], own[4 * g + 3] * own[4 * g + 3]);
        }
        const float t1 = sum32((s1[0] + s1[1]) + (s1[2] + s1[3])), t2 = sum32((s2[0] + s2[1]) + (s2[2] + s2[3]));
        if (h2 == 0) { u32x2 v = {__float_as_uint(t1), __float_as_uint(t2)}; __builtin_amdgcn_raw_buffer_store_b64(v, rs, r * 8, OFF_ST + xout * SZ_ST + (ttp * 14 + 2 * fs + kh) * 256, 16); }
      }
    };
    if (active) {
      IDS();
      f32x16 own = proj_init(2 * l), snd = {};
      bar();
      FS(34);
      u32x4 af[16];
      const unsigned ao = OFF_OA + l * SZ_OA + (ttp * KSO + kh * 16) * 1024;
      constexpr int AH = 10;
      const unsigned oa = ao + 2 * fs * 1024, ob_ = oa - 16 * 1024;
#define OLD(i) hld(rs, voff + (i) * 1024, (i) + 2 * fs < 16 ? oa : ob_)
      static_for<AH>([&](auto I) { constexpr int i = I; af[i] = OLD(i); });
      const LAS char* wbo[3] = {opq(ring + 3 * CHB + kh * 3072), opq(ring + 0 * CHB + kh * 3072), opq((const LAS char*)aux + WOT_L + lane * 16 + kh * 3072)};
      const LAS char* wbs[3] = {opq(ring + 3 * CHB + kh * 2048 + (kh ^ 1) * 1024), opq(ring + 0 * CHB + kh * 2048 + (kh ^ 1) * 1024), opq((const LAS char*)aux + WOT_L + lane * 16 + kh * 2048 + (kh ^ 1) * 1024)};
      constexpr int PF = 2;
      u32x4 wo_[PF], wsn[PF];
#pragma unroll
      for (int i = 0; i < PF; ++i) { wo_[i] = lds_rd(wbo[0], i * 4096); wsn[i] = lds_rd(wbs[0], i * 4096); }
      SB();
      static_for<16>([&](auto I) {
        constexpr int i = I, n = i + PF;
        own = mfma(wo_[i % PF], af[i], own); PIN(own);
        if constexpr (n < 16) wo_[i % PF] = lds_rd(wbo[n / 7], (n % 7) * 4096);
        if constexpr (i + AH < 16) af[i + AH] = OLD(i + AH);
        SB();
        snd = mfma(wsn[i % PF], af[i], snd); PIN(snd);
        if constexpr (n < 16) wsn[i % PF] = lds_rd(wbs[n / 7], (n % 7) * 4096);
        SB();
      });
      FS(35);
      proj_finish(own, snd, 2 * l + 1, false);
      FS(38);
      wait_vm_bar<0>();
      FS(39);
    }
    {
      IDS();
      bar();
      FS(0);
      constexpr int AH = 10;
      u32x4 af[KSD];
      const unsigned xo = OFF_XA + (2 * l + 1) * SZ_XA + tt * (KSD * 1024);
      const int rot = rot_x(j);
      const unsigned xa = xo + rot * 1024, xb = xa - KSD * 1024;
      StatLd sl; stats_issue(sl, rs, OFF_ST + (2 * l + 1) * SZ_ST + tt * 3584, lane);
      Stats st;
      static_for<AH>([&](auto I) { constexpr int ks = I; af[ks] = XLD(ks); });
      const LAS float* csl = (const LAS float*)(aux + CSG_L) + 4 * h2;
      const unsigned ho = OFF_HA + l * SZ_HA + tt * HSTR + 7 * j * 1024;
      f32x16 prev = {}, acc = {};
      u32x4 wr[4];
      {
        const LAS char* w0 = slot(5); const LAS char* w1 = slot(6);
        constexpr int PF = 3;
        u32x4 r0[PF], r1[PF];
#pragma unroll
        for (int i = 0; i < PF; ++i) { r0[i] = lds_rd(w0, i * 1024); r1[i] = lds_rd(w1, i * 1024); }
        SB();
        FS(2);
        static_for<KSD>([&](auto I) {
          constexpr int ks = I;
          prev = mfma(r0[ks % PF], af[ks], prev); PIN(prev);
          if constexpr (ks + PF < KSD) r0[ks % PF] = lds_rd(w0, (ks + PF) * 1024);
          if constexpr (ks + AH < KSD) af[ks + AH] = XLD(ks + AH);
          if constexpr (ks == 20) { st = stats_finish(sl); PIN(st.mean); PIN(st.rs); }
          SB();
          acc = mfma(r1[ks % PF], af[ks], acc); PIN(acc);
          if constexpr (ks + PF < KSD) r1[ks % PF] = lds_rd(w1, (ks + PF) * 1024);
          SB();
        });
      }
      FS(3);
      const float c1 = -1.4426950408889634f * st.rs, vk = __builtin_amdgcn_rcpf(st.rs * st.rs);
      u32x4 ov;
      f32x4 cc[4];
      float ea[8], eb[8], ex[8];
#define GU_STAGE(P, v, S) do { \
        if constexpr ((S) == 0) { ea[v] = P[v] - st.mean * cc[(v) >> 2][(v) & 3]; PIN(ea[v]); eb[v] = P[(v) + 8] - st.mean * cc[2 + ((v) >> 2)][(v) & 3]; PIN(eb[v]); } \
        else if constexpr ((S) == 1) { ex[v] = c1 * ea[v]; PIN(ex[v]); eb[v] = ea[v] * eb[v]; PIN(eb[v]); } \
        else if constexpr ((S) == 2) { ex[v] = __builtin_amdgcn_exp2f(ex[v]); PIN(ex[v]); } \
        else if constexpr ((S) == 3) { ex[v] = __builtin_fmaf(ex[v], vk, vk); PIN(ex[v]); } \
        else if constexpr ((S) == 4) { ex[v] = __builtin_amdgcn_rcpf(ex[v]); PIN(ex[v]); } \
        else { eb[v] = eb[v] * ex[v]; PIN(eb[v]); if constexpr ((v) & 1) { ov[(v) >> 1] = pk2(eb[(v) - 1], eb[v]); PIN(ov); } } } while (0)
#define GU_GAP(P, g, base, sp) static_for<8>([&](auto V_) { constexpr int v_ = V_; constexpr int s_ = (g) - (base) - (sp) * v_; if constexpr (s_ >= 0 && s_ < 6) GU_STAGE(P, v_, s_); })
#define GU_CC(t) _Pragma("unroll") for (int g = 0; g < 4; ++g) cc[g] = *(const LAS f32x4*)(csl + (t) * 32 + 8 * g)
      GU_CC(0);
      bar();
      { const LAS char* w2 = slot(7);
#pragma unroll
        for (int i = 0; i < 4; ++i) wr[i] = lds_rd(w2, i * 1024); }
      SB();
      FS(4);
      f32x16 t1 = acc;
      static_for<5>([&](auto T) {
        constexpr int t = T + 2;
        const LAS char* wl = slot(5 + t);
        const LAS char* wn = slot(6 + t);
        acc = f32x16{};
        static_for<KSD>([&](auto I) {
          constexpr int ks = I;
          if constexpr (ks == 14) {
            if constexpr (t < 6) bar();
            else asm volatile("s_waitcnt vmcnt(0)\n\ts_barrier" ::: "memory");
            SB();
          }
          acc = mfma(wr[ks & 3], af[ks], acc); PIN(acc);
          if constexpr (ks + 4 < KSD) wr[ks & 3] = lds_rd(wl, (ks + 4) * 1024);
          else if constexpr (t < 6) wr[ks & 3] = lds_rd(wn, (ks + 4 - KSD) * 1024);
          if constexpr (t == 2) {
            if constexpr (ks < 14) { GU_GAP(prev, ks, 0, 1); }
            if constexpr (ks == 12) { GU_CC(1); }
            if constexpr (ks == 13) hst(rs, voff, ho, ov);
            if constexpr (ks >= 14) { GU_GAP(t1, ks, 15, 1); }
            if constexpr (ks == 27) { hst(rs, voff + 1024, ho, ov); GU_CC(2); }
          } else {
            GU_GAP(prev, ks, 1, 3);
            if constexpr (ks == 27) { hst(rs, voff + (t - 1) * 1024, ho, ov); GU_CC(t); }
          }
          SB();
        });
        prev = acc;
        FS(3 + t);
      });
      static_for<13>([&](auto G) { constexpr int g = G; GU_GAP(prev, g, 0, 1); });
      hst(rs, voff + 6 * 1024, ho, ov);
      FS(10);
      wait_vm_bar<0>();
      FS(11);
    }
    if (active) {
      IDS();
      f32x16 own = proj_init(2 * l + 1), snd = {};
      bar();
      FS(41);
      constexpr int WIN = DN_WIN;
      constexpr int NR = WIN + 2;
      u32x4 af[NR];
      const unsigned ao = OFF_HA + l * SZ_HA + ttp * HSTR + kh * 56 * 1024;
#define HLD(i) hld(rs, voff + (kk_of(i) % 7) * 1024, ao + (((kk_of(i) / 7) + fs) & 7) * 7168)
      static_for<WIN>([&](auto I) { constexpr int i = I; af[i] = HLD(i); });
      const int oo_ = kh * 3072, so_ = kh * 2048 + (kh ^ 1) * 1024;
      constexpr int PF = 2;
      u32x4 wo_[PF], wsn[PF];
      const LAS char* po = opq(ring + 0 * CHB + oo_); const LAS char* ps = opq(ring + 0 * CHB + so_);
#pragma unroll
      for (int i = 0; i < PF; ++i) { wo_[i] = lds_rd(po, i * 4096); wsn[i] = lds_rd(ps, i * 4096); }
      SB();
      FS(42);
      const LAS char* pon = po; const LAS char* psn = ps;
      static_for<56>([&](auto I) {
        constexpr int i = I, c = i / 7, s = i % 7, n = s + PF;
        if constexpr (s == 0) { po = pon; ps = psn; if constexpr (c < 7) { pon = opq(ring + ((13 + c) & 3) * CHB + oo_); psn = opq(ring + ((13 + c) & 3) * CHB + so_); } }
        if constexpr (i == 1) FS(43);
        if constexpr (i == 28) FS(44);
        if constexpr (s == 3) { bar(); SB(); }
        const u32x4 a = af[i % NR];
        own = mfma(wo_[i % PF], a, own); PIN(own);
        if constexpr (n < 7) wo_[i % PF] = lds_rd(po, n * 4096); else if constexpr (c < 7) wo_[i % PF] = lds_rd(pon, (n - 7) * 4096);
        SB();
        snd = mfma(wsn[i % PF], a, snd); PIN(snd);
        if constexpr (n < 7) wsn[i % PF] = lds_rd(ps, n * 4096); else if constexpr (c < 7) wsn[i % PF] = lds_rd(psn, (n - 7) * 4096);
        if constexpr (i + WIN < 56) af[(i + WIN) % NR] = HLD(i + WIN);
        SB();
      });
      FS(45);
      proj_finish(own, snd, 2, l == 1);
      FS(46);
      wait_vm_bar<0>();
      FS(47);
    }
    if (l == 0) FS(61);
  }
}

}

extern "C" size_t stamp_offset() { return OFF_STAMP; }

extern "C" void kernel_launch(void* const* d_in, const int* in_sizes, int n_in, void* d_out, int out_size, void* d_ws, size_t ws_size,
                              hipStream_t stream) {
  const unsigned short* x = (const unsigned short*)d_in[0];
  const unsigned short* wqkv = (const unsigned short*)d_in[1];
  const unsigned short* wo = (const unsigned short*)d_in[2];
  const unsigned short* w1 = (const unsigned short*)d_in[4];
  const unsigned short* w3 = (const unsigned short*)d_in[5];
  const unsigned short* w2 = (const unsigned short*)d_in[6];
  char* ws = (char*)d_ws;
  (void)hipFuncSetAttribute((const void*)k_main, hipFuncAttributeMaxDynamicSharedMemorySize, LDS_BYTES);
  k_prep<<<PREP_BLOCKS, 256, PREP_LDS, stream>>>(x, wqkv, wo, w1, w3, w2, ws);
  k_main<<<256, 512, LDS_BYTES, stream>>>(ws, (unsigned short*)d_out);
}
```

```cpp
#include <hip/hip_runtime.h>
#include <stdint.h>
#include <utility>

typedef __attribute__((ext_vector_type(8))) __bf16 bf16x8;
typedef __attribute__((ext_vector_type(2))) __bf16 bf16x2;
typedef __attribute__((ext_vector_type(2))) float f32x2;
typedef __attribute__((ext_vector_type(4))) float f32x4;
typedef __attribute__((ext_vector_type(16))) float f32x16;
typedef __attribute__((ext_vector_type(4))) unsigned u32x4;
typedef __attribute__((ext_vector_type(2))) unsigned u32x2;
#define DEV __device__ __forceinline__
#define LAS __attribute__((address_space(3)))
#define GAS __attribute__((address_space(1)))
typedef GAS unsigned gu32;
#define RLX __ATOMIC_RELAXED, __HIP_MEMORY_SCOPE_AGENT

namespace {

constexpr int D_ = 448, F_ = 1792;
constexpr int NTT = 64;
constexpr int KSD = 28;
constexpr int KSO = 32;
constexpr int KSF = 112;
constexpr int CHB = 28672;

constexpr unsigned OFF_FLAG = 0;
constexpr int FL_KV = 0;
constexpr int FL_O = FL_KV + 1024;
constexpr int FL_X1 = FL_O + 512;
constexpr int FL_H = FL_X1 + 512;
constexpr int FL_X2 = FL_H + 1024;
constexpr int FL_TMO = FL_X2 + 256;
constexpr int NFLAG = FL_TMO + 1;
constexpr unsigned OFF_WQKV = 448 * 1024;
constexpr unsigned OFF_WO   = OFF_WQKV + 16 * 3 * CHB;
constexpr unsigned OFF_WGU  = OFF_WO + 7 * 64 * 1024;
constexpr unsigned OFF_WDN  = OFF_WGU + 16 * 7 * CHB;
constexpr unsigned OFF_CSQ  = OFF_WDN + 7 * 224 * 1024;
constexpr unsigned OFF_CSG  = OFF_CSQ + 16 * 3 * 32 * 4;
constexpr unsigned SZ_XA    = NTT * KSD * 1024;
constexpr unsigned OFF_XA   = OFF_CSG + 16 * 7 * 32 * 4;
constexpr unsigned SZ_ST    = NTT * 14 * 32 * 8;
constexpr unsigned OFF_ST   = OFF_XA + 4 * SZ_XA;
constexpr unsigned SZ_KV    = NTT * 16 * 4096;
constexpr unsigned OFF_KV   = OFF_ST + 4 * SZ_ST;
constexpr unsigned SZ_OA    = NTT * KSO * 1024;
constexpr unsigned OFF_OA   = OFF_KV + 2 * SZ_KV;
#define HPAD 0
constexpr unsigned HSTR = KSF * 1024 + HPAD;
constexpr unsigned SZ_HA    = NTT * HSTR;
constexpr unsigned OFF_HA   = OFF_OA + 2 * SZ_OA;
constexpr unsigned WS_END   = OFF_HA + 2 * SZ_HA;
constexpr unsigned OFF_STAMP = (WS_END + 4095) / 4096 * 4096;
static_assert(NFLAG * 128 <= OFF_WQKV, "flags");
static_assert(OFF_XA % 128 == 0 && OFF_ST % 128 == 0 && OFF_KV % 128 == 0 && OFF_OA % 128 == 0 && OFF_HA % 128 == 0, "lines");

#define STAMP(i) do {} while (0)
#define FS(i) do {} while (0)

template <class F, int... I> DEV void static_for_impl(F&& f, std::integer_sequence<int, I...>) { (f(std::integral_constant<int, I>{}), ...); }
template <int N, class F> DEV void static_for(F&& f) { static_for_impl(f, std::make_integer_sequence<int, N>{}); }

DEV int lane_id() { int l; asm volatile("v_mbcnt_lo_u32_b32 %0, -1, 0\n\tv_mbcnt_hi_u32_b32 %0, -1, %0" : "=v"(l)); return l; }
DEV unsigned pk2(float a, float b) { f32x2 v = {a, b}; return __builtin_bit_cast(unsigned, __builtin_convertvector(v, bf16x2)); }
DEV f32x16 mfma(u32x4 a, u32x4 b, f32x16 c) {
  return __builtin_amdgcn_mfma_f32_32x32x16_bf16(__builtin_bit_cast(bf16x8, a), __builtin_bit_cast(bf16x8, b), c, 0, 0, 0);
}
DEV float bflo(unsigned u) { return __uint_as_float(u << 16); }
DEV float bfhi(unsigned u) { return __uint_as_float(u & 0xffff0000u); }
DEV float swap32(float v) {
  const unsigned u = __float_as_uint(v);
  auto rr = __builtin_amdgcn_permlane32_swap(u, u, false, false);
  return __uint_as_float((threadIdx.x & 32) ? rr[0] : rr[1]);
}
DEV float sum32(float v) {
  const unsigned u = __float_as_uint(v);
  auto rr = __builtin_amdgcn_permlane32_swap(u, u, false, false);
  return __uint_as_float(rr[0]) + __uint_as_float(rr[1]);
}
DEV float max32(float v) {
  const unsigned u = __float_as_uint(v);
  auto rr = __builtin_amdgcn_permlane32_swap(u, u, false, false);
  return fmaxf(__uint_as_float(rr[0]), __uint_as_float(rr[1]));
}
DEV void dma7(const char* g, unsigned voff, unsigned lds) {
  unsigned keep;
  const char* g2 = g + 4096;
  lds = __builtin_amdgcn_readfirstlane(lds);
  asm volatile("s_mov_b32 %0, m0\n\ts_mov_b32 m0, %2\n\ts_nop 0\n\t"
               "global_load_lds_dwordx4 %1, %3 offset:0\n\t"
               "global_load_lds_dwordx4 %1, %3 offset:1024\n\t"
               "global_load_lds_dwordx4 %1, %3 offset:2048\n\t"
               "global_load_lds_dwordx4 %1, %3 offset:3072\n\t"
               "s_add_u32 m0, m0, 0x1000\n\ts_nop 0\n\t"
               "global_load_lds_dwordx4 %1, %4 offset:0\n\t"
               "global_load_lds_dwordx4 %1, %4 offset:1024\n\t"
               "global_load_lds_dwordx4 %1, %4 offset:2048\n\t"
               "s_mov_b32 m0, %0"
               : "=&s"(keep) : "v"(voff), "s"(lds), "s"(g), "s"(g2) : "memory", "scc");
}
template <int N> DEV void wait_vm() { asm volatile("s_waitcnt vmcnt(%0)" ::"n"(N) : "memory"); }
template <int N> DEV void wait_vm_bar() { asm volatile("s_waitcnt vmcnt(%0) lgkmcnt(0)\n\ts_barrier" ::"n"(N) : "memory"); }
DEV void bar_lgkm() { asm volatile("s_waitcnt lgkmcnt(0)\n\ts_barrier" ::: "memory"); }
DEV u32x4 lds_rd(const LAS char* base, int off) { return *(const LAS u32x4*)(base + off); }
DEV void lds_wr(LAS char* base, int off, u32x4 v) { *(LAS u32x4*)(base + off) = v; }
DEV u32x4 gld(const char* p) { return *(const GAS u32x4*)(p); }
DEV void gst(char* p, u32x4 v) { *(GAS u32x4*)(p) = v; }
typedef __amdgpu_buffer_rsrc_t rsrc_t;
DEV u32x4 hld(rsrc_t rs, unsigned voff, unsigned soff) { return __builtin_amdgcn_raw_buffer_load_b128(rs, voff, soff, 16); }
DEV void hst(rsrc_t rs, unsigned voff, unsigned soff, u32x4 v) { __builtin_amdgcn_raw_buffer_store_b128(v, rs, voff, soff, 16); }

struct Poll { unsigned v0, v1, v2, v3; };
DEV void poll_ge(Poll& q, char* ws, int flag, unsigned target, int on) {
  const char* c = ws + OFF_FLAG + (size_t)flag * 128;
  unsigned cnt, tmp;
  on = __builtin_amdgcn_readfirstlane(on);
  asm volatile(
      "s_mov_b32 %4, 0\n\t"
      "s_cmp_eq_u32 %9, 0\n\t"
      "s_cbranch_scc1 2f\n"
      "1:\n\t"
      "global_load_dword %0, %6, %7 sc1\n\t"
      "s_waitcnt vmcnt(0)\n\tv_readfirstlane_b32 %5, %0\n\ts_nop 0\n\ts_cmp_ge_u32 %5, %8\n\ts_cbranch_scc1 2f\n\ts_sleep 1\n\t"
      "s_add_u32 %4, %4, 1\n\ts_cmp_lt_u32 %4, 0x8000\n\ts_cbranch_scc1 1b\n"
      "2:"
      : "+v"(q.v0), "+v"(q.v1), "+v"(q.v2), "+v"(q.v3), "=&s"(cnt), "=&s"(tmp)
      : "v"(0u), "s"(c), "s"(target), "s"(on)
      : "memory", "scc");
  if (cnt >= 0x8000u && lane_id() == 0) __hip_atomic_store((gu32*)(ws + OFF_FLAG + (size_t)FL_TMO * 128), 1u, RLX);
}
DEV void poll_two(char* ws, int fa, int fb) {
  const char* c = ws + OFF_FLAG + (size_t)fa * 128;
  const unsigned vo = (lane_id() & 1) * (unsigned)((fb - fa) * 128);
  unsigned cnt, v;
  asm volatile(
      "s_mov_b32 %1, 0\n"
      "1:\n\t"
      "global_load_dword %0, %2, %3 sc1\n\t"
      "s_waitcnt vmcnt(0)\n\tv_cmp_eq_u32 vcc, 0, %0\n\ts_nop 1\n\ts_cbranch_vccz 2f\n\ts_sleep 1\n\t"
      "s_add_u32 %1, %1, 1\n\ts_cmp_lt_u32 %1, 0x8000\n\ts_cbranch_scc1 1b\n"
      "2:"
      : "=&v"(v), "=&s"(cnt) : "v"(vo), "s"(c) : "memory", "scc", "vcc");
  if (cnt >= 0x8000u && lane_id() == 0) __hip_atomic_store((gu32*)(ws + OFF_FLAG + (size_t)FL_TMO * 128), 1u, RLX);
}
DEV void poll_done(Poll& q, int on) {
  on = __builtin_amdgcn_readfirstlane(on);
  asm volatile("s_cmp_eq_u32 %4, 0\n\ts_cbranch_scc1 3f\n\ts_waitcnt vmcnt(0)\n3:" : "+v"(q.v0), "+v"(q.v1), "+v"(q.v2), "+v"(q.v3) : "s"(on) : "scc");
}
DEV void sig_add16(char* ws, int flag, int lane) { if (lane < 16) (void)__hip_atomic_fetch_add((gu32*)(ws + OFF_FLAG + (size_t)(flag + lane) * 128), 1u, RLX); }
DEV void sig_add(char* ws, int flag) { (void)__hip_atomic_fetch_add((gu32*)(ws + OFF_FLAG + (size_t)flag * 128), 1u, RLX); }

struct Stats { float mean, rs; };
DEV Stats load_stats(rsrc_t rs, unsigned off_st, int tt, int lane) {
  const int r = lane & 31, h2 = lane >> 5;
  float s1 = 0.f, s2 = 0.f;
  u32x2 v[7];
#pragma unroll
  for (int i = 0; i < 7; ++i) v[i] = __builtin_amdgcn_raw_buffer_load_b64(rs, h2 * 1792 + r * 8 + i * 256, off_st + tt * 3584, 16);
#pragma unroll
  for (int i = 0; i < 7; ++i) { s1 += __uint_as_float(v[i].x); s2 += __uint_as_float(v[i].y); }
  s1 = sum32(s1); s2 = sum32(s2);
  Stats s; s.mean = s1 * (1.0f / D_);
  const float var = fmaxf(s2 * (1.0f / D_) - s.mean * s.mean, 0.f);
  s.rs = __builtin_amdgcn_rsqf(var + 1e-5f);
  return s;
}
constexpr int G1T = 5, G1S = 8 * G1T;
__host__ __device__ constexpr int kk_of(int i) { return i < G1S ? 7 * (i / G1T) + i % G1T : 7 * ((i - G1S) / (7 - G1T)) + G1T + (i - G1S) % (7 - G1T); }

#define ROTX 0
__host__ __device__ constexpr int rot_x(int j) { return ROTX ? (7 * (j & 7)) >> 1 : 0; }
typedef short s16x4 __attribute__((ext_vector_type(4)));
DEV u32x2 tr_read(const LAS char* p) { return __builtin_bit_cast(u32x2, __builtin_amdgcn_ds_read_tr16_b64_v4i16((LAS s16x4*)p)); }
template <int KIND>
DEV void prep_unit(const unsigned short* __restrict__ src, const unsigned short* __restrict__ src2, int ld, int col, char* __restrict__ dst, float* cs, int a1, int a2, char* smem) {
  const int tid = threadIdx.x, lane = tid & 63, w = tid >> 6, r = lane & 31, h2 = lane >> 5;
  LAS char* img = (LAS char*)smem;
  if (KIND == 0) {
    u32x2 v[14];
    const int p = tid & 7, row0 = tid >> 3;
#pragma unroll
    for (int n = 0; n < 14; ++n) v[n] = p < 7 ? *(const GAS u32x2*)(src + (size_t)(row0 + 32 * n) * ld + col + 4 * p) : u32x2{0u, 0u};
#pragma unroll
    for (int n = 0; n < 14; ++n) *(LAS u32x2*)(img + (row0 + 32 * n) * 64 + p * 8) = v[n];
  } else {
    u32x4 v[7];
    const int p = tid & 3, row0 = tid >> 2;
    const unsigned short* sp = KIND == 1 ? (p < 2 ? src + col + 8 * p : src2 + col + 8 * (p - 2)) : src + col + 8 * p;
#pragma unroll
    for (int n = 0; n < 7; ++n) v[n] = *(const GAS u32x4*)(sp + (size_t)((KIND == 3 ? 448 * (a2 & 3) : 0) + row0 + 64 * n) * ld);
#pragma unroll
    for (int n = 0; n < 7; ++n) {
      const int row = row0 + 64 * n;
      const int irow = KIND == 2 ? 32 * (row / 28) + row % 28 : row;
      *(LAS u32x4*)(img + irow * 64 + p * 16) = v[n];
    }
    if (KIND == 2) { const int head = tid >> 4, d = 28 + ((tid >> 2) & 3); *(LAS u32x4*)(img + (32 * head + d) * 64 + p * 16) = u32x4{0u, 0u, 0u, 0u}; }
  }
  __syncthreads();
  constexpr int NKS = KIND == 2 ? 32 : 28;
  const LAS char* rd = img + (4 * h2 + ((lane & 15) >> 2)) * 64 + ((lane >> 4) & 1) * 32 + (lane & 3) * 8;
  float sum = 0.f;
#pragma unroll
  for (int n = 0; n < NKS / 4; ++n) {
    const int i = w + 4 * n;
    const u32x2 lo = tr_read(rd + i * 1024), hi = tr_read(rd + i * 1024 + 512);
    const u32x4 v = {lo.x, lo.y, hi.x, hi.y};
    size_t doff;
    if (KIND <= 1) doff = (size_t)((i + 28 - a2) % 28) * 1024;
    else if (KIND == 2) doff = (size_t)((((i - 2 * a2) & 15) * 2 + (i >> 4)) * 2 + a1) * 1024;
    else {
      const int ks = 28 * (a2 & 3) + i, kh = ks / 56, kk = ks % 56, j8 = (kk / 7 - (a2 >> 2)) & 7, t = kk % 7;
      const int step = t < G1T ? G1T * j8 + t : G1S + (7 - G1T) * j8 + (t - G1T);
      doff = (size_t)((step * 2 + kh) * 2 + a1) * 1024;
    }
    gst(dst + doff + lane * 16, v);
    if (KIND <= 1) {
#pragma unroll
      for (int q = 0; q < 4; ++q) sum += bflo(v[q]) + bfhi(v[q]);
    }
  }
  if (KIND <= 1) {
    sum = sum32(sum);
    __syncthreads();
    LAS float* red = (LAS float*)img;
    if (h2 == 0) red[w * 32 + r] = sum;
    __syncthreads();
    if (tid < 32) cs[tid] = (red[tid] + red[32 + tid]) + (red[64 + tid] + red[96 + tid]);
  }
}
constexpr int PREP_LDS = 32768;
constexpr int PREP_BLOCKS = 8 * 41;
__global__ __launch_bounds__(256) void k_prep(const unsigned short* __restrict__ x, const unsigned short* __restrict__ wqkv,
                                              const unsigned short* __restrict__ wo, const unsigned short* __restrict__ w1,
                                              const unsigned short* __restrict__ w3, const unsigned short* __restrict__ w2, char* __restrict__ ws) {
  extern __shared__ __attribute__((aligned(16))) char smem[];
  const int b = blockIdx.x, tid = threadIdx.x, lane = tid & 63, w = tid >> 6, r = lane & 31, h2 = lane >> 5;
  const int xg = b & 7, k = b >> 3;
  if (k >= 38) {
    const size_t a = (size_t)((k - 38) * 256 + tid) * 65536;
    if (a < WS_END) { unsigned v = *(const GAS unsigned*)(ws + a + 60 * 1024 + 4 * 250); asm volatile("" :: "v"(v)); }
    return;
  }
  if (k >= 30) {
    const int tt = xg * 8 + (k - 30);
    if (tt < 14) { const int f = tt * 256 + tid; if (f < NFLAG) *(GAS unsigned*)(ws + OFF_FLAG + (size_t)f * 128) = 0u; }
    LAS char* img = (LAS char*)smem;
    u32x4 v[7];
#pragma unroll
    for (int n = 0; n < 7; ++n) { const int c = tid + 256 * n; v[n] = *(const GAS u32x4*)(x + (size_t)tt * 32 * D_ + c * 8); }
#pragma unroll
    for (int n = 0; n < 7; ++n) { const int c = tid + 256 * n; *(LAS u32x4*)(img + (c / 56) * 912 + (c % 56) * 16) = v[n]; }
    __syncthreads();
    for (int ft = w; ft < 14; ft += 4) {
      float s1 = 0.f, s2 = 0.f;
#pragma unroll
      for (int s = 0; s < 2; ++s) {
        const int ks = 2 * ft + s;
        const u32x2 a = *(const LAS u32x2*)(img + r * 912 + ks * 32 + 8 * h2), c = *(const LAS u32x2*)(img + r * 912 + ks * 32 + 16 + 8 * h2);
        const u32x4 o = {a.x, a.y, c.x, c.y};
        gst(ws + OFF_XA + ((size_t)tt * KSD + ks) * 1024 + lane * 16, o);
#pragma unroll
        for (int q = 0; q < 4; ++q) { const float lo = bflo(o[q]), hi = bfhi(o[q]); s1 += lo + hi; s2 += lo * lo + hi * hi; }
      }
      s1 = sum32(s1); s2 = sum32(s2);
      if (h2 == 0) { f32x2 o = {s1, s2}; *(GAS f32x2*)(ws + OFF_ST + ((size_t)tt * 14 + ft) * 256 + r * 8) = o; }
    }
    return;
  }
  if (k < 14) {
    const int u = xg * 14 + k, j = u / 7, t = u % 7;
    prep_unit<1>(w1, w3, F_, 112 * j + 16 * t, ws + OFF_WGU + (size_t)u * CHB, (float*)(ws + OFF_CSG) + u * 32, 0, rot_x(j), smem);
  } else if (k < 20) {
    const int u = xg * 6 + (k - 14), j = u / 3, p = u % 3;
    prep_unit<0>(wqkv, nullptr, 3 * D_, (p == 2 ? 0 : p == 0 ? D_ : 2 * D_) + 28 * j, ws + OFF_WQKV + (size_t)u * CHB, (float*)(ws + OFF_CSQ) + u * 32, 0, rot_x(j), smem);
  } else if (xg < 7) {
    if (k < 28) { const int fs = xg, tile = (k - 20) & 1, q = (k - 20) >> 1;
      prep_unit<3>(w2, nullptr, D_, 64 * fs + 32 * tile, ws + OFF_WDN + (size_t)fs * 224 * 1024, nullptr, tile, q + 4 * fs, smem);
    } else { const int fs = xg, tile = k - 28;
      prep_unit<2>(wo, nullptr, D_, 64 * fs + 32 * tile, ws + OFF_WO + (size_t)fs * 64 * 1024, nullptr, tile, fs, smem);
    }
  }
}

constexpr int AUX = 4 * CHB;
constexpr int LDS_BYTES = AUX + 49152;
#define SB() __builtin_amdgcn_sched_barrier(0)
#define PIN(x) asm volatile("" : "+v"(x))
#define PACK16(o, expr) _Pragma("unroll") for (int q_ = 0; q_ < 8; ++q_) { const int i0 = 2 * q_, i1 = 2 * q_ + 1; (void)i0; (void)i1; o[q_ >> 2][q_ & 3] = expr; }
template <class T> DEV void opaque_s(T& v) { asm volatile("" : "+s"(v)); }

struct StatLd { u32x2 v[7]; };
DEV void stats_issue(StatLd& s, rsrc_t rs, unsigned soff, int lane) {
  const unsigned vo = (lane >> 5) * 1792 + (lane & 31) * 8;
#pragma unroll
  for (int i = 0; i < 7; ++i) s.v[i] = __builtin_amdgcn_raw_buffer_load_b64(rs, vo + i * 256, soff, 16);
}
DEV Stats stats_finish(const StatLd& s) {
  float s1 = 0.f, s2 = 0.f;
#pragma unroll
  for (int i = 0; i < 7; ++i) { s1 += __uint_as_float(s.v[i].x); s2 += __uint_as_float(s.v[i].y); }
  s1 = sum32(s1); s2 = sum32(s2);
  Stats o; o.mean = s1 * (1.0f / D_);
  const float var = fmaxf(s2 * (1.0f / D_) - o.mean * o.mean, 0.f);
  o.rs = __builtin_amdgcn_rsqf(var + 1e-5f);
  return o;
}

template <int N> DEV void dma_n(rsrc_t rs, unsigned voff, unsigned lds, unsigned soff) {
  if constexpr (N == 4)
    asm volatile("s_mov_b32 m0, %0\n\ts_nop 0\n\tbuffer_load_dwordx4 %1, %2, %3 offen lds\n\tbuffer_load_dwordx4 %1, %2, %3 offen offset:1024 lds\n\t"
                 "buffer_load_dwordx4 %1, %2, %3 offen offset:2048 lds\n\tbuffer_load_dwordx4 %1, %2, %3 offen offset:3072 lds" ::"s"(lds), "v"(voff), "s"(rs), "s"(soff) : "memory");
  else if constexpr (N == 3)
    asm volatile("s_mov_b32 m0, %0\n\ts_nop 0\n\tbuffer_load_dwordx4 %1, %2, %3 offen lds\n\tbuffer_load_dwordx4 %1, %2, %3 offen offset:1024 lds\n\t"
                 "buffer_load_dwordx4 %1, %2, %3 offen offset:2048 lds" ::"s"(lds), "v"(voff), "s"(rs), "s"(soff) : "memory");
  else
    asm volatile("s_mov_b32 m0, %0\n\ts_nop 0\n\tbuffer_load_dwordx4 %1, %2, %3 offen lds\n\tbuffer_load_dwordx4 %1, %2, %3 offen offset:1024 lds" ::"s"(lds), "v"(voff), "s"(rs), "s"(soff) : "memory");
}
DEV void dma_chunk(rsrc_t rs, unsigned voff, unsigned lds, unsigned soff, int w) {
  const unsigned o = (w - 1) * 9216;
  dma_n<4>(rs, voff, lds + o, soff + o); dma_n<4>(rs, voff, lds + o + 4096, soff + o + 4096);
  if (w == 3) dma_n<2>(rs, voff, lds + o + 8192, soff + o + 8192);
  else asm volatile("s_mov_b32 m0, %0\n\ts_nop 0\n\tbuffer_load_dwordx4 %1, %2, %3 offen lds" ::"s"(lds + o + 8192), "v"(voff), "s"(rs), "s"(soff + o + 8192) : "memory");
}
DEV void dma2(rsrc_t rs, unsigned voff, unsigned lds, unsigned soff) {
  asm volatile("s_mov_b32 m0, %0\n\ts_nop 0\n\t"
               "buffer_load_dwordx4 %1, %2, %3 offen lds\n\t"
               "buffer_load_dwordx4 %1, %2, %3 offen offset:1024 lds"
               ::"s"(lds), "v"(voff), "s"(rs), "s"(soff) : "memory");
}
DEV void bar() { asm volatile("s_barrier" ::: "memory"); }
DEV const LAS char* opq(const LAS char* p) { asm volatile("" : "+v"(p)); return p; }

#define DNW 14
constexpr int DN_WIN = DNW;

__global__ __launch_bounds__(512, 2) void k_main(char* __restrict__ ws, unsigned short* __restrict__ out) {
  extern __shared__ __attribute__((aligned(16))) char smem[];
  const int w8_ = __builtin_amdgcn_readfirstlane(threadIdx.x >> 6);
  const bool helper = w8_ >= 4;
  const int w_ = w8_ & 3;
  const int b = blockIdx.x, idx = b >> 3;
#define MAPA 1
  const int x_ = b & 7;
  const int rb_ = MAPA == 0 ? 2 * x_ + (idx >> 4) : MAPA == 1 ? 4 * (x_ & 3) + (idx >> 3) : 8 * (x_ & 1) + (idx >> 2);
  const int j_ = MAPA == 0 ? (idx & 15) : MAPA == 1 ? 8 * (x_ >> 2) + (idx & 7) : 4 * (x_ >> 1) + (idx & 3);
  const int rp_ = 2 * x_ + (idx >> 4), m_ = idx & 15;
  const bool active = m_ < 14;
  const unsigned lds0 = __builtin_amdgcn_readfirstlane((unsigned)(uintptr_t)smem);
  const rsrc_t rs = __builtin_amdgcn_make_buffer_rsrc(ws, 0, WS_END, 0x00020000);
#define IDS() \
  int w = w_, rb = rb_, j = j_, rp = rp_, mm = m_; asm volatile("" : "+s"(w), "+s"(rb), "+s"(j), "+s"(rp), "+s"(mm)); \
  const int lane = lane_id(), r = lane & 31, h2 = lane >> 5; \
  const bool t0 = w == 0 && lane == 0; (void)t0; \
  const int th = mm >= 7 ? 1 : 0, fs = mm - 7 * th, tt2 = w & 1, kh = w >> 1; \
  const int tt = 4 * rb + w, ttp = 4 * rp + 2 * th + tt2; \
  const unsigned voff = lane * 16; \
  const LAS char* ring = (const LAS char*)smem + lane * 16; \
  LAS char* aux = (LAS char*)smem + AUX; \
  (void)r; (void)h2; (void)fs; (void)kh; (void)tt; (void)ttp; (void)voff; (void)ring; (void)aux; (void)tt2; (void)rp;
#define slot(c) opq(ring + ((c) & 3) * CHB)
#define XLD(p) hld(rs, voff + (p) * 1024, (p) + rot < KSD ? xa : xb)
  constexpr int CSQ_L = 0;
  constexpr int CSG_L = 16384;
  constexpr int WOT_L = 24576;

  if (helper) {
    auto issue = [&](int c, int l) {
      const int sl = c & 3;
      if (c >= 20) { c -= 20; ++l; }
      const bool proj = (c >= 3 && c < 5) || c >= 12;
      int w = w_, j = j_, mm = m_; asm volatile("" : "+s"(w), "+s"(j), "+s"(mm));
      const int fs = mm >= 7 ? mm - 7 : mm;
      unsigned src;
      if (c < 3) src = OFF_WQKV + (j * 3 + c) * CHB;
      else if (c < 5) src = OFF_WO + fs * 65536 + (c - 3) * CHB;
      else if (c < 12) src = OFF_WGU + (j * 7 + c - 5) * CHB;
      else src = OFF_WDN + fs * 229376 + (c - 12) * CHB;
      if (!(l < 2 && (active || !proj))) src = WS_END;
      if (w != 0) dma_chunk(rs, lane_id() * 16, lds0 + sl * CHB, src, w);
    };
    auto stage_csq = [&]() { IDS(); if (w == 3 && lane < 24) *(LAS u32x4*)(aux + CSQ_L + lane * 16) = gld(ws + OFF_CSQ + j * 384 + lane * 16); };
    stage_csq();
    issue(0, 0); issue(1, 0); issue(2, 0);
    Poll pq = {0u, 0u, 0u, 0u};
    u32x4 hd[4][10];
#pragma unroll
    for (int c = 0; c < 4; ++c)
#pragma unroll
      for (int p = 0; p < 10; ++p) hd[c][p] = u32x4{0u, 0u, 0u, 0u};
    auto hold = [&](auto C) {
      constexpr int c = C;
      int w = w_, mm = m_; asm volatile("" : "+s"(w), "+s"(mm));
      if (w == 0 || !active) return;
      const int fs = mm >= 7 ? mm - 7 : mm;
      const char* src = ws + OFF_WDN + (size_t)fs * 229376 + (4 + c) * CHB + (w - 1) * 9216 + lane_id() * 16;
#pragma unroll
      for (int p = 0; p < 9; ++p) hd[c][p] = gld(src + p * 1024);
      if (w == 3) hd[c][9] = gld(src + 9 * 1024);
      SB();
    };
    auto drop = [&](auto C) {
      constexpr int c = C;
      int w = w_; asm volatile("" : "+s"(w));
      if (w == 0) return;
      LAS char* dst = (LAS char*)smem + (c & 3) * CHB + (w - 1) * 9216 + lane_id() * 16;
#pragma unroll
      for (int p = 0; p < 9; ++p) lds_wr(dst, p * 1024, hd[c][p]);
      if (w == 3) lds_wr(dst, 9 * 1024, hd[c][9]);
    };
    using I0 = std::integral_constant<int, 0>; using I1 = std::integral_constant<int, 1>; using I2 = std::integral_constant<int, 2>; using I3 = std::integral_constant<int, 3>;
#pragma unroll 1
    for (int l = 0; l < 2; ++l) {
      IDS();
      const int pw = w == 0 ? 1 : 0;
      poll_ge(pq, ws, FL_X2 + rb * 16 + j, 14, l == 1 ? pw : 0);
      if (w != 0) wait_vm<0>();
      bar_lgkm();
      poll_done(pq, pw);
      bar();
      bar();
      if (w == 1 && lane < 2) sig_add(ws, FL_KV + ((l * 16 + rb) * 16 + j) * 2 + lane);
      if (rb >= 1 && w == 0) poll_two(ws, FL_KV + (rb >= 2 ? ((l * 16 + rb - 2) * 16 + j) * 2 + 1 : ((l * 16 + rb - 1) * 16 + j) * 2), FL_KV + ((l * 16 + rb - 1) * 16 + j) * 2);
      bar();
      if (rb >= 1) {
        u32x4 hv[8];
#pragma unroll
        for (int n = 0; n < 2; ++n) {
          const int rel = 2 * w + n, kt = max(4 * rb - 8 + rel, 0);
#pragma unroll
          for (int q = 0; q < 4; ++q) hv[n * 4 + q] = hld(rs, voff + q * 1024, OFF_KV + l * SZ_KV + (kt * 16 + j) * 4096);
        }
        SB();
#pragma unroll
        for (int n = 0; n < 8; ++n) lds_wr(aux + lane * 16, (2 * w) * 4096 + n * 1024, hv[n]);
      }
      bar_lgkm();
      issue(3, l); issue(4, l); issue(5, l); issue(6, l);
      bar();
      if (w == 1) sig_add16(ws, FL_O + (l * 16 + rb) * 16, lane);
      if (w == 3 && lane < 56) *(LAS u32x4*)(aux + CSG_L + lane * 16) = gld(ws + OFF_CSG + j * 896 + lane * 16);
      if (active) {
        if (w == 1 || w == 2) dma_n<4>(rs, voff, lds0 + AUX + WOT_L + (w - 1) * 4096, OFF_WO + fs * 65536 + 2 * CHB + (w - 1) * 4096);
        poll_ge(pq, ws, FL_O + (l * 16 + rp) * 16 + mm, 16, pw);
        if (w != 0) wait_vm<0>();
        bar_lgkm();
        poll_done(pq, pw);
        bar();
        bar();
        if (w == 1) sig_add16(ws, FL_X1 + (l * 16 + rp) * 16, lane);
      }
      issue(7, l); issue(8, l);
      poll_ge(pq, ws, FL_X1 + (l * 16 + rb) * 16 + j, 14, pw);
      if (w != 0) wait_vm<18>();
      bar_lgkm();
      poll_done(pq, pw);
      wait_vm_bar<9>();
      issue(9, l); issue(10, l);
      wait_vm_bar<18>();
      const bool first = l == 0 && active;
      wait_vm_bar<9>(); issue(11, l);
      if (l == 0) hold(I0{});
      if (first) wait_vm<18>(); else wait_vm<9>();
      bar(); issue(12, l);
      if (l == 0) hold(I1{});
      if (first) wait_vm<27>(); else wait_vm<9>();
      bar(); issue(13, l);
      if (l == 0) hold(I2{});
      bar();
      if (w == 1) sig_add16(ws, FL_H + (l * 16 + rb) * 16, lane);
      issue(14, l);
      if (l == 0) hold(I3{});
      bar();
      if (w == 1) sig_add16(ws, FL_H + 512 + (l * 16 + rb) * 16, lane);
      if (active) {
        issue(15, l);
        poll_ge(pq, ws, FL_H + (l * 16 + rp) * 16 + mm, 16, pw);
        if (first) wait_vm<36>(); else wait_vm<18>();
        bar();
        poll_done(pq, pw);
        bar();
        if (first) wait_vm<18>(); else wait_vm<9>();
        bar();
        drop(I0{});
        poll_ge(pq, ws, FL_H + 512 + (l * 16 + rp) * 16 + mm, 16, pw);
        if (w != 0) wait_vm<0>();
        bar_lgkm();
        poll_done(pq, pw);
        drop(I1{});
        bar_lgkm();
        drop(I2{});
        bar_lgkm();
        drop(I3{});
        bar_lgkm();
        issue(20, l);
        bar_lgkm();
        issue(21, l);
        bar();
        issue(22, l);
        bar();
        bar();
        if (l == 0 && w == 1) sig_add16(ws, FL_X2 + rp * 16, lane);
        if (l == 0) stage_csq();
      } else if (l == 0) { stage_csq(); issue(20, l); issue(21, l); issue(22, l); }
    }
    return;
  }

  { const int l = 0; (void)l; FS(60); }
#pragma unroll 1
  for (int l = 0; l < 2; ++l) {
    u32x4 qf[2];
    {
      IDS();
      bar();
      FS(20);
      constexpr int AH = 10;
      u32x4 af[KSD];
      const unsigned xo = OFF_XA + 2 * l * SZ_XA + tt * (KSD * 1024);
      const int rot = rot_x(j);
      const unsigned xa = xo + rot * 1024, xb = xa - KSD * 1024;
      StatLd sl; stats_issue(sl, rs, OFF_ST + 2 * l * SZ_ST + tt * 3584, lane);
      Stats st;
      static_for<AH>([&](auto I) { constexpr int ks = I; af[ks] = XLD(ks); });
      const LAS char* wk = slot(0); const LAS char* wv = slot(1); const LAS char* wq = slot(2);
      f32x16 ak = {}, av = {};
      constexpr int PF = 3;
      u32x4 rk[PF], rv[PF];
#pragma unroll
      for (int i = 0; i < PF; ++i) { rk[i] = lds_rd(wk, i * 1024); rv[i] = lds_rd(wv, i * 1024); }
      SB();
      FS(21);
      u32x4 rq[4];
      static_for<KSD>([&](auto I) {
        constexpr int ks = I;
        ak = mfma(rk[ks % PF], af[ks], ak); PIN(ak);
        if constexpr (ks + PF < KSD) rk[ks % PF] = lds_rd(wk, (ks + PF) * 1024);
        if constexpr (ks + AH < KSD) af[ks + AH] = XLD(ks + AH);
        if constexpr (ks == 20) { st = stats_finish(sl); PIN(st.mean); PIN(st.rs); }
        SB();
        av = mfma(af[ks], rv[ks % PF], av); PIN(av);
        if constexpr (ks + PF < KSD) rv[ks % PF] = lds_rd(wv, (ks + PF) * 1024);
        if constexpr (ks >= KSD - 4) rq[ks - (KSD - 4)] = lds_rd(wq, (ks - (KSD - 4)) * 1024);
        SB();
      });
      FS(22);
      const LAS float* csl = (const LAS float*)(aux + CSQ_L);
      f32x4 ck[4];
#pragma unroll
      for (int g = 0; g < 4; ++g) ck[g] = *(const LAS f32x4*)(csl + 8 * g + 4 * h2);
      const float csv = csl[32 + r];
      LAS float* stl = (LAS float*)(aux + 8 * 4096 + w * 4096);
      if (h2 == 0) stl[r] = st.rs; else stl[32 + r] = st.mean;
      bar();
      FS(23);
      f32x16 aq = {};
      u32x4 ko[2], vo[2];
      f32x4 srs[2], smn[2];
      f32x4 cq[4];
      float e0, e1;
      const unsigned kvo = OFF_KV + l * SZ_KV + (tt * 16 + j) * 4096;
      static_for<KSD>([&](auto I) {
        constexpr int ks = I;
        aq = mfma(rq[ks & 3], af[ks], aq); PIN(aq);
        if constexpr (ks + 4 < KSD) rq[ks & 3] = lds_rd(wq, (ks + 4) * 1024);
        if constexpr (ks == 0) { srs[0] = *(const LAS f32x4*)(stl + 4 * h2); smn[0] = *(const LAS f32x4*)(stl + 32 + 4 * h2); srs[1] = *(const LAS f32x4*)(stl + 8 + 4 * h2); smn[1] = *(const LAS f32x4*)(stl + 40 + 4 * h2); }
        if constexpr (ks >= 1 && ks < 5) {
          constexpr int g = ks - 1;
          float v[4];
#pragma unroll
          for (int e = 0; e < 4; ++e) { v[e] = ak[4 * g + e] - st.mean * ck[g][e]; PIN(v[e]); }
#pragma unroll
          for (int e = 0; e < 4; ++e) { v[e] *= st.rs; PIN(v[e]); }
          ko[g >> 1][(g & 1) * 2] = pk2(v[0], v[1]); ko[g >> 1][(g & 1) * 2 + 1] = pk2(v[2], v[3]);
          PIN(ko[g >> 1]);
        }
        if constexpr (ks == 5) { hst(rs, voff, kvo, ko[0]); hst(rs, voff + 1024, kvo, ko[1]); }
        if constexpr (ks >= 5 && ks < 13) {
          constexpr int q = ks - 5, g = q >> 1, e = (q & 1) * 2;
          if constexpr ((q & 1) == 0) { PIN(srs[g & 1]); PIN(smn[g & 1]); }
          e0 = av[2 * q] - smn[g & 1][e] * csv; e1 = av[2 * q + 1] - smn[g & 1][e + 1] * csv; PIN(e0); PIN(e1);
          e0 *= srs[g & 1][e]; e1 *= srs[g & 1][e + 1]; PIN(e0); PIN(e1);
          const unsigned pv = pk2(e0, e1);
          vo[q >> 2][q & 3] = r == 28 ? 0x3f803f80u : pv;
          PIN(vo[q >> 2]);
          if constexpr (q == 1 || q == 3) {
            srs[g & 1] = *(const LAS f32x4*)(stl + 8 * (g + 2) + 4 * h2); smn[g & 1] = *(const LAS f32x4*)(stl + 32 + 8 * (g + 2) + 4 * h2);
          }
        }
        if constexpr (ks == 13) { hst(rs, voff + 2048, kvo, vo[0]); hst(rs, voff + 3072, kvo, vo[1]); }
        if constexpr (ks == 14) { LAS char* kvl = aux + (8 + w) * 4096 + lane * 16; lds_wr(kvl, 0, ko[0]); lds_wr(kvl, 1024, ko[1]); lds_wr(kvl, 2048, vo[0]); lds_wr(kvl, 3072, vo[1]); }
        if constexpr (ks >= 16 && ks < 20) cq[ks - 16] = *(const LAS f32x4*)(csl + 64 + 8 * (ks - 16) + 4 * h2);
        if constexpr (ks == 25) { wait_vm_bar<0>(); }
        SB();
      });
      FS(24);
      const float qs = st.rs * (0.18898223650461362f * 1.4426950408889634f);
#pragma unroll
      for (int g = 0; g < 4; ++g) {
        float v[4];
#pragma unroll
        for (int e = 0; e < 4; ++e) { v[e] = aq[4 * g + e] - st.mean * cq[g][e]; PIN(v[e]); v[e] *= qs; PIN(v[e]); }
        qf[g >> 1][(g & 1) * 2] = pk2(v[0], v[1]); qf[g >> 1][(g & 1) * 2 + 1] = pk2(v[2], v[3]);
      }
      FS(25);
    }
    {
      IDS();
      const float NEG = -1e30f;
      f32x16 cd, co;
#pragma unroll
      for (int i = 0; i < 16; ++i) { const int key = (i & 3) + 8 * (i >> 2) + 4 * h2; cd[i] = key <= r ? 0.f : NEG; co[i] = key > r ? 0.f : NEG; }
      PIN(cd); PIN(co);
      bar();
      const LAS char* kw = opq((const LAS char*)aux + lane * 16 + w * 4096);
      bar();
      FS(29);
      float m = NEG;
      f32x16 o = {};
      if (rb >= 2) {
        u32x4 kf[9][2];
#pragma unroll
        for (int it = 0; it < 9; ++it) { kf[it][0] = lds_rd(kw, it * 4096); kf[it][1] = lds_rd(kw, it * 4096 + 1024); }
        SB();
        {
          const f32x16 z = {};
          f32x16 s[3];
          static_for<11>([&](auto IT) {
            constexpr int it = IT;
            if constexpr (it < 9) { s[it % 3] = mfma(kf[it][0], qf[0], it == 0 ? co : it == 8 ? cd : z); s[it % 3] = mfma(kf[it][1], qf[1], s[it % 3]); PIN(s[it % 3]); }
            if constexpr (it >= 2) {
              const f32x16& t = s[(it - 2) % 3];
              float ma = fmaxf(fmaxf(t[0], t[1]), t[2]), mb = fmaxf(fmaxf(t[3], t[4]), t[5]);
              ma = fmaxf(fmaxf(ma, t[6]), t[7]); mb = fmaxf(fmaxf(mb, t[8]), t[9]);
              ma = fmaxf(fmaxf(ma, t[10]), t[11]); mb = fmaxf(fmaxf(mb, t[12]), t[13]);
              ma = fmaxf(fmaxf(ma, t[14]), t[15]);
              m = fmaxf(fmaxf(m, ma), mb);
              PIN(m);
            }
            SB();
          });
        }
        m = -max32(m);
        PIN(m);
        FS(30);
        f32x16 cm;
#pragma unroll
        for (int i = 0; i < 16; ++i) { cm[i] = m; co[i] = co[i] + m; }
        u32x4 vf[2][2];
        vf[0][0] = lds_rd(kw, 2048); vf[0][1] = lds_rd(kw, 3072);
        f32x16 s[3];
        s[0] = mfma(kf[0][0], qf[0], co); s[0] = mfma(kf[0][1], qf[1], s[0]);
        s[1] = mfma(kf[1][0], qf[0], cm); s[1] = mfma(kf[1][1], qf[1], s[1]);
#pragma unroll
        for (int i = 0; i < 16; ++i) cd[i] = cd[i] + m;
        SB();
        static_for<9>([&](auto IT) {
          constexpr int it = IT;
          if constexpr (it < 8) { vf[(it + 1) & 1][0] = lds_rd(kw, (it + 1) * 4096 + 2048); vf[(it + 1) & 1][1] = lds_rd(kw, (it + 1) * 4096 + 3072); }
          u32x4 p[2];
          const f32x16& c = s[it % 3];
          if constexpr (it + 2 < 9) { s[(it + 2) % 3] = mfma(kf[it + 2][0], qf[0], it + 2 == 8 ? cd : cm); }
#pragma unroll
          for (int q = 0; q < 4; ++q) p[0][q] = pk2(__builtin_amdgcn_exp2f(c[2 * q]), __builtin_amdgcn_exp2f(c[2 * q + 1]));
          PIN(p[0]);
          SB();
          if constexpr (it + 2 < 9) { s[(it + 2) % 3] = mfma(kf[it + 2][1], qf[1], s[(it + 2) % 3]); PIN(s[(it + 2) % 3]); }
          o = mfma(vf[it & 1][0], p[0], o);
#pragma unroll
          for (int q = 0; q < 4; ++q) p[1][q] = pk2(__builtin_amdgcn_exp2f(c[8 + 2 * q]), __builtin_amdgcn_exp2f(c[8 + 2 * q + 1]));
          PIN(p[1]);
          SB();
          o = mfma(vf[it & 1][1], p[1], o); PIN(o);
          SB();
        });
      } else {
        const LAS char* kq = kw;
        const f32x16 z = {};
#pragma unroll
        for (int it = 0; it < 9; ++it) {
          if (tt - 8 + it >= 0) {
            f32x16 s = mfma(lds_rd(kq, it * 4096), qf[0], it == 0 ? co : it == 8 ? cd : z);
            s = mfma(lds_rd(kq, it * 4096 + 1024), qf[1], s);
#pragma unroll
            for (int i = 0; i < 16; i += 2) m = fmaxf(fmaxf(m, s[i]), s[i + 1]);
          }
        }
        m = -max32(m);
        f32x16 cm;
#pragma unroll
        for (int i = 0; i < 16; ++i) { cd[i] += m; cm[i] = m; }
#pragma unroll
        for (int it = 0; it < 9; ++it) {
          if (it >= 1 && tt - 8 + it >= 0) {
            f32x16 s = mfma(lds_rd(kq, it * 4096), qf[0], it == 8 ? cd : cm);
            s = mfma(lds_rd(kq, it * 4096 + 1024), qf[1], s);
            u32x4 p[2];
            PACK16(p, pk2(__builtin_amdgcn_exp2f(s[i0]), __builtin_amdgcn_exp2f(s[i1])));
            o = mfma(lds_rd(kq, it * 4096 + 2048), p[0], o);
            o = mfma(lds_rd(kq, it * 4096 + 3072), p[1], o);
          }
        }
      }
      FS(31);
      const unsigned lu = __float_as_uint(o[12]);
      auto rr = __builtin_amdgcn_permlane32_swap(lu, lu, false, false);
      const float inv = __builtin_amdgcn_rcpf(__uint_as_float(rr[1]));
      u32x4 ob[2];
      PACK16(ob, pk2(o[i0] * inv, o[i1] * inv));
      const unsigned oo = OFF_OA + l * SZ_OA + (tt * KSO + 2 * j) * 1024;
      hst(rs, voff, oo, ob[0]); hst(rs, voff + 1024, oo, ob[1]);
      FS(32);
      wait_vm_bar<0>();
      FS(33);
    }
    auto proj_init = [&](int xin) { IDS(); const unsigned xo = OFF_XA + xin * SZ_XA + (ttp * KSD + 4 * fs + 2 * kh) * 1024;
      u32x4 xr[2]; xr[0] = hld(rs, voff, xo); xr[1] = hld(rs, voff + 1024, xo);
      f32x16 a;
#pragma unroll
      for (int q = 0; q < 8; ++q) { const unsigned xv = xr[q >> 2][q & 3]; a[2 * q] = bflo(xv); a[2 * q + 1] = bfhi(xv); }
      PIN(a);
      return a; };
    auto proj_finish = [&](f32x16 own, const f32x16& snd, int xout, bool final_) {
      IDS();
      u32x4 xres[2];
      LAS f32x4* xs = (LAS f32x4*)(aux) + (tt2 * 2 + kh) * 256 + lane;
      LAS f32x4* xrv = (LAS f32x4*)(aux) + (tt2 * 2 + (kh ^ 1)) * 256 + lane;
#pragma unroll
      for (int g = 0; g < 4; ++g) { f32x4 v = {snd[4 * g], snd[4 * g + 1], snd[4 * g + 2], snd[4 * g + 3]}; xs[g * 64] = v; }
      bar_lgkm();
#pragma unroll
      for (int g = 0; g < 4; ++g) { const f32x4 v = xrv[g * 64]; own[4 * g] += v.x; own[4 * g + 1] += v.y; own[4 * g + 2] += v.z; own[4 * g + 3] += v.w; }
#pragma unroll
      for (int q = 0; q < 8; ++q) xres[q >> 2][q & 3] = pk2(own[2 * q], own[2 * q + 1]);
      if (final_) {
        unsigned short* orow = out + (size_t)(ttp * 32 + r) * D_ + 64 * fs + 32 * kh + 4 * h2;
#pragma unroll
        for (int g = 0; g < 4; ++g) { u32x2 v = {xres[g >> 1][(g & 1) * 2], xres[g >> 1][(g & 1) * 2 + 1]}; *(GAS u32x2*)(orow + 8 * g) = v; }
      } else {
        const unsigned xo = OFF_XA + xout * SZ_XA + (ttp * KSD + 4 * fs + 2 * kh) * 1024;
        hst(rs, voff, xo, xres[0]); hst(rs, voff + 1024, xo, xres[1]);
        float s1[4], s2[4];
#pragma unroll
        for (int g = 0; g < 4; ++g) {
          s1[g] = (own[4 * g] + own[4 * g + 1]) + (own[4 * g + 2] + own[4 * g + 3]);
          s2[g] = __builtin_fmaf(own[4 * g], own[4 * g], own[4 * g + 1] * own[4 * g + 1]) + __builtin_fmaf(own[4 * g + 2], own[4 * g + 2], own[4 * g + 3] * own[4 * g + 3]);
        }
        const float t1 = sum32((s1[0] + s1[1]) + (s1[2] + s1[3])), t2 = sum32((s2[0] + s2[1]) + (s2[2] + s2[3]));
        if (h2 == 0) { u32x2 v = {__float_as_uint(t1), __float_as_uint(t2)}; __builtin_amdgcn_raw_buffer_store_b64(v, rs, r * 8, OFF_ST + xout * SZ_ST + (ttp * 14 + 2 * fs + kh) * 256, 16); }
      }
    };
    if (active) {
      IDS();
      f32x16 own = proj_init(2 * l), snd = {};
      bar();
      FS(34);
      u32x4 af[16];
      const unsigned ao = OFF_OA + l * SZ_OA + (ttp * KSO + kh * 16) * 1024;
      constexpr int AH = 10;
      const unsigned oa = ao + 2 * fs * 1024, ob_ = oa - 16 * 1024;
#define OLD(i) hld(rs, voff + (i) * 1024, (i) + 2 * fs < 16 ? oa : ob_)
      static_for<AH>([&](auto I) { constexpr int i = I; af[i] = OLD(i); });
      const LAS char* wbo[3] = {opq(ring + 3 * CHB + kh * 3072), opq(ring + 0 * CHB + kh * 3072), opq((const LAS char*)aux + WOT_L + lane * 16 + kh * 3072)};
      const LAS char* wbs[3] = {opq(ring + 3 * CHB + kh * 2048 + (kh ^ 1) * 1024), opq(ring + 0 * CHB + kh * 2048 + (kh ^ 1) * 1024), opq((const LAS char*)aux + WOT_L + lane * 16 + kh * 2048 + (kh ^ 1) * 1024)};
      constexpr int PF = 2;
      u32x4 wo_[PF], wsn[PF];
#pragma unroll
      for (int i = 0; i < PF; ++i) { wo_[i] = lds_rd(wbo[0], i * 4096); wsn[i] = lds_rd(wbs[0], i * 4096); }
      SB();
      static_for<16>([&](auto I) {
        constexpr int i = I, n = i + PF;
        own = mfma(wo_[i % PF], af[i], own); PIN(own);
        if constexpr (n < 16) wo_[i % PF] = lds_rd(wbo[n / 7], (n % 7) * 4096);
        if constexpr (i + AH < 16) af[i + AH] = OLD(i + AH);
        SB();
        snd = mfma(wsn[i % PF], af[i], snd); PIN(snd);
        if constexpr (n < 16) wsn[i % PF] = lds_rd(wbs[n / 7], (n % 7) * 4096);
        SB();
      });
      FS(35);
      proj_finish(own, snd, 2 * l + 1, false);
      FS(38);
      wait_vm_bar<0>();
      FS(39);
    }
    {
      IDS();
      bar();
      FS(0);
      constexpr int AH = 10;
      u32x4 af[KSD];
      const unsigned xo = OFF_XA + (2 * l + 1) * SZ_XA + tt * (KSD * 1024);
      const int rot = rot_x(j);
      const unsigned xa = xo + rot * 1024, xb = xa - KSD * 1024;
      StatLd sl; stats_issue(sl, rs, OFF_ST + (2 * l + 1) * SZ_ST + tt * 3584, lane);
      Stats st;
      static_for<AH>([&](auto I) { constexpr int ks = I; af[ks] = XLD(ks); });
      const LAS float* csl = (const LAS float*)(aux + CSG_L) + 4 * h2;
      const unsigned ho = OFF_HA + l * SZ_HA + tt * HSTR + 7 * j * 1024;
      f32x16 prev = {}, acc = {};
      u32x4 wr[4];
      {
        const LAS char* w0 = slot(5); const LAS char* w1 = slot(6);
        constexpr int PF = 3;
        u32x4 r0[PF], r1[PF];
#pragma unroll
        for (int i = 0; i < PF; ++i) { r0[i] = lds_rd(w0, i * 1024); r1[i] = lds_rd(w1, i * 1024); }
        SB();
        FS(2);
        static_for<KSD>([&](auto I) {
          constexpr int ks = I;
          prev = mfma(r0[ks % PF], af[ks], prev); PIN(prev);
          if constexpr (ks + PF < KSD) r0[ks % PF] = lds_rd(w0, (ks + PF) * 1024);
          if constexpr (ks + AH < KSD) af[ks + AH] = XLD(ks + AH);
          if constexpr (ks == 20) { st = stats_finish(sl); PIN(st.mean); PIN(st.rs); }
          SB();
          acc = mfma(r1[ks % PF], af[ks], acc); PIN(acc);
          if constexpr (ks + PF < KSD) r1[ks % PF] = lds_rd(w1, (ks + PF) * 1024);
          SB();
        });
      }
      FS(3);
      const float c1 = -1.4426950408889634f * st.rs, vk = __builtin_amdgcn_rcpf(st.rs * st.rs);
      u32x4 ov;
      f32x4 cc[4];
      float ea[8], eb[8], ex[8];
#define GU_STAGE(P, v, S) do { \
        if constexpr ((S) == 0) { ea[v] = P[v] - st.mean * cc[(v) >> 2][(v) & 3]; PIN(ea[v]); eb[v] = P[(v) + 8] - st.mean * cc[2 + ((v) >> 2)][(v) & 3]; PIN(eb[v]); } \
        else if constexpr ((S) == 1) { ex[v] = c1 * ea[v]; PIN(ex[v]); eb[v] = ea[v] * eb[v]; PIN(eb[v]); } \
        else if constexpr ((S) == 2) { ex[v] = __builtin_amdgcn_exp2f(ex[v]); PIN(ex[v]); } \
        else if constexpr ((S) == 3) { ex[v] = __builtin_fmaf(ex[v], vk, vk); PIN(ex[v]); } \
        else if constexpr ((S) == 4) { ex[v] = __builtin_amdgcn_rcpf(ex[v]); PIN(ex[v]); } \
        else { eb[v] = eb[v] * ex[v]; PIN(eb[v]); if constexpr ((v) & 1) { ov[(v) >> 1] = pk2(eb[(v) - 1], eb[v]); PIN(ov); } } } while (0)
#define GU_GAP(P, g, base, sp) static_for<8>([&](auto V_) { constexpr int v_ = V_; constexpr int s_ = (g) - (base) - (sp) * v_; if constexpr (s_ >= 0 && s_ < 6) GU_STAGE(P, v_, s_); })
#define GU_CC(t) _Pragma("unroll") for (int g = 0; g < 4; ++g) cc[g] = *(const LAS f32x4*)(csl + (t) * 32 + 8 * g)
      GU_CC(0);
      bar();
      { const LAS char* w2 = slot(7);
#pragma unroll
        for (int i = 0; i < 4; ++i) wr[i] = lds_rd(w2, i * 1024); }
      SB();
      FS(4);
      f32x16 t1 = acc;
      static_for<5>([&](auto T) {
        constexpr int t = T + 2;
        const LAS char* wl = slot(5 + t);
        const LAS char* wn = slot(6 + t);
        acc = f32x16{};
        static_for<KSD>([&](auto I) {
          constexpr int ks = I;
          if constexpr (ks == 14) {
            if constexpr (t < 6) bar();
            else asm volatile("s_waitcnt vmcnt(0)\n\ts_barrier" ::: "memory");
            SB();
          }
          acc = mfma(wr[ks & 3], af[ks], acc); PIN(acc);
          if constexpr (ks + 4 < KSD) wr[ks & 3] = lds_rd(wl, (ks + 4) * 1024);
          else if constexpr (t < 6) wr[ks & 3] = lds_rd(wn, (ks + 4 - KSD) * 1024);
          if constexpr (t == 2) {
            if constexpr (ks < 14) { GU_GAP(prev, ks, 0, 1); }
            if constexpr (ks == 12) { GU_CC(1); }
            if constexpr (ks == 13) hst(rs, voff, ho, ov);
            if constexpr (ks >= 14) { GU_GAP(t1, ks, 15, 1); }
            if constexpr (ks == 27) { hst(rs, voff + 1024, ho, ov); GU_CC(2); }
          } else {
            GU_GAP(prev, ks, 1, 3);
            if constexpr (ks == 27) { hst(rs, voff + (t - 1) * 1024, ho, ov); GU_CC(t); }
          }
          SB();
        });
        prev = acc;
        FS(3 + t);
      });
      static_for<13>([&](auto G) { constexpr int g = G; GU_GAP(prev, g, 0, 1); });
      hst(rs, voff + 6 * 1024, ho, ov);
      FS(10);
      wait_vm_bar<0>();
      FS(11);
    }
    if (active) {
      IDS();
      f32x16 own = proj_init(2 * l + 1), snd = {};
      bar();
      FS(41);
      constexpr int WIN = DN_WIN;
      constexpr int NR = WIN + 2;
      u32x4 af[NR];
      const unsigned ao = OFF_HA + l * SZ_HA + ttp * HSTR + kh * 56 * 1024;
#define HLD(i) hld(rs, voff + (kk_of(i) % 7) * 1024, ao + (((kk_of(i) / 7) + fs) & 7) * 7168)
      static_for<WIN>([&](auto I) { constexpr int i = I; af[i] = HLD(i); });
      const int oo_ = kh * 3072, so_ = kh * 2048 + (kh ^ 1) * 1024;
      constexpr int PF = 2;
      u32x4 wo_[PF], wsn[PF];
      const LAS char* po = opq(ring + 0 * CHB + oo_); const LAS char* ps = opq(ring + 0 * CHB + so_);
#pragma unroll
      for (int i = 0; i < PF; ++i) { wo_[i] = lds_rd(po, i * 4096); wsn[i] = lds_rd(ps, i * 4096); }
      SB();
      FS(42);
      const LAS char* pon = po; const LAS char* psn = ps;
      static_for<56>([&](auto I) {
        constexpr int i = I, c = i / 7, s = i % 7, n = s + PF;
        if constexpr (s == 0) { po = pon; ps = psn; if constexpr (c < 7) { pon = opq(ring + ((13 + c) & 3) * CHB + oo_); psn = opq(ring + ((13 + c) & 3) * CHB + so_); } }
        if constexpr (i == 1) FS(43);
        if constexpr (i == 28) FS(44);
        if constexpr (s == 3) { bar(); SB(); }
        const u32x4 a = af[i % NR];
        own = mfma(wo_[i % PF], a, own); PIN(own);
        if constexpr (n < 7) wo_[i % PF] = lds_rd(po, n * 4096); else if constexpr (c < 7) wo_[i % PF] = lds_rd(pon, (n - 7) * 4096);
        SB();
        snd = mfma(wsn[i % PF], a, snd); PIN(snd);
        if constexpr (n < 7) wsn[i % PF] = lds_rd(ps, n * 4096); else if constexpr (c < 7) wsn[i % PF] = lds_rd(psn, (n - 7) * 4096);
        if constexpr (i + WIN < 56) af[(i + WIN) % NR] = HLD(i + WIN);
        SB();
      });
      FS(45);
      proj_finish(own, snd, 2, l == 1);
      FS(46);
      wait_vm_bar<0>();
      FS(47);
    }
    if (l == 0) FS(61);
  }
}

}

extern "C" size_t stamp_offset() { return OFF_STAMP; }

extern "C" void kernel_launch(void* const* d_in, const int* in_sizes, int n_in, void* d_out, int out_size, void* d_ws, size_t ws_size,
                              hipStream_t stream) {
  const unsigned short* x = (const unsigned short*)d_in[0];
  const unsigned short* wqkv = (const unsigned short*)d_in[1];
  const unsigned short* wo = (const unsigned short*)d_in[2];
  const unsigned short* w1 = (const unsigned short*)d_in[4];
  const unsigned short* w3 = (const unsigned short*)d_in[5];
  const unsigned short* w2 = (const unsigned short*)d_in[6];
  char* ws = (char*)d_ws;
  (void)hipFuncSetAttribute((const void*)k_main, hipFuncAttributeMaxDynamicSharedMemorySize, LDS_BYTES);
  k_prep<<<PREP_BLOCKS, 256, PREP_LDS, stream>>>(x, wqkv, wo, w1, w3, w2, ws);
  k_main<<<256, 512, LDS_BYTES, stream>>>(ws, (unsigned short*)d_out);
}
```

```cpp
#include <hip/hip_runtime.h>
#include <stdint.h>
#include <utility>

typedef __attribute__((ext_vector_type(8))) __bf16 bf16x8;
typedef __attribute__((ext_vector_type(2))) __bf16 bf16x2;
typedef __attribute__((ext_vector_type(2))) float f32x2;
typedef __attribute__((ext_vector_type(4))) float f32x4;
typedef __attribute__((ext_vector_type(16))) float f32x16;
typedef __attribute__((ext_vector_type(4))) unsigned u32x4;
typedef __attribute__((ext_vector_type(2))) unsigned u32x2;
#define DEV __device__ __forceinline__
#define LAS __attribute__((address_space(3)))
#define GAS __attribute__((address_space(1)))
typedef GAS unsigned gu32;
#define RLX __ATOMIC_RELAXED, __HIP_MEMORY_SCOPE_AGENT

namespace {

constexpr int D_ = 448, F_ = 1792;
constexpr int NTT = 64;
constexpr int KSD = 28;
constexpr int KSO = 32;
constexpr int KSF = 112;
constexpr int CHB = 28672;

constexpr unsigned OFF_FLAG = 0;
constexpr int FL_KV = 0;
constexpr int FL_O = FL_KV + 1024;
constexpr int FL_X1 = FL_O + 512;
constexpr int FL_H = FL_X1 + 512;
constexpr int FL_X2 = FL_H + 1024;
constexpr int FL_TMO = FL_X2 + 256;
constexpr int NFLAG = FL_TMO + 1;
constexpr unsigned OFF_WQKV = 448 * 1024;
constexpr unsigned OFF_WO   = OFF_WQKV + 16 * 3 * CHB;
constexpr unsigned OFF_WGU  = OFF_WO + 7 * 64 * 1024;
constexpr unsigned OFF_WDN  = OFF_WGU + 16 * 7 * CHB;
constexpr unsigned OFF_CSQ  = OFF_WDN + 7 * 224 * 1024;
constexpr unsigned OFF_CSG  = OFF_CSQ + 16 * 3 * 32 * 4;
constexpr unsigned SZ_XA    = NTT * KSD * 1024;
constexpr unsigned OFF_XA   = OFF_CSG + 16 * 7 * 32 * 4;
constexpr unsigned SZ_ST    = NTT * 14 * 32 * 8;
constexpr unsigned OFF_ST   = OFF_XA + 4 * SZ_XA;
constexpr unsigned SZ_KV    = NTT * 16 * 4096;
constexpr unsigned OFF_KV   = OFF_ST + 4 * SZ_ST;
constexpr unsigned SZ_OA    = NTT * KSO * 1024;
constexpr unsigned OFF_OA   = OFF_KV + 2 * SZ_KV;
#define HPAD 0
constexpr unsigned HSTR = KSF * 1024 + HPAD;
constexpr unsigned SZ_HA    = NTT * HSTR;
constexpr unsigned OFF_HA   = OFF_OA + 2 * SZ_OA;
constexpr unsigned WS_END   = OFF_HA + 2 * SZ_HA;
constexpr unsigned OFF_STAMP = (WS_END + 4095) / 4096 * 4096;
static_assert(NFLAG * 128 <= OFF_WQKV, "flags");
static_assert(OFF_XA % 128 == 0 && OFF_ST % 128 == 0 && OFF_KV % 128 == 0 && OFF_OA % 128 == 0 && OFF_HA % 128 == 0, "lines");

#define STAMP(i) do {} while (0)
#define FS(i) do {} while (0)

template <class F, int... I> DEV void static_for_impl(F&& f, std::integer_sequence<int, I...>) { (f(std::integral_constant<int, I>{}), ...); }
template <int N, class F> DEV void static_for(F&& f) { static_for_impl(f, std::make_integer_sequence<int, N>{}); }

DEV int lane_id() { int l; asm volatile("v_mbcnt_lo_u32_b32 %0, -1, 0\n\tv_mbcnt_hi_u32_b32 %0, -1, %0" : "=v"(l)); return l; }
DEV unsigned pk2(float a, float b) { f32x2 v = {a, b}; return __builtin_bit_cast(unsigned, __builtin_convertvector(v, bf16x2)); }
DEV f32x16 mfma(u32x4 a, u32x4 b, f32x16 c) {
  return __builtin_amdgcn_mfma_f32_32x32x16_bf16(__builtin_bit_cast(bf16x8, a), __builtin_bit_cast(bf16x8, b), c, 0, 0, 0);
}
DEV float bflo(unsigned u) { return __uint_as_float(u << 16); }
DEV float bfhi(unsigned u) { return __uint_as_float(u & 0xffff0000u); }
DEV float swap32(float v) {
  const unsigned u = __float_as_uint(v);
  auto rr = __builtin_amdgcn_permlane32_swap(u, u, false, false);
  return __uint_as_float((threadIdx.x & 32) ? rr[0] : rr[1]);
}
DEV float sum32(float v) {
  const unsigned u = __float_as_uint(v);
  auto rr = __builtin_amdgcn_permlane32_swap(u, u, false, false);
  return __uint_as_float(rr[0]) + __uint_as_float(rr[1]);
}
DEV float max32(float v) {
  const unsigned u = __float_as_uint(v);
  auto rr = __builtin_amdgcn_permlane32_swap(u, u, false, false);
  return fmaxf(__uint_as_float(rr[0]), __uint_as_float(rr[1]));
}
DEV void dma7(const char* g, unsigned voff, unsigned lds) {
  unsigned keep;
  const char* g2 = g + 4096;
  lds = __builtin_amdgcn_readfirstlane(lds);
  asm volatile("s_mov_b32 %0, m0\n\ts_mov_b32 m0, %2\n\ts_nop 0\n\t"
               "global_load_lds_dwordx4 %1, %3 offset:0\n\t"
               "global_load_lds_dwordx4 %1, %3 offset:1024\n\t"
               "global_load_lds_dwordx4 %1, %3 offset:2048\n\t"
               "global_load_lds_dwordx4 %1, %3 offset:3072\n\t"
               "s_add_u32 m0, m0, 0x1000\n\ts_nop 0\n\t"
               "global_load_lds_dwordx4 %1, %4 offset:0\n\t"
               "global_load_lds_dwordx4 %1, %4 offset:1024\n\t"
               "global_load_lds_dwordx4 %1, %4 offset:2048\n\t"
               "s_mov_b32 m0, %0"
               : "=&s"(keep) : "v"(voff), "s"(lds), "s"(g), "s"(g2) : "memory", "scc");
}
template <int N> DEV void wait_vm() { asm volatile("s_waitcnt vmcnt(%0)" ::"n"(N) : "memory"); }
template <int N> DEV void wait_vm_bar() { asm volatile("s_waitcnt vmcnt(%0) lgkmcnt(0)\n\ts_barrier" ::"n"(N) : "memory"); }
DEV void bar_lgkm() { asm volatile("s_waitcnt lgkmcnt(0)\n\ts_barrier" ::: "memory"); }
DEV u32x4 lds_rd(const LAS char* base, int off) { return *(const LAS u32x4*)(base + off); }
DEV void lds_wr(LAS char* base, int off, u32x4 v) { *(LAS u32x4*)(base + off) = v; }
DEV u32x4 gld(const char* p) { return *(const GAS u32x4*)(p); }
DEV void gst(char* p, u32x4 v) { *(GAS u32x4*)(p) = v; }
typedef __amdgpu_buffer_rsrc_t rsrc_t;
DEV u32x4 hld(rsrc_t rs, unsigned voff, unsigned soff) { return __builtin_amdgcn_raw_buffer_load_b128(rs, voff, soff, 16); }
DEV void hst(rsrc_t rs, unsigned voff, unsigned soff, u32x4 v) { __builtin_amdgcn_raw_buffer_store_b128(v, rs, voff, soff, 16); }

struct Poll { unsigned v0, v1, v2, v3; };
DEV void poll_ge(Poll& q, char* ws, int flag, unsigned target, int on) {
  const char* c = ws + OFF_FLAG + (size_t)flag * 128;
  unsigned cnt, tmp;
  on = __builtin_amdgcn_readfirstlane(on);
  asm volatile(
      "s_mov_b32 %4, 0\n\t"
      "s_cmp_eq_u32 %9, 0\n\t"
      "s_cbranch_scc1 2f\n"
      "1:\n\t"
      "global_load_dword %0, %6, %7 sc1\n\t"
      "s_waitcnt vmcnt(0)\n\tv_readfirstlane_b32 %5, %0\n\ts_nop 0\n\ts_cmp_ge_u32 %5, %8\n\ts_cbranch_scc1 2f\n\ts_sleep 1\n\t"
      "s_add_u32 %4, %4, 1\n\ts_cmp_lt_u32 %4, 0x8000\n\ts_cbranch_scc1 1b\n"
      "2:"
      : "+v"(q.v0), "+v"(q.v1), "+v"(q.v2), "+v"(q.v3), "=&s"(cnt), "=&s"(tmp)
      : "v"(0u), "s"(c), "s"(target), "s"(on)
      : "memory", "scc");
  if (cnt >= 0x8000u && lane_id() == 0) __hip_atomic_store((gu32*)(ws + OFF_FLAG + (size_t)FL_TMO * 128), 1u, RLX);
}
DEV void poll_two(char* ws, int fa, int fb) {
  const char* c = ws + OFF_FLAG + (size_t)fa * 128;
  const unsigned vo = (lane_id() & 1) * (unsigned)((fb - fa) * 128);
  unsigned cnt, v;
  asm volatile(
      "s_mov_b32 %1, 0\n"
      "1:\n\t"
      "global_load_dword %0, %2, %3 sc1\n\t"
      "s_waitcnt vmcnt(0)\n\tv_cmp_eq_u32 vcc, 0, %0\n\ts_nop 1\n\ts_cbranch_vccz 2f\n\ts_sleep 1\n\t"
      "s_add_u32 %1, %1, 1\n\ts_cmp_lt_u32 %1, 0x8000\n\ts_cbranch_scc1 1b\n"
      "2:"
      : "=&v"(v), "=&s"(cnt) : "v"(vo), "s"(c) : "memory", "scc", "vcc");
  if (cnt >= 0x8000u && lane_id() == 0) __hip_atomic_store((gu32*)(ws + OFF_FLAG + (size_t)FL_TMO * 128), 1u, RLX);
}
DEV void poll_done(Poll& q, int on) {
  on = __builtin_amdgcn_readfirstlane(on);
  asm volatile("s_cmp_eq_u32 %4, 0\n\ts_cbranch_scc1 3f\n\ts_waitcnt vmcnt(0)\n3:" : "+v"(q.v0), "+v"(q.v1), "+v"(q.v2), "+v"(q.v3) : "s"(on) : "scc");
}
DEV void sig_add16(char* ws, int flag, int lane) { if (lane < 16) (void)__hip_atomic_fetch_add((gu32*)(ws + OFF_FLAG + (size_t)(flag + lane) * 128), 1u, RLX); }
DEV void sig_add(char* ws, int flag) { (void)__hip_atomic_fetch_add((gu32*)(ws + OFF_FLAG + (size_t)flag * 128), 1u, RLX); }

struct Stats { float mean, rs; };
DEV Stats load_stats(rsrc_t rs, unsigned off_st, int tt, int lane) {
  const int r = lane & 31, h2 = lane >> 5;
  float s1 = 0.f, s2 = 0.f;
  u32x2 v[7];
#pragma unroll
  for (int i = 0; i < 7; ++i) v[i] = __builtin_amdgcn_raw_buffer_load_b64(rs, h2 * 1792 + r * 8 + i * 256, off_st + tt * 3584, 16);
#pragma unroll
  for (int i = 0; i < 7; ++i) { s1 += __uint_as_float(v[i].x); s2 += __uint_as_float(v[i].y); }
  s1 = sum32(s1); s2 = sum32(s2);
  Stats s; s.mean = s1 * (1.0f / D_);
  const float var = fmaxf(s2 * (1.0f / D_) - s.mean * s.mean, 0.f);
  s.rs = __builtin_amdgcn_rsqf(var + 1e-5f);
  return s;
}
constexpr int G1T = 4, G1S = 8 * G1T;
__host__ __device__ constexpr int kk_of(int i) { return i < G1S ? 7 * (i / G1T) + i % G1T : 7 * ((i - G1S) / (7 - G1T)) + G1T + (i - G1S) % (7 - G1T); }

#define ROTX 0
__host__ __device__ constexpr int rot_x(int j) { return ROTX ? (7 * (j & 7)) >> 1 : 0; }
typedef short s16x4 __attribute__((ext_vector_type(4)));
DEV u32x2 tr_read(const LAS char* p) { return __builtin_bit_cast(u32x2, __builtin_amdgcn_ds_read_tr16_b64_v4i16((LAS s16x4*)p)); }
template <int KIND>
DEV void prep_unit(const unsigned short* __restrict__ src, const unsigned short* __restrict__ src2, int ld, int col, char* __restrict__ dst, float* cs, int a1, int a2, char* smem) {
  const int tid = threadIdx.x, lane = tid & 63, w = tid >> 6, r = lane & 31, h2 = lane >> 5;
  LAS char* img = (LAS char*)smem;
  if (KIND == 0) {
    u32x2 v[14];
    const int p = tid & 7, row0 = tid >> 3;
#pragma unroll
    for (int n = 0; n < 14; ++n) v[n] = p < 7 ? *(const GAS u32x2*)(src + (size_t)(row0 + 32 * n) * ld + col + 4 * p) : u32x2{0u, 0u};
#pragma unroll
    for (int n = 0; n < 14; ++n) *(LAS u32x2*)(img + (row0 + 32 * n) * 64 + p * 8) = v[n];
  } else {
    u32x4 v[7];
    const int p = tid & 3, row0 = tid >> 2;
    const unsigned short* sp = KIND == 1 ? (p < 2 ? src + col + 8 * p : src2 + col + 8 * (p - 2)) : src + col + 8 * p;
#pragma unroll
    for (int n = 0; n < 7; ++n) v[n] = *(const GAS u32x4*)(sp + (size_t)((KIND == 3 ? 448 * (a2 & 3) : 0) + row0 + 64 * n) * ld);
#pragma unroll
    for (int n = 0; n < 7; ++n) {
      const int row = row0 + 64 * n;
      const int irow = KIND == 2 ? 32 * (row / 28) + row % 28 : row;
      *(LAS u32x4*)(img + irow * 64 + p * 16) = v[n];
    }
    if (KIND == 2) { const int head = tid >> 4, d = 28 + ((tid >> 2) & 3); *(LAS u32x4*)(img + (32 * head + d) * 64 + p * 16) = u32x4{0u, 0u, 0u, 0u}; }
  }
  __syncthreads();
  constexpr int NKS = KIND == 2 ? 32 : 28;
  const LAS char* rd = img + (4 * h2 + ((lane & 15) >> 2)) * 64 + ((lane >> 4) & 1) * 32 + (lane & 3) * 8;
  float sum = 0.f;
#pragma unroll
  for (int n = 0; n < NKS / 4; ++n) {
    const int i = w + 4 * n;
    const u32x2 lo = tr_read(rd + i * 1024), hi = tr_read(rd + i * 1024 + 512);
    const u32x4 v = {lo.x, lo.y, hi.x, hi.y};
    size_t doff;
    if (KIND <= 1) doff = (size_t)((i + 28 - a2) % 28) * 1024;
    else if (KIND == 2) doff = (size_t)((((i - 2 * a2) & 15) * 2 + (i >> 4)) * 2 + a1) * 1024;
    else {
      const int ks = 28 * (a2 & 3) + i, kh = ks / 56, kk = ks % 56, j8 = (kk / 7 - (a2 >> 2)) & 7, t = kk % 7;
      const int step = t < G1T ? G1T * j8 + t : G1S + (7 - G1T) * j8 + (t - G1T);
      doff = (size_t)((step * 2 + kh) * 2 + a1) * 1024;
    }
    gst(dst + doff + lane * 16, v);
    if (KIND <= 1) {
#pragma unroll
      for (int q = 0; q < 4; ++q) sum += bflo(v[q]) + bfhi(v[q]);
    }
  }
  if (KIND <= 1) {
    sum = sum32(sum);
    __syncthreads();
    LAS float* red = (LAS float*)img;
    if (h2 == 0) red[w * 32 + r] = sum;
    __syncthreads();
    if (tid < 32) cs[tid] = (red[tid] + red[32 + tid]) + (red[64 + tid] + red[96 + tid]);
  }
}
constexpr int PREP_LDS = 32768;
constexpr int PREP_BLOCKS = 8 * 46;
__global__ __launch_bounds__(256) void k_prep(const unsigned short* __restrict__ x, const unsigned short* __restrict__ wqkv,
                                              const unsigned short* __restrict__ wo, const unsigned short* __restrict__ w1,
                                              const unsigned short* __restrict__ w3, const unsigned short* __restrict__ w2, char* __restrict__ ws) {
  extern __shared__ __attribute__((aligned(16))) char smem[];
  const int b = blockIdx.x, tid = threadIdx.x, lane = tid & 63, w = tid >> 6, r = lane & 31, h2 = lane >> 5;
  const int xg = b & 7, k = b >> 3;
  if (k >= 30) {
    const int u = xg * 16 + (k - 30), tt = u >> 1, hf = u & 1;
    if (u < 14) { const int f = u * 256 + tid; if (f < NFLAG) *(GAS unsigned*)(ws + OFF_FLAG + (size_t)f * 128) = 0u; }
    LAS char* img = (LAS char*)smem;
    u32x4 v[4];
#pragma unroll
    for (int n = 0; n < 4; ++n) { const int c = min(tid + 256 * n, 895); v[n] = *(const GAS u32x4*)(x + (size_t)(tt * 32 + c / 28) * D_ + hf * 224 + (c % 28) * 8); }
#pragma unroll
    for (int n = 0; n < 4; ++n) { const int c = tid + 256 * n; if (c < 896) *(LAS u32x4*)(img + (c / 28) * 464 + (c % 28) * 16) = v[n]; }
    __syncthreads();
#pragma unroll
    for (int n = 0; n < 2; ++n) {
      const int fl = w + 4 * n;
      if (fl < 7) {
        float s1 = 0.f, s2 = 0.f;
#pragma unroll
        for (int s = 0; s < 2; ++s) {
          const int kl = 2 * fl + s;
          const u32x2 a = *(const LAS u32x2*)(img + r * 464 + kl * 32 + 8 * h2), c = *(const LAS u32x2*)(img + r * 464 + kl * 32 + 16 + 8 * h2);
          const u32x4 o = {a.x, a.y, c.x, c.y};
          gst(ws + OFF_XA + ((size_t)tt * KSD + 14 * hf + kl) * 1024 + lane * 16, o);
#pragma unroll
          for (int q = 0; q < 4; ++q) { const float lo = bflo(o[q]), hi = bfhi(o[q]); s1 += lo + hi; s2 += lo * lo + hi * hi; }
        }
        s1 = sum32(s1); s2 = sum32(s2);
        if (h2 == 0) { f32x2 o = {s1, s2}; *(GAS f32x2*)(ws + OFF_ST + ((size_t)tt * 14 + 7 * hf + fl) * 256 + r * 8) = o; }
      }
    }
    return;
  }
  if (k < 14) {
    const int u = xg * 14 + k, j = u / 7, t = u % 7;
    prep_unit<1>(w1, w3, F_, 112 * j + 16 * t, ws + OFF_WGU + (size_t)u * CHB, (float*)(ws + OFF_CSG) + u * 32, 0, rot_x(j), smem);
  } else if (k < 20) {
    const int u = xg * 6 + (k - 14), j = u / 3, p = u % 3;
    prep_unit<0>(wqkv, nullptr, 3 * D_, (p == 2 ? 0 : p == 0 ? D_ : 2 * D_) + 28 * j, ws + OFF_WQKV + (size_t)u * CHB, (float*)(ws + OFF_CSQ) + u * 32, 0, rot_x(j), smem);
  } else if (xg < 7) {
    if (k < 28) { const int fs = xg, tile = (k - 20) & 1, q = (k - 20) >> 1;
      prep_unit<3>(w2, nullptr, D_, 64 * fs + 32 * tile, ws + OFF_WDN + (size_t)fs * 224 * 1024, nullptr, tile, q + 4 * fs, smem);
    } else { const int fs = xg, tile = k - 28;
      prep_unit<2>(wo, nullptr, D_, 64 * fs + 32 * tile, ws + OFF_WO + (size_t)fs * 64 * 1024, nullptr, tile, fs, smem);
    }
  }
}

constexpr int AUX = 4 * CHB;
constexpr int LDS_BYTES = AUX + 49152;
#define SB() __builtin_amdgcn_sched_barrier(0)
#define PIN(x) asm volatile("" : "+v"(x))
#define PACK16(o, expr) _Pragma("unroll") for (int q_ = 0; q_ < 8; ++q_) { const int i0 = 2 * q_, i1 = 2 * q_ + 1; (void)i0; (void)i1; o[q_ >> 2][q_ & 3] = expr; }
template <class T> DEV void opaque_s(T& v) { asm volatile("" : "+s"(v)); }

struct StatLd { u32x2 v[7]; };
DEV void stats_issue(StatLd& s, rsrc_t rs, unsigned soff, int lane) {
  const unsigned vo = (lane >> 5) * 1792 + (lane & 31) * 8;
#pragma unroll
  for (int i = 0; i < 7; ++i) s.v[i] = __builtin_amdgcn_raw_buffer_load_b64(rs, vo + i * 256, soff, 16);
}
DEV Stats stats_finish(const StatLd& s) {
  float s1 = 0.f, s2 = 0.f;
#pragma unroll
  for (int i = 0; i < 7; ++i) { s1 += __uint_as_float(s.v[i].x); s2 += __uint_as_float(s.v[i].y); }
  s1 = sum32(s1); s2 = sum32(s2);
  Stats o; o.mean = s1 * (1.0f / D_);
  const float var = fmaxf(s2 * (1.0f / D_) - o.mean * o.mean, 0.f);
  o.rs = __builtin_amdgcn_rsqf(var + 1e-5f);
  return o;
}

template <int N> DEV void dma_n(rsrc_t rs, unsigned voff, unsigned lds, unsigned soff) {
  if constexpr (N == 4)
    asm volatile("s_mov_b32 m0, %0\n\ts_nop 0\n\tbuffer_load_dwordx4 %1, %2, %3 offen lds\n\tbuffer_load_dwordx4 %1, %2, %3 offen offset:1024 lds\n\t"
                 "buffer_load_dwordx4 %1, %2, %3 offen offset:2048 lds\n\tbuffer_load_dwordx4 %1, %2, %3 offen offset:3072 lds" ::"s"(lds), "v"(voff), "s"(rs), "s"(soff) : "memory");
  else if constexpr (N == 3)
    asm volatile("s_mov_b32 m0, %0\n\ts_nop 0\n\tbuffer_load_dwordx4 %1, %2, %3 offen lds\n\tbuffer_load_dwordx4 %1, %2, %3 offen offset:1024 lds\n\t"
                 "buffer_load_dwordx4 %1, %2, %3 offen offset:2048 lds" ::"s"(lds), "v"(voff), "s"(rs), "s"(soff) : "memory");
  else
    asm volatile("s_mov_b32 m0, %0\n\ts_nop 0\n\tbuffer_load_dwordx4 %1, %2, %3 offen lds\n\tbuffer_load_dwordx4 %1, %2, %3 offen offset:1024 lds" ::"s"(lds), "v"(voff), "s"(rs), "s"(soff) : "memory");
}
DEV void dma_chunk(rsrc_t rs, unsigned voff, unsigned lds, unsigned soff, int w) {
  const unsigned o = (w - 1) * 9216;
  dma_n<4>(rs, voff, lds + o, soff + o); dma_n<4>(rs, voff, lds + o + 4096, soff + o + 4096);
  if (w == 3) dma_n<2>(rs, voff, lds + o + 8192, soff + o + 8192);
  else asm volatile("s_mov_b32 m0, %0\n\ts_nop 0\n\tbuffer_load_dwordx4 %1, %2, %3 offen lds" ::"s"(lds + o + 8192), "v"(voff), "s"(rs), "s"(soff + o + 8192) : "memory");
}
DEV void dma2(rsrc_t rs, unsigned voff, unsigned lds, unsigned soff) {
  asm volatile("s_mov_b32 m0, %0\n\ts_nop 0\n\t"
               "buffer_load_dwordx4 %1, %2, %3 offen lds\n\t"
               "buffer_load_dwordx4 %1, %2, %3 offen offset:1024 lds"
               ::"s"(lds), "v"(voff), "s"(rs), "s"(soff) : "memory");
}
DEV void bar() { asm volatile("s_barrier" ::: "memory"); }
DEV const LAS char* opq(const LAS char* p) { asm volatile("" : "+v"(p)); return p; }

#define DNW 14
constexpr int DN_WIN = DNW;

__global__ __launch_bounds__(512, 2) void k_main(char* __restrict__ ws, unsigned short* __restrict__ out) {
  extern __shared__ __attribute__((aligned(16))) char smem[];
  const int w8_ = __builtin_amdgcn_readfirstlane(threadIdx.x >> 6);
  const bool helper = w8_ >= 4;
  const int w_ = w8_ & 3;
  const int b = blockIdx.x, idx = b >> 3;
#define MAPA 1
  const int x_ = b & 7;
  const int rb_ = MAPA == 0 ? 2 * x_ + (idx >> 4) : MAPA == 1 ? 4 * (x_ & 3) + (idx >> 3) : 8 * (x_ & 1) + (idx >> 2);
  const int j_ = MAPA == 0 ? (idx & 15) : MAPA == 1 ? 8 * (x_ >> 2) + (idx & 7) : 4 * (x_ >> 1) + (idx & 3);
  const int rp_ = 2 * x_ + (idx >> 4), m_ = idx & 15;
  const bool active = m_ < 14;
  const unsigned lds0 = __builtin_amdgcn_readfirstlane((unsigned)(uintptr_t)smem);
  const rsrc_t rs = __builtin_amdgcn_make_buffer_rsrc(ws, 0, WS_END, 0x00020000);
#define IDS() \
  int w = w_, rb = rb_, j = j_, rp = rp_, mm = m_; asm volatile("" : "+s"(w), "+s"(rb), "+s"(j), "+s"(rp), "+s"(mm)); \
  const int lane = lane_id(), r = lane & 31, h2 = lane >> 5; \
  const bool t0 = w == 0 && lane == 0; (void)t0; \
  const int th = mm >= 7 ? 1 : 0, fs = mm - 7 * th, tt2 = w & 1, kh = w >> 1; \
  const int tt = 4 * rb + w, ttp = 4 * rp + 2 * th + tt2; \
  const unsigned voff = lane * 16; \
  const LAS char* ring = (const LAS char*)smem + lane * 16; \
  LAS char* aux = (LAS char*)smem + AUX; \
  (void)r; (void)h2; (void)fs; (void)kh; (void)tt; (void)ttp; (void)voff; (void)ring; (void)aux; (void)tt2; (void)rp;
#define slot(c) opq(ring + ((c) & 3) * CHB)
#define XLD(p) hld(rs, voff + (p) * 1024, (p) + rot < KSD ? xa : xb)
  constexpr int CSQ_L = 0;
  constexpr int CSG_L = 16384;
  constexpr int WOT_L = 24576;

  if (helper) {
    auto issue = [&](int c, int l) {
      const int sl = c & 3;
      if (c >= 20) { c -= 20; ++l; }
      const bool proj = (c >= 3 && c < 5) || c >= 12;
      int w = w_, j = j_, mm = m_; asm volatile("" : "+s"(w), "+s"(j), "+s"(mm));
      const int fs = mm >= 7 ? mm - 7 : mm;
      unsigned src;
      if (c < 3) src = OFF_WQKV + (j * 3 + c) * CHB;
      else if (c < 5) src = OFF_WO + fs * 65536 + (c - 3) * CHB;
      else if (c < 12) src = OFF_WGU + (j * 7 + c - 5) * CHB;
      else src = OFF_WDN + fs * 229376 + (c - 12) * CHB;
      if (!(l < 2 && (active || !proj))) src = WS_END;
      if (w != 0) dma_chunk(rs, lane_id() * 16, lds0 + sl * CHB, src, w);
    };
    auto stage_csq = [&]() { IDS(); if (w == 3 && lane < 24) *(LAS u32x4*)(aux + CSQ_L + lane * 16) = gld(ws + OFF_CSQ + j * 384 + lane * 16); };
    stage_csq();
    issue(0, 0); issue(1, 0); issue(2, 0);
    Poll pq = {0u, 0u, 0u, 0u};
    u32x4 hd[4][10];
#pragma unroll
    for (int c = 0; c < 4; ++c)
#pragma unroll
      for (int p = 0; p < 10; ++p) hd[c][p] = u32x4{0u, 0u, 0u, 0u};
    auto hold = [&](auto C) {
      constexpr int c = C;
      int w = w_, mm = m_; asm volatile("" : "+s"(w), "+s"(mm));
      if (w == 0 || !active) return;
      const int fs = mm >= 7 ? mm - 7 : mm;
      const char* src = ws + OFF_WDN + (size_t)fs * 229376 + (4 + c) * CHB + (w - 1) * 9216 + lane_id() * 16;
#pragma unroll
      for (int p = 0; p < 9; ++p) hd[c][p] = gld(src + p * 1024);
      if (w == 3) hd[c][9] = gld(src + 9 * 1024);
      SB();
    };
    auto drop = [&](auto C) {
      constexpr int c = C;
      int w = w_; asm volatile("" : "+s"(w));
      if (w == 0) return;
      LAS char* dst = (LAS char*)smem + (c & 3) * CHB + (w - 1) * 9216 + lane_id() * 16;
#pragma unroll
      for (int p = 0; p < 9; ++p) lds_wr(dst, p * 1024, hd[c][p]);
      if (w == 3) lds_wr(dst, 9 * 1024, hd[c][9]);
    };
    using I0 = std::integral_constant<int, 0>; using I1 = std::integral_constant<int, 1>; using I2 = std::integral_constant<int, 2>; using I3 = std::integral_constant<int, 3>;
#pragma unroll 1
    for (int l = 0; l < 2; ++l) {
      IDS();
      const int pw = w == 0 ? 1 : 0;
      poll_ge(pq, ws, FL_X2 + rb * 16 + j, 14, l == 1 ? pw : 0);
      if (w != 0) wait_vm<0>();
      bar_lgkm();
      poll_done(pq, pw);
      bar();
      bar();
      if (w == 1 && lane < 2) sig_add(ws, FL_KV + ((l * 16 + rb) * 16 + j) * 2 + lane);
      if (rb >= 1 && w == 0) poll_two(ws, FL_KV + (rb >= 2 ? ((l * 16 + rb - 2) * 16 + j) * 2 + 1 : ((l * 16 + rb - 1) * 16 + j) * 2), FL_KV + ((l * 16 + rb - 1) * 16 + j) * 2);
      bar();
      if (rb >= 1) {
        u32x4 hv[8];
#pragma unroll
        for (int n = 0; n < 2; ++n) {
          const int rel = 2 * w + n, kt = max(4 * rb - 8 + rel, 0);
#pragma unroll
          for (int q = 0; q < 4; ++q) hv[n * 4 + q] = hld(rs, voff + q * 1024, OFF_KV + l * SZ_KV + (kt * 16 + j) * 4096);
        }
        SB();
#pragma unroll
        for (int n = 0; n < 8; ++n) lds_wr(aux + lane * 16, (2 * w) * 4096 + n * 1024, hv[n]);
      }
      bar_lgkm();
      issue(3, l); issue(4, l); issue(5, l); issue(6, l);
      bar();
      if (w == 1) sig_add16(ws, FL_O + (l * 16 + rb) * 16, lane);
      if (w == 3 && lane < 56) *(LAS u32x4*)(aux + CSG_L + lane * 16) = gld(ws + OFF_CSG + j * 896 + lane * 16);
      if (active) {
        if (w == 1 || w == 2) dma_n<4>(rs, voff, lds0 + AUX + WOT_L + (w - 1) * 4096, OFF_WO + fs * 65536 + 2 * CHB + (w - 1) * 4096);
        poll_ge(pq, ws, FL_O + (l * 16 + rp) * 16 + mm, 16, pw);
        if (w != 0) wait_vm<0>();
        bar_lgkm();
        poll_done(pq, pw);
        bar();
        bar();
        if (w == 1) sig_add16(ws, FL_X1 + (l * 16 + rp) * 16, lane);
      }
      issue(7, l); issue(8, l);
      poll_ge(pq, ws, FL_X1 + (l * 16 + rb) * 16 + j, 14, pw);
      if (w != 0) wait_vm<18>();
      bar_lgkm();
      poll_done(pq, pw);
      wait_vm_bar<9>();
      issue(9, l); issue(10, l);
      wait_vm_bar<18>();
      const bool first = l == 0 && active;
      wait_vm_bar<9>(); issue(11, l);
      if (l == 0) hold(I0{});
      if (first) wait_vm<18>(); else wait_vm<9>();
      bar(); issue(12, l);
      if (l == 0) hold(I1{});
      if (first) wait_vm<27>(); else wait_vm<9>();
      bar(); issue(13, l);
      if (l == 0) hold(I2{});
      bar();
      if (w == 1) sig_add16(ws, FL_H + (l * 16 + rb) * 16, lane);
      issue(14, l);
      if (l == 0) hold(I3{});
      bar();
      if (w == 1) sig_add16(ws, FL_H + 512 + (l * 16 + rb) * 16, lane);
      if (active) {
        issue(15, l);
        poll_ge(pq, ws, FL_H + (l * 16 + rp) * 16 + mm, 16, pw);
        if (first) wait_vm<36>(); else wait_vm<18>();
        bar();
        poll_done(pq, pw);
        bar();
        if (first) wait_vm<18>(); else wait_vm<9>();
        bar();
        drop(I0{});
        poll_ge(pq, ws, FL_H + 512 + (l * 16 + rp) * 16 + mm, 16, pw);
        if (w != 0) wait_vm<0>();
        bar_lgkm();
        poll_done(pq, pw);
        drop(I1{});
        bar_lgkm();
        drop(I2{});
        bar_lgkm();
        drop(I3{});
        bar_lgkm();
        issue(20, l);
        bar_lgkm();
        issue(21, l);
        bar();
        issue(22, l);
        bar();
        if (l == 1) bar();
        bar();
        if (l == 0 && w == 1) sig_add16(ws, FL_X2 + rp * 16, lane);
        if (l == 0) stage_csq();
      } else if (l == 0) { stage_csq(); issue(20, l); issue(21, l); issue(22, l); }
    }
    return;
  }

  { const int l = 0; (void)l; FS(60); }
#pragma unroll 1
  for (int l = 0; l < 2; ++l) {
    u32x4 qf[2];
    {
      IDS();
      if (l == 1) bar();
      FS(20);
      constexpr int AH = 10;
      u32x4 af[KSD];
      const unsigned xo = OFF_XA + 2 * l * SZ_XA + tt * (KSD * 1024);
      const int rot = rot_x(j);
      const unsigned xa = xo + rot * 1024, xb = xa - KSD * 1024;
      StatLd sl; stats_issue(sl, rs, OFF_ST + 2 * l * SZ_ST + tt * 3584, lane);
      Stats st;
      static_for<AH>([&](auto I) { constexpr int ks = I; af[ks] = XLD(ks); });
      if (l == 0) bar();
      const LAS char* wk = slot(0); const LAS char* wv = slot(1); const LAS char* wq = slot(2);
      f32x16 ak = {}, av = {};
      constexpr int PF = 3;
      u32x4 rk[PF], rv[PF];
#pragma unroll
      for (int i = 0; i < PF; ++i) { rk[i] = lds_rd(wk, i * 1024); rv[i] = lds_rd(wv, i * 1024); }
      SB();
      FS(21);
      u32x4 rq[4];
      static_for<KSD>([&](auto I) {
        constexpr int ks = I;
        ak = mfma(rk[ks % PF], af[ks], ak); PIN(ak);
        if constexpr (ks + PF < KSD) rk[ks % PF] = lds_rd(wk, (ks + PF) * 1024);
        if constexpr (ks + AH < KSD) af[ks + AH] = XLD(ks + AH);
        if constexpr (ks == 20) { st = stats_finish(sl); PIN(st.mean); PIN(st.rs); }
        SB();
        av = mfma(af[ks], rv[ks % PF], av); PIN(av);
        if constexpr (ks + PF < KSD) rv[ks % PF] = lds_rd(wv, (ks + PF) * 1024);
        if constexpr (ks >= KSD - 4) rq[ks - (KSD - 4)] = lds_rd(wq, (ks - (KSD - 4)) * 1024);
        SB();
      });
      FS(22);
      const LAS float* csl = (const LAS float*)(aux + CSQ_L);
      f32x4 ck[4];
#pragma unroll
      for (int g = 0; g < 4; ++g) ck[g] = *(const LAS f32x4*)(csl + 8 * g + 4 * h2);
      const float csv = csl[32 + r];
      LAS float* stl = (LAS float*)(aux + 8 * 4096 + w * 4096);
      if (h2 == 0) stl[r] = st.rs; else stl[32 + r] = st.mean;
      bar();
      FS(23);
      f32x16 aq = {};
      u32x4 ko[2], vo[2];
      f32x4 srs[2], smn[2];
      f32x4 cq[4];
      float e0, e1;
      const unsigned kvo = OFF_KV + l * SZ_KV + (tt * 16 + j) * 4096;
      static_for<KSD>([&](auto I) {
        constexpr int ks = I;
        aq = mfma(rq[ks & 3], af[ks], aq); PIN(aq);
        if constexpr (ks + 4 < KSD) rq[ks & 3] = lds_rd(wq, (ks + 4) * 1024);
        if constexpr (ks == 0) { srs[0] = *(const LAS f32x4*)(stl + 4 * h2); smn[0] = *(const LAS f32x4*)(stl + 32 + 4 * h2); srs[1] = *(const LAS f32x4*)(stl + 8 + 4 * h2); smn[1] = *(const LAS f32x4*)(stl + 40 + 4 * h2); }
        if constexpr (ks >= 1 && ks < 5) {
          constexpr int g = ks - 1;
          float v[4];
#pragma unroll
          for (int e = 0; e < 4; ++e) { v[e] = ak[4 * g + e] - st.mean * ck[g][e]; PIN(v[e]); }
#pragma unroll
          for (int e = 0; e < 4; ++e) { v[e] *= st.rs; PIN(v[e]); }
          ko[g >> 1][(g & 1) * 2] = pk2(v[0], v[1]); ko[g >> 1][(g & 1) * 2 + 1] = pk2(v[2], v[3]);
          PIN(ko[g >> 1]);
        }
        if constexpr (ks == 5) { hst(rs, voff, kvo, ko[0]); hst(rs, voff + 1024, kvo, ko[1]); }
        if constexpr (ks >= 5 && ks < 13) {
          constexpr int q = ks - 5, g = q >> 1, e = (q & 1) * 2;
          if constexpr ((q & 1) == 0) { PIN(srs[g & 1]); PIN(smn[g & 1]); }
          e0 = av[2 * q] - smn[g & 1][e] * csv; e1 = av[2 * q + 1] - smn[g & 1][e + 1] * csv; PIN(e0); PIN(e1);
          e0 *= srs[g & 1][e]; e1 *= srs[g & 1][e + 1]; PIN(e0); PIN(e1);
          const unsigned pv = pk2(e0, e1);
          vo[q >> 2][q & 3] = r == 28 ? 0x3f803f80u : pv;
          PIN(vo[q >> 2]);
          if constexpr (q == 1 || q == 3) {
            srs[g & 1] = *(const LAS f32x4*)(stl + 8 * (g + 2) + 4 * h2); smn[g & 1] = *(const LAS f32x4*)(stl + 32 + 8 * (g + 2) + 4 * h2);
          }
        }
        if constexpr (ks == 13) { hst(rs, voff + 2048, kvo, vo[0]); hst(rs, voff + 3072, kvo, vo[1]); }
        if constexpr (ks == 14) { LAS char* kvl = aux + (8 + w) * 4096 + lane * 16; lds_wr(kvl, 0, ko[0]); lds_wr(kvl, 1024, ko[1]); lds_wr(kvl, 2048, vo[0]); lds_wr(kvl, 3072, vo[1]); }
        if constexpr (ks >= 16 && ks < 20) cq[ks - 16] = *(const LAS f32x4*)(csl + 64 + 8 * (ks - 16) + 4 * h2);
        if constexpr (ks == 25) { wait_vm_bar<0>(); }
        SB();
      });
      FS(24);
      const float qs = st.rs * (0.18898223650461362f * 1.4426950408889634f);
#pragma unroll
      for (int g = 0; g < 4; ++g) {
        float v[4];
#pragma unroll
        for (int e = 0; e < 4; ++e) { v[e] = aq[4 * g + e] - st.mean * cq[g][e]; PIN(v[e]); v[e] *= qs; PIN(v[e]); }
        qf[g >> 1][(g & 1) * 2] = pk2(v[0], v[1]); qf[g >> 1][(g & 1) * 2 + 1] = pk2(v[2], v[3]);
      }
      FS(25);
    }
    {
      IDS();
      const float NEG = -1e30f;
      f32x16 cd, co;
#pragma unroll
      for (int i = 0; i < 16; ++i) { const int key = (i & 3) + 8 * (i >> 2) + 4 * h2; cd[i] = key <= r ? 0.f : NEG; co[i] = key > r ? 0.f : NEG; }
      PIN(cd); PIN(co);
      bar();
      const LAS char* kw = opq((const LAS char*)aux + lane * 16 + w * 4096);
      bar();
      FS(29);
      float m = NEG;
      f32x16 o = {};
      if (rb >= 2) {
        u32x4 kf[9][2];
#pragma unroll
        for (int it = 0; it < 9; ++it) { kf[it][0] = lds_rd(kw, it * 4096); kf[it][1] = lds_rd(kw, it * 4096 + 1024); }
        SB();
        {
          const f32x16 z = {};
          f32x16 s[3];
          static_for<11>([&](auto IT) {
            constexpr int it = IT;
            if constexpr (it < 9) { s[it % 3] = mfma(kf[it][0], qf[0], it == 0 ? co : it == 8 ? cd : z); s[it % 3] = mfma(kf[it][1], qf[1], s[it % 3]); PIN(s[it % 3]); }
            if constexpr (it >= 2) {
              const f32x16& t = s[(it - 2) % 3];
              float ma = fmaxf(fmaxf(t[0], t[1]), t[2]), mb = fmaxf(fmaxf(t[3], t[4]), t[5]);
              ma = fmaxf(fmaxf(ma, t[6]), t[7]); mb = fmaxf(fmaxf(mb, t[8]), t[9]);
              ma = fmaxf(fmaxf(ma, t[10]), t[11]); mb = fmaxf(fmaxf(mb, t[12]), t[13]);
              ma = fmaxf(fmaxf(ma, t[14]), t[15]);
              m = fmaxf(fmaxf(m, ma), mb);
              PIN(m);
            }
            SB();
          });
        }
        m = -max32(m);
        PIN(m);
        FS(30);
        f32x16 cm;
#pragma unroll
        for (int i = 0; i < 16; ++i) { cm[i] = m; co[i] = co[i] + m; }
        u32x4 vf[2][2];
        vf[0][0] = lds_rd(kw, 2048); vf[0][1] = lds_rd(kw, 3072);
        f32x16 s[3];
        s[0] = mfma(kf[0][0], qf[0], co); s[0] = mfma(kf[0][1], qf[1], s[0]);
        s[1] = mfma(kf[1][0], qf[0], cm); s[1] = mfma(kf[1][1], qf[1], s[1]);
#pragma unroll
        for (int i = 0; i < 16; ++i) cd[i] = cd[i] + m;
        SB();
        static_for<9>([&](auto IT) {
          constexpr int it = IT;
          if constexpr (it < 8) { vf[(it + 1) & 1][0] = lds_rd(kw, (it + 1) * 4096 + 2048); vf[(it + 1) & 1][1] = lds_rd(kw, (it + 1) * 4096 + 3072); }
          u32x4 p[2];
          const f32x16& c = s[it % 3];
          if constexpr (it + 2 < 9) { s[(it + 2) % 3] = mfma(kf[it + 2][0], qf[0], it + 2 == 8 ? cd : cm); }
#pragma unroll
          for (int q = 0; q < 4; ++q) p[0][q] = pk2(__builtin_amdgcn_exp2f(c[2 * q]), __builtin_amdgcn_exp2f(c[2 * q + 1]));
          PIN(p[0]);
          SB();
          if constexpr (it + 2 < 9) { s[(it + 2) % 3] = mfma(kf[it + 2][1], qf[1], s[(it + 2) % 3]); PIN(s[(it + 2) % 3]); }
          o = mfma(vf[it & 1][0], p[0], o);
#pragma unroll
          for (int q = 0; q < 4; ++q) p[1][q] = pk2(__builtin_amdgcn_exp2f(c[8 + 2 * q]), __builtin_amdgcn_exp2f(c[8 + 2 * q + 1]));
          PIN(p[1]);
          SB();
          o = mfma(vf[it & 1][1], p[1], o); PIN(o);
          SB();
        });
      } else {
        const LAS char* kq = kw;
        const f32x16 z = {};
#pragma unroll
        for (int it = 0; it < 9; ++it) {
          if (tt - 8 + it >= 0) {
            f32x16 s = mfma(lds_rd(kq, it * 4096), qf[0], it == 0 ? co : it == 8 ? cd : z);
            s = mfma(lds_rd(kq, it * 4096 + 1024), qf[1], s);
#pragma unroll
            for (int i = 0; i < 16; i += 2) m = fmaxf(fmaxf(m, s[i]), s[i + 1]);
          }
        }
        m = -max32(m);
        f32x16 cm;
#pragma unroll
        for (int i = 0; i < 16; ++i) { cd[i] += m; cm[i] = m; }
#pragma unroll
        for (int it = 0; it < 9; ++it) {
          if (it >= 1 && tt - 8 + it >= 0) {
            f32x16 s = mfma(lds_rd(kq, it * 4096), qf[0], it == 8 ? cd : cm);
            s = mfma(lds_rd(kq, it * 4096 + 1024), qf[1], s);
            u32x4 p[2];
            PACK16(p, pk2(__builtin_amdgcn_exp2f(s[i0]), __builtin_amdgcn_exp2f(s[i1])));
            o = mfma(lds_rd(kq, it * 4096 + 2048), p[0], o);
            o = mfma(lds_rd(kq, it * 4096 + 3072), p[1], o);
          }
        }
      }
      FS(31);
      const unsigned lu = __float_as_uint(o[12]);
      auto rr = __builtin_amdgcn_permlane32_swap(lu, lu, false, false);
      const float inv = __builtin_amdgcn_rcpf(__uint_as_float(rr[1]));
      u32x4 ob[2];
      PACK16(ob, pk2(o[i0] * inv, o[i1] * inv));
      const unsigned oo = OFF_OA + l * SZ_OA + (tt * KSO + 2 * j) * 1024;
      hst(rs, voff, oo, ob[0]); hst(rs, voff + 1024, oo, ob[1]);
      FS(32);
      wait_vm_bar<0>();
      FS(33);
    }
    auto proj_init = [&](int xin) { IDS(); const unsigned xo = OFF_XA + xin * SZ_XA + (ttp * KSD + 4 * fs + 2 * kh) * 1024;
      u32x4 xr[2]; xr[0] = hld(rs, voff, xo); xr[1] = hld(rs, voff + 1024, xo);
      f32x16 a;
#pragma unroll
      for (int q = 0; q < 8; ++q) { const unsigned xv = xr[q >> 2][q & 3]; a[2 * q] = bflo(xv); a[2 * q + 1] = bfhi(xv); }
      PIN(a);
      return a; };
    auto proj_finish = [&](f32x16 own, const f32x16& snd, int xout, bool final_) {
      IDS();
      u32x4 xres[2];
      LAS f32x4* xs = (LAS f32x4*)(aux) + (tt2 * 2 + kh) * 256 + lane;
      LAS f32x4* xrv = (LAS f32x4*)(aux) + (tt2 * 2 + (kh ^ 1)) * 256 + lane;
#pragma unroll
      for (int g = 0; g < 4; ++g) { f32x4 v = {snd[4 * g], snd[4 * g + 1], snd[4 * g + 2], snd[4 * g + 3]}; xs[g * 64] = v; }
      bar_lgkm();
#pragma unroll
      for (int g = 0; g < 4; ++g) { const f32x4 v = xrv[g * 64]; own[4 * g] += v.x; own[4 * g + 1] += v.y; own[4 * g + 2] += v.z; own[4 * g + 3] += v.w; }
#pragma unroll
      for (int q = 0; q < 8; ++q) xres[q >> 2][q & 3] = pk2(own[2 * q], own[2 * q + 1]);
      if (final_) {
        LAS char* img = aux + 16384;
#pragma unroll
        for (int g = 0; g < 4; ++g) { u32x2 v = {xres[g >> 1][(g & 1) * 2], xres[g >> 1][(g & 1) * 2 + 1]}; *(LAS u32x2*)(img + (tt2 * 32 + r) * 144 + kh * 64 + g * 16 + h2 * 8) = v; }
        bar_lgkm();
#pragma unroll
        for (int n = 0; n < 2; ++n) {
          const int row = w * 16 + n * 8 + (lane >> 3);
          const u32x4 v = *(const LAS u32x4*)(img + row * 144 + (lane & 7) * 16);
          *(GAS u32x4*)(out + (size_t)((4 * rp + 2 * th) * 32 + row) * D_ + 64 * fs + (lane & 7) * 8) = v;
        }
      } else {
        const unsigned xo = OFF_XA + xout * SZ_XA + (ttp * KSD + 4 * fs + 2 * kh) * 1024;
        hst(rs, voff, xo, xres[0]); hst(rs, voff + 1024, xo, xres[1]);
        float s1[4], s2[4];
#pragma unroll
        for (int g = 0; g < 4; ++g) {
          s1[g] = (own[4 * g] + own[4 * g + 1]) + (own[4 * g + 2] + own[4 * g + 3]);
          s2[g] = __builtin_fmaf(own[4 * g], own[4 * g], own[4 * g + 1] * own[4 * g + 1]) + __builtin_fmaf(own[4 * g + 2], own[4 * g + 2], own[4 * g + 3] * own[4 * g + 3]);
        }
        const float t1 = sum32((s1[0] + s1[1]) + (s1[2] + s1[3])), t2 = sum32((s2[0] + s2[1]) + (s2[2] + s2[3]));
        if (h2 == 0) { u32x2 v = {__float_as_uint(t1), __float_as_uint(t2)}; __builtin_amdgcn_raw_buffer_store_b64(v, rs, r * 8, OFF_ST + xout * SZ_ST + (ttp * 14 + 2 * fs + kh) * 256, 16); }
      }
    };
    if (active) {
      IDS();
      f32x16 own = proj_init(2 * l), snd = {};
      bar();
      FS(34);
      u32x4 af[16];
      const unsigned ao = OFF_OA + l * SZ_OA + (ttp * KSO + kh * 16) * 1024;
      constexpr int AH = 10;
      const unsigned oa = ao + 2 * fs * 1024, ob_ = oa - 16 * 1024;
#define OLD(i) hld(rs, voff + (i) * 1024, (i) + 2 * fs < 16 ? oa : ob_)
      static_for<AH>([&](auto I) { constexpr int i = I; af[i] = OLD(i); });
      const LAS char* wbo[3] = {opq(ring + 3 * CHB + kh * 3072), opq(ring + 0 * CHB + kh * 3072), opq((const LAS char*)aux + WOT_L + lane * 16 + kh * 3072)};
      const LAS char* wbs[3] = {opq(ring + 3 * CHB + kh * 2048 + (kh ^ 1) * 1024), opq(ring + 0 * CHB + kh * 2048 + (kh ^ 1) * 1024), opq((const LAS char*)aux + WOT_L + lane * 16 + kh * 2048 + (kh ^ 1) * 1024)};
      constexpr int PF = 2;
      u32x4 wo_[PF], wsn[PF];
#pragma unroll
      for (int i = 0; i < PF; ++i) { wo_[i] = lds_rd(wbo[0], i * 4096); wsn[i] = lds_rd(wbs[0], i * 4096); }
      SB();
      static_for<16>([&](auto I) {
        constexpr int i = I, n = i + PF;
        own = mfma(wo_[i % PF], af[i], own); PIN(own);
        if constexpr (n < 16) wo_[i % PF] = lds_rd(wbo[n / 7], (n % 7) * 4096);
        if constexpr (i + AH < 16) af[i + AH] = OLD(i + AH);
        SB();
        snd = mfma(wsn[i % PF], af[i], snd); PIN(snd);
        if constexpr (n < 16) wsn[i % PF] = lds_rd(wbs[n / 7], (n % 7) * 4096);
        SB();
      });
      FS(35);
      proj_finish(own, snd, 2 * l + 1, false);
      FS(38);
      wait_vm_bar<0>();
      FS(39);
    }
    {
      IDS();
      bar();
      FS(0);
      constexpr int AH = 10;
      u32x4 af[KSD];
      const unsigned xo = OFF_XA + (2 * l + 1) * SZ_XA + tt * (KSD * 1024);
      const int rot = rot_x(j);
      const unsigned xa = xo + rot * 1024, xb = xa - KSD * 1024;
      StatLd sl; stats_issue(sl, rs, OFF_ST + (2 * l + 1) * SZ_ST + tt * 3584, lane);
      Stats st;
      static_for<AH>([&](auto I) { constexpr int ks = I; af[ks] = XLD(ks); });
      const LAS float* csl = (const LAS float*)(aux + CSG_L) + 4 * h2;
      const unsigned ho = OFF_HA + l * SZ_HA + tt * HSTR + 7 * j * 1024;
      f32x16 prev = {}, acc = {};
      u32x4 wr[4];
      {
        const LAS char* w0 = slot(5); const LAS char* w1 = slot(6);
        constexpr int PF = 3;
        u32x4 r0[PF], r1[PF];
#pragma unroll
        for (int i = 0; i < PF; ++i) { r0[i] = lds_rd(w0, i * 1024); r1[i] = lds_rd(w1, i * 1024); }
        SB();
        FS(2);
        static_for<KSD>([&](auto I) {
          constexpr int ks = I;
          prev = mfma(r0[ks % PF], af[ks], prev); PIN(prev);
          if constexpr (ks + PF < KSD) r0[ks % PF] = lds_rd(w0, (ks + PF) * 1024);
          if constexpr (ks + AH < KSD) af[ks + AH] = XLD(ks + AH);
          if constexpr (ks == 20) { st = stats_finish(sl); PIN(st.mean); PIN(st.rs); }
          SB();
          acc = mfma(r1[ks % PF], af[ks], acc); PIN(acc);
          if constexpr (ks + PF < KSD) r1[ks % PF] = lds_rd(w1, (ks + PF) * 1024);
          SB();
        });
      }
      FS(3);
      const float c1 = -1.4426950408889634f * st.rs, vk = __builtin_amdgcn_rcpf(st.rs * st.rs);
      u32x4 ov;
      f32x4 cc[4];
      float ea[8], eb[8], ex[8];
#define GU_STAGE(P, v, S) do { \
        if constexpr ((S) == 0) { ea[v] = P[v] - st.mean * cc[(v) >> 2][(v) & 3]; PIN(ea[v]); eb[v] = P[(v) + 8] - st.mean * cc[2 + ((v) >> 2)][(v) & 3]; PIN(eb[v]); } \
        else if constexpr ((S) == 1) { ex[v] = c1 * ea[v]; PIN(ex[v]); eb[v] = ea[v] * eb[v]; PIN(eb[v]); } \
        else if constexpr ((S) == 2) { ex[v] = __builtin_amdgcn_exp2f(ex[v]); PIN(ex[v]); } \
        else if constexpr ((S) == 3) { ex[v] = __builtin_fmaf(ex[v], vk, vk); PIN(ex[v]); } \
        else if constexpr ((S) == 4) { ex[v] = __builtin_amdgcn_rcpf(ex[v]); PIN(ex[v]); } \
        else { eb[v] = eb[v] * ex[v]; PIN(eb[v]); if constexpr ((v) & 1) { ov[(v) >> 1] = pk2(eb[(v) - 1], eb[v]); PIN(ov); } } } while (0)
#define GU_GAP(P, g, base, sp) static_for<8>([&](auto V_) { constexpr int v_ = V_; constexpr int s_ = (g) - (base) - (sp) * v_; if constexpr (s_ >= 0 && s_ < 6) GU_STAGE(P, v_, s_); })
#define GU_CC(t) _Pragma("unroll") for (int g = 0; g < 4; ++g) cc[g] = *(const LAS f32x4*)(csl + (t) * 32 + 8 * g)
      GU_CC(0);
      bar();
      { const LAS char* w2 = slot(7);
#pragma unroll
        for (int i = 0; i < 4; ++i) wr[i] = lds_rd(w2, i * 1024); }
      SB();
      FS(4);
      f32x16 t1 = acc;
      static_for<5>([&](auto T) {
        constexpr int t = T + 2;
        const LAS char* wl = slot(5 + t);
        const LAS char* wn = slot(6 + t);
        acc = f32x16{};
        static_for<KSD>([&](auto I) {
          constexpr int ks = I;
          if constexpr (ks == 14) {
            if constexpr (t < 6) bar();
            else asm volatile("s_waitcnt vmcnt(1)\n\ts_barrier" ::: "memory");
            SB();
          }
          acc = mfma(wr[ks & 3], af[ks], acc); PIN(acc);
          if constexpr (ks + 4 < KSD) wr[ks & 3] = lds_rd(wl, (ks + 4) * 1024);
          else if constexpr (t < 6) wr[ks & 3] = lds_rd(wn, (ks + 4 - KSD) * 1024);
          if constexpr (t == 2) {
            if constexpr (ks < 14) { GU_GAP(prev, ks, 0, 1); }
            if constexpr (ks == 12) { GU_CC(1); }
            if constexpr (ks == 13) hst(rs, voff, ho, ov);
            if constexpr (ks >= 14) { GU_GAP(t1, ks, 15, 1); }
            if constexpr (ks == 27) { hst(rs, voff + 1024, ho, ov); GU_CC(2); }
          } else {
            GU_GAP(prev, ks, 1, 3);
            if constexpr (ks == 27) { hst(rs, voff + (t - 1) * 1024, ho, ov); GU_CC(t); }
          }
          SB();
        });
        prev = acc;
        FS(3 + t);
      });
      static_for<13>([&](auto G) { constexpr int g = G; GU_GAP(prev, g, 0, 1); });
      hst(rs, voff + 6 * 1024, ho, ov);
      FS(10);
      wait_vm_bar<0>();
      FS(11);
    }
    if (active) {
      IDS();
      f32x16 own = proj_init(2 * l + 1), snd = {};
      bar();
      FS(41);
      constexpr int WIN = DN_WIN;
      constexpr int NR = WIN + 2;
      u32x4 af[NR];
      const unsigned ao = OFF_HA + l * SZ_HA + ttp * HSTR + kh * 56 * 1024;
#define HLD(i) hld(rs, voff + (kk_of(i) % 7) * 1024, ao + (((kk_of(i) / 7) + fs) & 7) * 7168)
      static_for<WIN>([&](auto I) { constexpr int i = I; af[i] = HLD(i); });
      const int oo_ = kh * 3072, so_ = kh * 2048 + (kh ^ 1) * 1024;
      constexpr int PF = 2;
      u32x4 wo_[PF], wsn[PF];
      const LAS char* po = opq(ring + 0 * CHB + oo_); const LAS char* ps = opq(ring + 0 * CHB + so_);
#pragma unroll
      for (int i = 0; i < PF; ++i) { wo_[i] = lds_rd(po, i * 4096); wsn[i] = lds_rd(ps, i * 4096); }
      SB();
      FS(42);
      const LAS char* pon = po; const LAS char* psn = ps;
      static_for<56>([&](auto I) {
        constexpr int i = I, c = i / 7, s = i % 7, n = s + PF;
        if constexpr (s == 0) { po = pon; ps = psn; if constexpr (c < 7) { pon = opq(ring + ((13 + c) & 3) * CHB + oo_); psn = opq(ring + ((13 + c) & 3) * CHB + so_); } }
        if constexpr (i == 1) FS(43);
        if constexpr (i == 28) FS(44);
        if constexpr (s == 3) { bar(); SB(); }
        const u32x4 a = af[i % NR];
        own = mfma(wo_[i % PF], a, own); PIN(own);
        if constexpr (n < 7) wo_[i % PF] = lds_rd(po, n * 4096); else if constexpr (c < 7) wo_[i % PF] = lds_rd(pon, (n - 7) * 4096);
        SB();
        snd = mfma(wsn[i % PF], a, snd); PIN(snd);
        if constexpr (n < 7) wsn[i % PF] = lds_rd(ps, n * 4096); else if constexpr (c < 7) wsn[i % PF] = lds_rd(psn, (n - 7) * 4096);
        if constexpr (i + WIN < 56) af[(i + WIN) % NR] = HLD(i + WIN);
        SB();
      });
      FS(45);
      proj_finish(own, snd, 2, l == 1);
      FS(46);
      wait_vm_bar<0>();
      FS(47);
    }
    if (l == 0) FS(61);
  }
}

}

extern "C" size_t stamp_offset() { return OFF_STAMP; }

extern "C" void kernel_launch(void* const* d_in, const int* in_sizes, int n_in, void* d_out, int out_size, void* d_ws, size_t ws_size,
                              hipStream_t stream) {
  const unsigned short* x = (const unsigned short*)d_in[0];
  const unsigned short* wqkv = (const unsigned short*)d_in[1];
  const unsigned short* wo = (const unsigned short*)d_in[2];
  const unsigned short* w1 = (const unsigned short*)d_in[4];
  const unsigned short* w3 = (const unsigned short*)d_in[5];
  const unsigned short* w2 = (const unsigned short*)d_in[6];
  char* ws = (char*)d_ws;
  (void)hipFuncSetAttribute((const void*)k_main, hipFuncAttributeMaxDynamicSharedMemorySize, LDS_BYTES);
  k_prep<<<PREP_BLOCKS, 256, PREP_LDS, stream>>>(x, wqkv, wo, w1, w3, w2, ws);
  k_main<<<256, 512, LDS_BYTES, stream>>>(ws, (unsigned short*)d_out);
}
```

```cpp
#include <hip/hip_runtime.h>
#include <stdint.h>
#include <utility>

typedef __attribute__((ext_vector_type(8))) __bf16 bf16x8;
typedef __attribute__((ext_vector_type(2))) __bf16 bf16x2;
typedef __attribute__((ext_vector_type(2))) float f32x2;
typedef __attribute__((ext_vector_type(4))) float f32x4;
typedef __attribute__((ext_vector_type(16))) float f32x16;
typedef __attribute__((ext_vector_type(4))) unsigned u32x4;
typedef __attribute__((ext_vector_type(2))) unsigned u32x2;
#define DEV __device__ __forceinline__
#define LAS __attribute__((address_space(3)))
#define GAS __attribute__((address_space(1)))
typedef GAS unsigned gu32;
#define RLX __ATOMIC_RELAXED, __HIP_MEMORY_SCOPE_AGENT

#define SB() __builtin_amdgcn_sched_barrier(0)
#define PIN(x) asm volatile("" : "+v"(x))

namespace {

constexpr int D_ = 448, F_ = 1792;
constexpr int NTT = 64;
constexpr int KSD = 28;
constexpr int KSO = 32;
constexpr int KSF = 112;
constexpr int CHB = 28672;

constexpr unsigned OFF_FLAG = 0;
constexpr int FL_GEN = 0;
constexpr int FL_STARTED = 1;
constexpr int FL_START = 16;
constexpr int FL_WG = FL_START + 256;
constexpr int FL_WP = FL_WG + 256;
constexpr int FL_KV = FL_WP + 512;
constexpr int FL_O = FL_KV + 1024;
constexpr int FL_X1 = FL_O + 512;
constexpr int FL_H = FL_X1 + 512;
constexpr int FL_X2 = FL_H + 1024;
constexpr int NFLAG = FL_X2 + 256;
constexpr unsigned OFF_WQKV = 576 * 1024;
constexpr unsigned OFF_WO   = OFF_WQKV + 16 * 3 * CHB;
constexpr unsigned OFF_WGU  = OFF_WO + 7 * 64 * 1024;
constexpr unsigned OFF_WDN  = OFF_WGU + 16 * 7 * CHB;
constexpr unsigned OFF_CSQ  = OFF_WDN + 7 * 224 * 1024;
constexpr unsigned OFF_CSG  = OFF_CSQ + 48 * 512;
constexpr unsigned SZ_XA    = NTT * KSD * 1024;
constexpr unsigned OFF_XA   = OFF_CSG + 112 * 512;
constexpr unsigned SZ_ST    = NTT * 14 * 32 * 8;
constexpr unsigned OFF_ST   = OFF_XA + 4 * SZ_XA;
constexpr unsigned SZ_KV    = NTT * 16 * 4096;
constexpr unsigned OFF_KV   = OFF_ST + 4 * SZ_ST;
constexpr unsigned SZ_OA    = NTT * KSO * 1024;
constexpr unsigned OFF_OA   = OFF_KV + 2 * SZ_KV;
#define HPAD 0
constexpr unsigned HSTR = KSF * 1024 + HPAD;
constexpr unsigned SZ_HA    = NTT * HSTR;
constexpr unsigned OFF_HA   = OFF_OA + 2 * SZ_OA;
constexpr unsigned WS_END   = OFF_HA + 2 * SZ_HA;
constexpr unsigned OFF_STAMP = (WS_END + 4095) / 4096 * 4096;
static_assert(NFLAG * 128 <= OFF_WQKV, "flags");
static_assert(OFF_XA % 128 == 0 && OFF_ST % 128 == 0 && OFF_KV % 128 == 0 && OFF_OA % 128 == 0 && OFF_HA % 128 == 0, "lines");

#define STAMP(i) do {} while (0)
#define FS(i) do {} while (0)
#define FS_L 0

template <class F, int... I> DEV void static_for_impl(F&& f, std::integer_sequence<int, I...>) { (f(std::integral_constant<int, I>{}), ...); }
template <int N, class F> DEV void static_for(F&& f) { static_for_impl(f, std::make_integer_sequence<int, N>{}); }

DEV int lane_id() { int l; asm volatile("v_mbcnt_lo_u32_b32 %0, -1, 0\n\tv_mbcnt_hi_u32_b32 %0, -1, %0" : "=v"(l)); return l; }
DEV unsigned pk2(float a, float b) { f32x2 v = {a, b}; return __builtin_bit_cast(unsigned, __builtin_convertvector(v, bf16x2)); }
DEV f32x16 mfma(u32x4 a, u32x4 b, f32x16 c) {
  return __builtin_amdgcn_mfma_f32_32x32x16_bf16(__builtin_bit_cast(bf16x8, a), __builtin_bit_cast(bf16x8, b), c, 0, 0, 0);
}
DEV float bflo(unsigned u) { return __uint_as_float(u << 16); }
DEV float bfhi(unsigned u) { return __uint_as_float(u & 0xffff0000u); }
DEV float swap32(float v) {
  const unsigned u = __float_as_uint(v);
  auto rr = __builtin_amdgcn_permlane32_swap(u, u, false, false);
  return __uint_as_float((threadIdx.x & 32) ? rr[0] : rr[1]);
}
DEV float sum32(float v) {
  const unsigned u = __float_as_uint(v);
  auto rr = __builtin_amdgcn_permlane32_swap(u, u, false, false);
  return __uint_as_float(rr[0]) + __uint_as_float(rr[1]);
}
DEV float max32(float v) {
  const unsigned u = __float_as_uint(v);
  auto rr = __builtin_amdgcn_permlane32_swap(u, u, false, false);
  return fmaxf(__uint_as_float(rr[0]), __uint_as_float(rr[1]));
}
DEV void dma7(const char* g, unsigned voff, unsigned lds) {
  unsigned keep;
  const char* g2 = g + 4096;
  lds = __builtin_amdgcn_readfirstlane(lds);
  asm volatile("s_mov_b32 %0, m0\n\ts_mov_b32 m0, %2\n\ts_nop 0\n\t"
               "global_load_lds_dwordx4 %1, %3 offset:0\n\t"
               "global_load_lds_dwordx4 %1, %3 offset:1024\n\t"
               "global_load_lds_dwordx4 %1, %3 offset:2048\n\t"
               "global_load_lds_dwordx4 %1, %3 offset:3072\n\t"
               "s_add_u32 m0, m0, 0x1000\n\ts_nop 0\n\t"
               "global_load_lds_dwordx4 %1, %4 offset:0\n\t"
               "global_load_lds_dwordx4 %1, %4 offset:1024\n\t"
               "global_load_lds_dwordx4 %1, %4 offset:2048\n\t"
               "s_mov_b32 m0, %0"
               : "=&s"(keep) : "v"(voff), "s"(lds), "s"(g), "s"(g2) : "memory", "scc");
}
template <int N> DEV void wait_vm() { asm volatile("s_waitcnt vmcnt(%0)" ::"n"(N) : "memory"); }
template <int N> DEV void wait_vm_bar() { asm volatile("s_waitcnt vmcnt(%0) lgkmcnt(0)\n\ts_barrier" ::"n"(N) : "memory"); }
DEV void bar_lgkm() { asm volatile("s_waitcnt lgkmcnt(0)\n\ts_barrier" ::: "memory"); }
DEV u32x4 lds_rd(const LAS char* base, int off) { return *(const LAS u32x4*)(base + off); }
DEV void lds_wr(LAS char* base, int off, u32x4 v) { *(LAS u32x4*)(base + off) = v; }
DEV u32x4 gld(const char* p) { return *(const GAS u32x4*)(p); }
DEV void gst(char* p, u32x4 v) { *(GAS u32x4*)(p) = v; }
typedef __amdgpu_buffer_rsrc_t rsrc_t;
DEV u32x4 hld(rsrc_t rs, unsigned voff, unsigned soff) { return __builtin_amdgcn_raw_buffer_load_b128(rs, voff, soff, 16); }
DEV void hst(rsrc_t rs, unsigned voff, unsigned soff, u32x4 v) { __builtin_amdgcn_raw_buffer_store_b128(v, rs, voff, soff, 16); asm volatile("s_nop 1" ::"v"(v) : "memory"); }

DEV void poll(const char* ws, unsigned vo, unsigned T) {
  unsigned cnt, v;
  asm volatile(
      "s_mov_b32 %1, 0\n"
      "1:\n\t"
      "global_load_dword %0, %2, %3 sc1\n\t"
      "s_waitcnt vmcnt(0)\n\tv_cmp_ne_u32 vcc, %4, %0\n\ts_nop 1\n\ts_cbranch_vccz 2f\n\ts_sleep 1\n\t"
      "s_add_u32 %1, %1, 1\n\ts_cmp_lt_u32 %1, 0x8000\n\ts_cbranch_scc1 1b\n"
      "2:"
      : "=&v"(v), "=&s"(cnt) : "v"(vo), "s"(ws), "s"(T) : "memory", "scc", "vcc");
}
DEV void poll_line(const char* ws, int line, int nwords, unsigned T, int lane) { poll(ws, OFF_FLAG + line * 128 + min(lane, nwords - 1) * 4, T); }
DEV void put(char* ws, unsigned off, unsigned T) { __hip_atomic_store((gu32*)(ws + off), T, RLX); }
DEV void sig16(char* ws, int line0, int stride, int word, unsigned T, int lane) { if (lane < 16) put(ws, OFF_FLAG + (line0 + lane * stride) * 128 + word * 4, T); }

struct Stats { float mean, rs; };
DEV Stats load_stats(rsrc_t rs, unsigned off_st, int tt, int lane) {
  const int r = lane & 31, h2 = lane >> 5;
  float s1 = 0.f, s2 = 0.f;
  u32x2 v[7];
#pragma unroll
  for (int i = 0; i < 7; ++i) v[i] = __builtin_amdgcn_raw_buffer_load_b64(rs, h2 * 1792 + r * 8 + i * 256, off_st + tt * 3584, 16);
#pragma unroll
  for (int i = 0; i < 7; ++i) { s1 += __uint_as_float(v[i].x); s2 += __uint_as_float(v[i].y); }
  s1 = sum32(s1); s2 = sum32(s2);
  Stats s; s.mean = s1 * (1.0f / D_);
  const float var = fmaxf(s2 * (1.0f / D_) - s.mean * s.mean, 0.f);
  s.rs = __builtin_amdgcn_rsqf(var + 1e-5f);
  return s;
}
constexpr int G1T = 4, G1S = 8 * G1T;
__host__ __device__ constexpr int kk_of(int i) { return i < G1S ? 7 * (i / G1T) + i % G1T : 7 * ((i - G1S) / (7 - G1T)) + G1T + (i - G1S) % (7 - G1T); }

typedef short s16x4 __attribute__((ext_vector_type(4)));
DEV u32x2 tr_read(const LAS char* p) { return __builtin_bit_cast(u32x2, __builtin_amdgcn_ds_read_tr16_b64_v4i16((LAS s16x4*)p)); }
DEV u32x4 tr_frag(const LAS char* scr, int lane) {
  const LAS char* rd = scr + (4 * (lane >> 5) + ((lane & 15) >> 2)) * 64 + ((lane >> 4) & 1) * 32 + (lane & 3) * 8;
  const u32x2 lo = tr_read(rd), hi = tr_read(rd + 512);
  return u32x4{lo.x, lo.y, hi.x, hi.y};
}
DEV float frag_sum(u32x4 v) { float t = 0.f;
#pragma unroll
  for (int e = 0; e < 4; ++e) t += bflo(v[e]) + bfhi(v[e]);
  return t; }
template <int KIND>
DEV void unit_load(u32x4 (&v)[8], const unsigned short* __restrict__ src, const unsigned short* __restrict__ src2, int ld, int col, int a2, int q, int lane) {
  constexpr int NI = KIND == 2 ? 8 : 7;
  const int p = lane & 3, rw = lane >> 2;
  const unsigned short* sp = KIND == 1 ? (p < 2 ? src + col + 8 * p : src2 + col + 8 * (p - 2)) : src + col + 8 * p;
#pragma unroll
  for (int n = 0; n < NI; ++n) {
    const int ks = q + 4 * n;
    int row; bool ok = true;
    if (KIND == 2) { const int d = 16 * (ks & 1) + rw; ok = d < 28; row = 28 * (ks >> 1) + (ok ? d : 0); }
    else row = (KIND == 3 ? 448 * (a2 & 3) : 0) + 16 * ks + rw;
    const u32x4 t = *(const GAS u32x4*)(sp + (size_t)row * ld);
    v[n] = ok ? t : u32x4{0u, 0u, 0u, 0u};
  }
}
template <int KIND>
DEV void unit_finish(const u32x4 (&v)[8], rsrc_t rs, unsigned dst, unsigned cs, int a1, int a2, LAS char* scr, int q, int lane) {
  constexpr int NI = KIND == 2 ? 8 : 7;
  float sum = 0.f;
#pragma unroll
  for (int h = 0; h < 2; ++h) {
#pragma unroll
    for (int n = 4 * h; n < 4 * h + 4; ++n) if (n < NI) *(LAS u32x4*)(scr + (n & 3) * 1024 + lane * 16) = v[n];
    asm volatile("" ::: "memory");
#pragma unroll
    for (int n = 4 * h; n < 4 * h + 4; ++n) {
      if (n < NI) {
        const int i = q + 4 * n;
        const u32x4 f = tr_frag(scr + (n & 3) * 1024, lane);
        unsigned doff;
        if (KIND == 1) doff = i * 1024;
        else if (KIND == 2) doff = ((((i - 2 * a2) & 15) * 2 + (i >> 4)) * 2 + a1) * 1024;
        else {
          const int ks = 28 * (a2 & 3) + i, kh = ks / 56, kk = ks % 56, j8 = (kk / 7 - (a2 >> 2)) & 7, t = kk % 7;
          const int step = t < G1T ? G1T * j8 + t : G1S + (7 - G1T) * j8 + (t - G1T);
          doff = ((step * 2 + kh) * 2 + a1) * 1024;
        }
        hst(rs, lane * 16, dst + doff, f);
        if (KIND == 1) sum += frag_sum(f);
      }
    }
    asm volatile("s_waitcnt lgkmcnt(0)" ::: "memory");
  }
  if (KIND == 1) {
    sum = sum32(sum);
    if (lane < 32) __builtin_amdgcn_raw_buffer_store_b32(__float_as_uint(sum), rs, lane * 4, cs + q * 128, 16);
  }
}

constexpr int AUX = 4 * CHB;
constexpr int LDS_BYTES = AUX + 49152;
#define PACK16(o, expr) _Pragma("unroll") for (int q_ = 0; q_ < 8; ++q_) { const int i0 = 2 * q_, i1 = 2 * q_ + 1; (void)i0; (void)i1; o[q_ >> 2][q_ & 3] = expr; }
template <class T> DEV void opaque_s(T& v) { asm volatile("" : "+s"(v)); }

struct StatLd { u32x2 v[7]; };
DEV void stats_issue(StatLd& s, rsrc_t rs, unsigned soff, int lane) {
  const unsigned vo = (lane >> 5) * 1792 + (lane & 31) * 8;
#pragma unroll
  for (int i = 0; i < 7; ++i) s.v[i] = __builtin_amdgcn_raw_buffer_load_b64(rs, vo + i * 256, soff, 16);
}
DEV Stats stats_finish(const StatLd& s) {
  float s1 = 0.f, s2 = 0.f;
#pragma unroll
  for (int i = 0; i < 7; ++i) { s1 += __uint_as_float(s.v[i].x); s2 += __uint_as_float(s.v[i].y); }
  s1 = sum32(s1); s2 = sum32(s2);
  Stats o; o.mean = s1 * (1.0f / D_);
  const float var = fmaxf(s2 * (1.0f / D_) - o.mean * o.mean, 0.f);
  o.rs = __builtin_amdgcn_rsqf(var + 1e-5f);
  return o;
}

template <int N> DEV void dma_n(rsrc_t rs, unsigned voff, unsigned lds, unsigned soff) {
  if constexpr (N == 4)
    asm volatile("s_mov_b32 m0, %0\n\ts_nop 0\n\tbuffer_load_dwordx4 %1, %2, %3 offen lds\n\tbuffer_load_dwordx4 %1, %2, %3 offen offset:1024 lds\n\t"
                 "buffer_load_dwordx4 %1, %2, %3 offen offset:2048 lds\n\tbuffer_load_dwordx4 %1, %2, %3 offen offset:3072 lds" ::"s"(lds), "v"(voff), "s"(rs), "s"(soff) : "memory");
  else if constexpr (N == 3)
    asm volatile("s_mov_b32 m0, %0\n\ts_nop 0\n\tbuffer_load_dwordx4 %1, %2, %3 offen lds\n\tbuffer_load_dwordx4 %1, %2, %3 offen offset:1024 lds\n\t"
                 "buffer_load_dwordx4 %1, %2, %3 offen offset:2048 lds" ::"s"(lds), "v"(voff), "s"(rs), "s"(soff) : "memory");
  else
    asm volatile("s_mov_b32 m0, %0\n\ts_nop 0\n\tbuffer_load_dwordx4 %1, %2, %3 offen lds\n\tbuffer_load_dwordx4 %1, %2, %3 offen offset:1024 lds" ::"s"(lds), "v"(voff), "s"(rs), "s"(soff) : "memory");
}
DEV void dma_chunk(rsrc_t rs, unsigned voff, unsigned lds, unsigned soff, int w) {
  const unsigned o = (w - 1) * 9216;
  dma_n<4>(rs, voff, lds + o, soff + o); dma_n<4>(rs, voff, lds + o + 4096, soff + o + 4096);
  if (w == 3) dma_n<2>(rs, voff, lds + o + 8192, soff + o + 8192);
  else asm volatile("s_mov_b32 m0, %0\n\ts_nop 0\n\tbuffer_load_dwordx4 %1, %2, %3 offen lds" ::"s"(lds + o + 8192), "v"(voff), "s"(rs), "s"(soff + o + 8192) : "memory");
}
DEV void dma2(rsrc_t rs, unsigned voff, unsigned lds, unsigned soff) {
  asm volatile("s_mov_b32 m0, %0\n\ts_nop 0\n\t"
               "buffer_load_dwordx4 %1, %2, %3 offen lds\n\t"
               "buffer_load_dwordx4 %1, %2, %3 offen offset:1024 lds"
               ::"s"(lds), "v"(voff), "s"(rs), "s"(soff) : "memory");
}
DEV void bar() { asm volatile("s_barrier" ::: "memory"); }
DEV const LAS char* opq(const LAS char* p) { asm volatile("" : "+v"(p)); return p; }

#define DNW 14
constexpr int DN_WIN = DNW;

__global__ __launch_bounds__(512, 2) void k_main(char* __restrict__ ws, unsigned short* __restrict__ out, const unsigned short* __restrict__ x, const unsigned short* __restrict__ wqkv,
                                                  const unsigned short* __restrict__ wo, const unsigned short* __restrict__ w1, const unsigned short* __restrict__ w3, const unsigned short* __restrict__ w2) {
  extern __shared__ __attribute__((aligned(16))) char smem[];
  const int w8_ = __builtin_amdgcn_readfirstlane(threadIdx.x >> 6);
  const bool helper = w8_ >= 4;
  const int w_ = w8_ & 3;
  const int b = blockIdx.x, idx = b >> 3;
#define MAPA 1
  const int x_ = b & 7;
  const int rb_ = MAPA == 0 ? 2 * x_ + (idx >> 4) : MAPA == 1 ? 4 * (x_ & 3) + (idx >> 3) : 8 * (x_ & 1) + (idx >> 2);
  const int j_ = MAPA == 0 ? (idx & 15) : MAPA == 1 ? 8 * (x_ >> 2) + (idx & 7) : 4 * (x_ >> 1) + (idx & 3);
  const int rp_ = 2 * x_ + (idx >> 4), m_ = idx & 15;
  const bool active = m_ < 14;
  const unsigned lds0 = __builtin_amdgcn_readfirstlane((unsigned)(uintptr_t)smem);
  const rsrc_t rs = __builtin_amdgcn_make_buffer_rsrc(ws, 0, WS_END, 0x00020000);
#define IDS() \
  int w = w_, rb = rb_, j = j_, rp = rp_, mm = m_; asm volatile("" : "+s"(w), "+s"(rb), "+s"(j), "+s"(rp), "+s"(mm)); \
  const int lane = lane_id(), r = lane & 31, h2 = lane >> 5; \
  const bool t0 = w == 0 && lane == 0; (void)t0; \
  const int th = mm >= 7 ? 1 : 0, fs = mm - 7 * th, tt2 = w & 1, kh = w >> 1; \
  const int tt = 4 * rb + w, ttp = 4 * rp + 2 * th + tt2; \
  const unsigned voff = lane * 16; \
  const LAS char* ring = (const LAS char*)smem + lane * 16; \
  LAS char* aux = (LAS char*)smem + AUX; \
  (void)r; (void)h2; (void)fs; (void)kh; (void)tt; (void)ttp; (void)voff; (void)ring; (void)aux; (void)tt2; (void)rp;
#define slot(c) opq(ring + ((c) & 3) * CHB)
#define XLD(p) hld(rs, voff + (p) * 1024, xo)
  constexpr int CSQ_L = 0;
  constexpr int CSG_L = 16384;
  constexpr int WOT_L = 24576;

  { const int l = FS_L; (void)l; FS(48); }
  if (helper) {
    auto issue = [&](int c, int l) {
      const int sl = c & 3;
      if (c >= 20) { c -= 20; ++l; }
      const bool proj = (c >= 3 && c < 5) || c >= 12;
      int w = w_, j = j_, mm = m_; asm volatile("" : "+s"(w), "+s"(j), "+s"(mm));
      const int fs = mm >= 7 ? mm - 7 : mm;
      unsigned src;
      if (c < 3) src = OFF_WQKV + (j * 3 + c) * CHB;
      else if (c < 5) src = OFF_WO + fs * 65536 + (c - 3) * CHB;
      else if (c < 12) src = OFF_WGU + (j * 7 + c - 5) * CHB;
      else src = OFF_WDN + fs * 229376 + (c - 12) * CHB;
      if (!(l < 2 && (active || !proj))) src = WS_END;
      if (w != 0) dma_chunk(rs, lane_id() * 16, lds0 + sl * CHB, src, w);
    };
    unsigned tv = __hip_atomic_load((gu32*)(ws + OFF_FLAG + FL_GEN * 128), RLX);
    {
      const int lane = lane_id(), q = w_, j = j_;
      const rsrc_t rq = __builtin_amdgcn_make_buffer_rsrc((void*)wqkv, 0, D_ * 3 * D_ * 2, 0x00020000);
      const unsigned wvo = (lane >> 2) * (3 * D_ * 2) + (lane & 3) * 16;
#pragma unroll
      for (int p = 0; p < 3; ++p)
#pragma unroll
        for (int n = 0; n < 7; ++n)
          asm volatile("s_mov_b32 m0, %0\n\ts_nop 0\n\tbuffer_load_dwordx4 %1, %2, %3 offen lds"
                       ::"s"(lds0 + p * CHB + (q + 4 * n) * 1024), "v"(wvo), "s"(rq), "s"(16 * (q + 4 * n) * (3 * D_ * 2) + 56 * j - 8 * (j & 1) + (p == 2 ? 0 : (p + 1) * D_ * 2)) : "memory");
      SB();
      wait_vm<0>();
#pragma unroll
      for (int p = 0; p < 3; ++p) {
        float sum = 0.f;
        u32x4 fr[7];
#pragma unroll
        for (int n = 0; n < 7; ++n) fr[n] = tr_frag((const LAS char*)smem + p * CHB + (q + 4 * n) * 1024 + 8 * (j & 1), lane);
        asm volatile("s_waitcnt lgkmcnt(0)" ::: "memory");
#pragma unroll
        for (int n = 0; n < 7; ++n) {
          u32x4 f = fr[n];
          if ((lane & 31) >= 28) f = u32x4{0u, 0u, 0u, 0u};
          *(LAS u32x4*)((LAS char*)smem + p * CHB + (q + 4 * n) * 1024 + lane * 16) = f;
          if (rb_ == 0) hst(rs, lane * 16, OFF_WQKV + (j * 3 + p) * CHB + (q + 4 * n) * 1024, f);
          sum += frag_sum(f);
        }
        sum = sum32(sum);
        if (lane < 32) { *(LAS float*)((LAS char*)smem + AUX + 512 + (p * 4 + q) * 128 + lane * 4) = sum;
          if (rb_ == 0) __builtin_amdgcn_raw_buffer_store_b32(__float_as_uint(sum), rs, lane * 4, OFF_CSQ + (j * 3 + p) * 512 + q * 128, 16); }
      }
    }
    PIN(tv);
    const unsigned T = __builtin_amdgcn_readfirstlane(tv) + 1u;
    auto stage_cs = [&](unsigned src, int n16, int dst) {
      const int lane = lane_id();
      if (lane < n16) {
        f32x4 a = {0.f, 0.f, 0.f, 0.f};
#pragma unroll
        for (int q = 0; q < 4; ++q) a += __builtin_bit_cast(f32x4, hld(rs, (lane >> 3) * 512 + (lane & 7) * 16 + q * 128, src));
        *(LAS f32x4*)((LAS char*)smem + AUX + dst + lane * 16) = a;
      }
    };
    auto stage_csq = [&]() { if (w_ == 3) stage_cs(OFF_CSQ + j_ * 3 * 512, 24, CSQ_L); };
    u32x4 hd[4][10];
#pragma unroll
    for (int c = 0; c < 4; ++c)
#pragma unroll
      for (int p = 0; p < 10; ++p) hd[c][p] = u32x4{0u, 0u, 0u, 0u};
    auto hold = [&](auto C) {
      constexpr int c = C;
      int w = w_, mm = m_; asm volatile("" : "+s"(w), "+s"(mm));
      if (w == 0 || !active) return;
      const int fs = mm >= 7 ? mm - 7 : mm;
      const char* src = ws + OFF_WDN + (size_t)fs * 229376 + (4 + c) * CHB + (w - 1) * 9216 + lane_id() * 16;
#pragma unroll
      for (int p = 0; p < 9; ++p) hd[c][p] = gld(src + p * 1024);
      if (w == 3) hd[c][9] = gld(src + 9 * 1024);
      SB();
    };
    auto drop = [&](auto C) {
      constexpr int c = C;
      int w = w_; asm volatile("" : "+s"(w));
      if (w == 0) return;
      LAS char* dst = (LAS char*)smem + (c & 3) * CHB + (w - 1) * 9216 + lane_id() * 16;
#pragma unroll
      for (int p = 0; p < 9; ++p) lds_wr(dst, p * 1024, hd[c][p]);
      if (w == 3) lds_wr(dst, 9 * 1024, hd[c][9]);
    };
    using I0 = std::integral_constant<int, 0>; using I1 = std::integral_constant<int, 1>; using I2 = std::integral_constant<int, 2>; using I3 = std::integral_constant<int, 3>;
#pragma unroll 1
    for (int l = 0; l < 2; ++l) {
      IDS();
      if (l == 0) {
        FS(54);
        bar_lgkm();
        FS(55);
        if (w == 1 && lane == 0) put(ws, OFF_FLAG + FL_STARTED * 128 + b * 4, T);
        if (w == 3 && lane < 24) {
          const LAS char* ps = (const LAS char*)aux + 512 + (lane >> 3) * 512 + (lane & 7) * 16;
          const f32x4 a = (*(const LAS f32x4*)(ps) + *(const LAS f32x4*)(ps + 128)) + (*(const LAS f32x4*)(ps + 256) + *(const LAS f32x4*)(ps + 384));
          *(LAS f32x4*)(aux + CSQ_L + lane * 16) = a;
        }
        bar_lgkm();
        wait_vm<0>();
        if (rb == 0) { if (lane < 48) put(ws, OFF_FLAG + (FL_START + (lane & 15) * 16 + j) * 128 + ((lane >> 4) * 4 + w) * 4, T); }
        if (w == 2) {
          poll(ws, OFF_FLAG + (lane < 12 ? (FL_START + rb * 16 + j) * 128 + lane * 4 : (FL_WG + rb * 16 + j) * 128 + min(lane - 12, 27) * 4), T);
          if (active) poll(ws, OFF_FLAG + (FL_WP + (rp * 16 + mm) * 2) * 128 + (lane < 32 ? lane * 4 : 128 + min(lane - 32, 7) * 4), T);
          FS(56);
          __builtin_amdgcn_fence(__ATOMIC_ACQUIRE, "agent");
        }
      } else {
        if (w == 0) poll_line(ws, FL_X2 + rb * 16 + j, 14, T, lane); else wait_vm<0>();
        bar_lgkm();
        bar();
      }
      bar();
      if (w == 1 && lane < 4) put(ws, OFF_FLAG + (FL_KV + ((l * 16 + rb) * 16 + j) * 2 + (lane >> 1)) * 128 + (lane & 1) * 4, T);
      if (w == 0) {
        if (rb >= 1) {
          const int far = (lane & 2) && rb >= 2;
          poll(ws, OFF_FLAG + (FL_KV + ((l * 16 + rb - 1 - far) * 16 + j) * 2 + far) * 128 + (lane & 1) * 4, T);
        }
      }
      bar();
      if (rb >= 1) {
        u32x4 hv[8];
#pragma unroll
        for (int n = 0; n < 2; ++n) {
          const int rel = 2 * w + n, kt = max(4 * rb - 8 + rel, 0);
#pragma unroll
          for (int q = 0; q < 4; ++q) hv[n * 4 + q] = hld(rs, voff + q * 1024, OFF_KV + l * SZ_KV + (kt * 16 + j) * 4096);
        }
        SB();
#pragma unroll
        for (int n = 0; n < 8; ++n) lds_wr(aux + lane * 16, (2 * w) * 4096 + n * 1024, hv[n]);
      }
      if (w == 2) wait_vm<0>();
      bar_lgkm();
      issue(3, l); issue(4, l); issue(5, l); issue(6, l);
      bar();
      if (w == 1) sig16(ws, FL_O + (l * 16 + rb) * 16, 1, j, T, lane);
      if (w == 3) stage_cs(OFF_CSG + j * 7 * 512, 56, CSG_L);
      if (active) {
        if (w == 1 || w == 2) dma_n<4>(rs, voff, lds0 + AUX + WOT_L + (w - 1) * 4096, OFF_WO + fs * 65536 + 2 * CHB + (w - 1) * 4096);
        if (w == 0) poll_line(ws, FL_O + (l * 16 + rp) * 16 + mm, 16, T, lane);
        if (w != 0) wait_vm<0>();
        bar_lgkm();
        bar();
        bar();
        if (w == 1) sig16(ws, FL_X1 + (l * 16 + rp) * 16, 1, mm, T, lane);
      }
      issue(7, l); issue(8, l);
      if (w == 0) poll_line(ws, FL_X1 + (l * 16 + rb) * 16 + j, 14, T, lane);
      if (w != 0) wait_vm<18>();
      bar_lgkm();
      wait_vm_bar<9>();
      issue(9, l); issue(10, l);
      wait_vm_bar<18>();
      const bool first = l == 0 && active;
      wait_vm_bar<9>(); issue(11, l);
      if (l == 0) hold(I0{});
      if (first) wait_vm<18>(); else wait_vm<9>();
      bar(); issue(12, l);
      if (l == 0) hold(I1{});
      if (first) wait_vm<27>(); else wait_vm<9>();
      bar(); issue(13, l);
      if (l == 0) hold(I2{});
      bar();
      if (w == 1) sig16(ws, FL_H + (l * 16 + rb) * 16, 1, j, T, lane);
      issue(14, l);
      if (l == 0) hold(I3{});
      bar();
      if (w == 1) sig16(ws, FL_H + 512 + (l * 16 + rb) * 16, 1, j, T, lane);
      if (active) {
        issue(15, l);
        if (w == 0) poll_line(ws, FL_H + (l * 16 + rp) * 16 + mm, 16, T, lane);
        if (first) wait_vm<36>(); else wait_vm<18>();
        bar();
        bar();
        if (first) wait_vm<18>(); else wait_vm<9>();
        bar();
        drop(I0{});
        if (w == 0) poll_line(ws, FL_H + 512 + (l * 16 + rp) * 16 + mm, 16, T, lane);
        if (w != 0) wait_vm<0>();
        bar_lgkm();
        drop(I1{});
        bar_lgkm();
        drop(I2{});
        bar_lgkm();
        drop(I3{});
        bar_lgkm();
        issue(20, l);
        bar_lgkm();
        issue(21, l);
        bar();
        issue(22, l);
        bar();
        if (l == 1) bar();
        bar();
        if (l == 0 && w == 1) sig16(ws, FL_X2 + rp * 16, 1, mm, T, lane);
        if (l == 0) stage_csq();
      } else if (l == 0) { stage_csq(); issue(20, l); issue(21, l); issue(22, l); }
      else if (b == 112 && w == 0) {
#pragma unroll 1
        for (int n = 0; n < 4; ++n) poll(ws, OFF_FLAG + FL_STARTED * 128 + (n * 64 + lane) * 4, T);
        if (lane == 0) put(ws, OFF_FLAG + FL_GEN * 128, T);
      }
    }
    return;
  }

  { const int l = 0; (void)l; FS(60); }
#pragma unroll 1
  for (int l = 0; l < 2; ++l) {
    u32x4 qf[2];
    {
      IDS();
      if (l == 1) bar();
      FS(20);
      constexpr int AH = 10;
      u32x4 af[KSD];
      const unsigned xo = OFF_XA + 2 * l * SZ_XA + tt * (KSD * 1024);
      Stats st;
      const LAS char* wk = slot(0); const LAS char* wv = slot(1); const LAS char* wq = slot(2);
      f32x16 ak = {}, av = {};
      u32x4 rq[4];
      auto passA = [&](auto FIRST) {
        StatLd sl;
        if constexpr (FIRST) {
#pragma unroll
          for (int m = 0; m < 4; ++m)
#pragma unroll
            for (int n = 0; n < 7; ++n) { const int c = 64 * n + lane; af[7 * m + n] = *(const GAS u32x4*)(x + (size_t)(tt * 32 + c / 14) * D_ + 112 * m + (c % 14) * 8); }
          const unsigned tv = __hip_atomic_load((gu32*)(ws + OFF_FLAG + FL_GEN * 128), RLX);
          const int uu = x_ * 14 + idx, uj = uu / 7, ut = uu % 7;
          const int ukind = idx < 14 ? 1 : (x_ < 7 && idx < 22) ? 3 : (x_ < 7 && idx < 24) ? 2 : 0;
          u32x4 uv[8];
#pragma unroll
          for (int n = 0; n < 8; ++n) uv[n] = u32x4{0u, 0u, 0u, 0u};
          if (ukind == 1) unit_load<1>(uv, w1, w3, F_, 112 * uj + 16 * ut, 0, w, lane);
          else if (ukind == 3) unit_load<3>(uv, w2, nullptr, D_, 64 * x_ + 32 * ((idx - 14) & 1), (idx - 14) >> 1, w, lane);
          else if (ukind == 2) unit_load<2>(uv, wo, nullptr, D_, 64 * x_ + 32 * (idx - 22), 0, w, lane);
          SB();
          LAS char* scr = w < 3 ? (LAS char*)smem + 3 * CHB + w * 7680 : aux + 40960;
          float s1a = 0.f, s1b = 0.f, s2a = 0.f, s2b = 0.f;
#pragma unroll
          for (int m = 0; m < 4; ++m) {
#pragma unroll
            for (int n = 0; n < 7; ++n) { const int c = 64 * n + lane; *(LAS u32x4*)(scr + (c / 14) * 240 + (c % 14) * 16) = af[7 * m + n]; }
            asm volatile("" ::: "memory");
#pragma unroll
            for (int n = 0; n < 7; ++n) {
              const u32x2 a = *(const LAS u32x2*)(scr + r * 240 + n * 32 + 8 * h2), c = *(const LAS u32x2*)(scr + r * 240 + n * 32 + 16 + 8 * h2);
              af[7 * m + n] = u32x4{a.x, a.y, c.x, c.y};
            }
            asm volatile("s_waitcnt lgkmcnt(0)" ::: "memory");
#pragma unroll
            for (int n = 0; n < 7; ++n)
#pragma unroll
              for (int e = 0; e < 4; ++e) { const float lo = bflo(af[7 * m + n][e]), hi = bfhi(af[7 * m + n][e]); s1a += lo; s1b += hi; s2a = __builtin_fmaf(lo, lo, s2a); s2b = __builtin_fmaf(hi, hi, s2b); }
            SB();
          }
          const float s1 = sum32(s1a + s1b), s2 = sum32(s2a + s2b);
          st.mean = s1 * (1.0f / D_);
          st.rs = __builtin_amdgcn_rsqf(fmaxf(s2 * (1.0f / D_) - st.mean * st.mean, 0.f) + 1e-5f);
          if (ukind == 1) unit_finish<1>(uv, rs, OFF_WGU + uu * CHB, OFF_CSG + uu * 512, 0, 0, scr, w, lane);
          else if (ukind == 3) unit_finish<3>(uv, rs, OFF_WDN + x_ * 229376, 0, (idx - 14) & 1, ((idx - 14) >> 1) + 4 * x_, scr, w, lane);
          else if (ukind == 2) unit_finish<2>(uv, rs, OFF_WO + x_ * 65536, 0, idx - 22, x_, scr, w, lane);
          if (ukind != 0) {
            wait_vm<0>();
            const unsigned T = __builtin_amdgcn_readfirstlane(tv) + 1u;
            if (ukind == 1) sig16(ws, FL_WG + uj, 16, ut * 4 + w, T, lane);
            else {
              const int u8 = idx - 14, line = (ukind == 3 && u8 >= 6) ? 1 : 0, word = (ukind == 2 ? (idx - 22) * 4 : u8 < 6 ? 8 + u8 * 4 : (u8 - 6) * 4) + w;
              if (lane < 32) put(ws, OFF_FLAG + (FL_WP + ((lane >> 1) * 16 + x_ + 7 * (lane & 1)) * 2 + line) * 128 + word * 4, T);
            }
          }
          PIN(st.mean);
          { unsigned t2 = tv; PIN(t2); }
          FS(57);
          bar_lgkm();
        } else {
          stats_issue(sl, rs, OFF_ST + 2 * l * SZ_ST + tt * 3584, lane);
          static_for<AH>([&](auto I) { constexpr int ks = I; af[ks] = XLD(ks); });
        }
        constexpr int PF = 3;
        u32x4 rk[PF], rv[PF];
#pragma unroll
        for (int i = 0; i < PF; ++i) { rk[i] = lds_rd(wk, i * 1024); rv[i] = lds_rd(wv, i * 1024); }
        SB();
        FS(21);
        static_for<KSD>([&](auto I) {
          constexpr int ks = I;
          if constexpr (ks == 14) { bar(); SB(); }
          ak = mfma(rk[ks % PF], af[ks], ak); PIN(ak);
          if constexpr (ks + PF < KSD) rk[ks % PF] = lds_rd(wk, (ks + PF) * 1024);
          if constexpr (!FIRST && ks + AH < KSD) af[ks + AH] = XLD(ks + AH);
          if constexpr (!FIRST && ks == 20) { st = stats_finish(sl); PIN(st.mean); PIN(st.rs); }
          SB();
          av = mfma(af[ks], rv[ks % PF], av); PIN(av);
          if constexpr (ks + PF < KSD) rv[ks % PF] = lds_rd(wv, (ks + PF) * 1024);
          if constexpr (ks >= KSD - 4) rq[ks - (KSD - 4)] = lds_rd(wq, (ks - (KSD - 4)) * 1024);
          SB();
        });
      };
      if (l == 0) passA(std::true_type{}); else passA(std::false_type{});
      FS(22);
      const LAS float* csl = (const LAS float*)(aux + CSQ_L);
      f32x4 ck[4];
#pragma unroll
      for (int g = 0; g < 4; ++g) ck[g] = *(const LAS f32x4*)(csl + 8 * g + 4 * h2);
      const float csv = csl[32 + r];
      LAS float* stl = (LAS float*)(aux + 8 * 4096 + w * 4096);
      if (h2 == 0) stl[r] = st.rs; else stl[32 + r] = st.mean;
      FS(23);
      f32x16 aq = {};
      u32x4 ko[2], vo[2];
      f32x4 srs[2], smn[2];
      f32x4 cq[4];
      float e0, e1;
      const unsigned kvo = OFF_KV + l * SZ_KV + (tt * 16 + j) * 4096;
      static_for<KSD>([&](auto I) {
        constexpr int ks = I;
        aq = mfma(rq[ks & 3], af[ks], aq); PIN(aq);
        if constexpr (ks + 4 < KSD) rq[ks & 3] = lds_rd(wq, (ks + 4) * 1024);
        if constexpr (ks == 0) { srs[0] = *(const LAS f32x4*)(stl + 4 * h2); smn[0] = *(const LAS f32x4*)(stl + 32 + 4 * h2); srs[1] = *(const LAS f32x4*)(stl + 8 + 4 * h2); smn[1] = *(const LAS f32x4*)(stl + 40 + 4 * h2); }
        if constexpr (ks >= 1 && ks < 5) {
          constexpr int g = ks - 1;
          float v[4];
#pragma unroll
          for (int e = 0; e < 4; ++e) { v[e] = ak[4 * g + e] - st.mean * ck[g][e]; PIN(v[e]); }
#pragma unroll
          for (int e = 0; e < 4; ++e) { v[e] *= st.rs; PIN(v[e]); }
          ko[g >> 1][(g & 1) * 2] = pk2(v[0], v[1]); ko[g >> 1][(g & 1) * 2 + 1] = pk2(v[2], v[3]);
          PIN(ko[g >> 1]);
        }
        if constexpr (ks == 5) { hst(rs, voff, kvo, ko[0]); hst(rs, voff + 1024, kvo, ko[1]); }
        if constexpr (ks >= 5 && ks < 13) {
          constexpr int q = ks - 5, g = q >> 1, e = (q & 1) * 2;
          if constexpr ((q & 1) == 0) { PIN(srs[g & 1]); PIN(smn[g & 1]); }
          e0 = av[2 * q] - smn[g & 1][e] * csv; e1 = av[2 * q + 1] - smn[g & 1][e + 1] * csv; PIN(e0); PIN(e1);
          e0 *= srs[g & 1][e]; e1 *= srs[g & 1][e + 1]; PIN(e0); PIN(e1);
          const unsigned pv = pk2(e0, e1);
          vo[q >> 2][q & 3] = r == 28 ? 0x3f803f80u : pv;
          PIN(vo[q >> 2]);
          if constexpr (q == 1 || q == 3) {
            srs[g & 1] = *(const LAS f32x4*)(stl + 8 * (g + 2) + 4 * h2); smn[g & 1] = *(const LAS f32x4*)(stl + 32 + 8 * (g + 2) + 4 * h2);
          }
        }
        if constexpr (ks == 13) { hst(rs, voff + 2048, kvo, vo[0]); hst(rs, voff + 3072, kvo, vo[1]); }
        if constexpr (ks == 14) { LAS char* kvl = aux + (8 + w) * 4096 + lane * 16; lds_wr(kvl, 0, ko[0]); lds_wr(kvl, 1024, ko[1]); lds_wr(kvl, 2048, vo[0]); lds_wr(kvl, 3072, vo[1]); }
        if constexpr (ks >= 16 && ks < 20) cq[ks - 16] = *(const LAS f32x4*)(csl + 64 + 8 * (ks - 16) + 4 * h2);
        if constexpr (ks == 25) { wait_vm_bar<0>(); }
        SB();
      });
      FS(24);
      const float qs = st.rs * (0.18898223650461362f * 1.4426950408889634f);
#pragma unroll
      for (int g = 0; g < 4; ++g) {
        float v[4];
#pragma unroll
        for (int e = 0; e < 4; ++e) { v[e] = aq[4 * g + e] - st.mean * cq[g][e]; PIN(v[e]); v[e] *= qs; PIN(v[e]); }
        qf[g >> 1][(g & 1) * 2] = pk2(v[0], v[1]); qf[g >> 1][(g & 1) * 2 + 1] = pk2(v[2], v[3]);
      }
      FS(25);
    }
    {
      IDS();
      const float NEG = -1e30f;
      f32x16 cd, co;
#pragma unroll
      for (int i = 0; i < 16; ++i) { const int key = (i & 3) + 8 * (i >> 2) + 4 * h2; cd[i] = key <= r ? 0.f : NEG; co[i] = key > r ? 0.f : NEG; }
      PIN(cd); PIN(co);
      bar();
      const LAS char* kw = opq((const LAS char*)aux + lane * 16 + w * 4096);
      bar();
      FS(29);
      float m = NEG;
      f32x16 o = {};
      if (rb >= 2) {
        u32x4 kf[9][2];
#pragma unroll
        for (int it = 0; it < 9; ++it) { kf[it][0] = lds_rd(kw, it * 4096); kf[it][1] = lds_rd(kw, it * 4096 + 1024); }
        SB();
        {
          const f32x16 z = {};
          f32x16 s[3];
          static_for<11>([&](auto IT) {
            constexpr int it = IT;
            if constexpr (it < 9) { s[it % 3] = mfma(kf[it][0], qf[0], it == 0 ? co : it == 8 ? cd : z); s[it % 3] = mfma(kf[it][1], qf[1], s[it % 3]); PIN(s[it % 3]); }
            if constexpr (it >= 2) {
              const f32x16& t = s[(it - 2) % 3];
              float ma = fmaxf(fmaxf(t[0], t[1]), t[2]), mb = fmaxf(fmaxf(t[3], t[4]), t[5]);
              ma = fmaxf(fmaxf(ma, t[6]), t[7]); mb = fmaxf(fmaxf(mb, t[8]), t[9]);
              ma = fmaxf(fmaxf(ma, t[10]), t[11]); mb = fmaxf(fmaxf(mb, t[12]), t[13]);
              ma = fmaxf(fmaxf(ma, t[14]), t[15]);
              m = fmaxf(fmaxf(m, ma), mb);
              PIN(m);
            }
            SB();
          });
        }
        m = -max32(m);
        PIN(m);
        FS(30);
        f32x16 cm;
#pragma unroll
        for (int i = 0; i < 16; ++i) { cm[i] = m; co[i] = co[i] + m; }
        u32x4 vf[2][2];
        vf[0][0] = lds_rd(kw, 2048); vf[0][1] = lds_rd(kw, 3072);
        f32x16 s[3];
        s[0] = mfma(kf[0][0], qf[0], co); s[0] = mfma(kf[0][1], qf[1], s[0]);
        s[1] = mfma(kf[1][0], qf[0], cm); s[1] = mfma(kf[1][1], qf[1], s[1]);
#pragma unroll
        for (int i = 0; i < 16; ++i) cd[i] = cd[i] + m;
        SB();
        static_for<9>([&](auto IT) {
          constexpr int it = IT;
          if constexpr (it < 8) { vf[(it + 1) & 1][0] = lds_rd(kw, (it + 1) * 4096 + 2048); vf[(it + 1) & 1][1] = lds_rd(kw, (it + 1) * 4096 + 3072); }
          u32x4 p[2];
          const f32x16& c = s[it % 3];
          if constexpr (it + 2 < 9) { s[(it + 2) % 3] = mfma(kf[it + 2][0], qf[0], it + 2 == 8 ? cd : cm); }
#pragma unroll
          for (int q = 0; q < 4; ++q) p[0][q] = pk2(__builtin_amdgcn_exp2f(c[2 * q]), __builtin_amdgcn_exp2f(c[2 * q + 1]));
          PIN(p[0]);
          SB();
          if constexpr (it + 2 < 9) { s[(it + 2) % 3] = mfma(kf[it + 2][1], qf[1], s[(it + 2) % 3]); PIN(s[(it + 2) % 3]); }
          o = mfma(vf[it & 1][0], p[0], o);
#pragma unroll
          for (int q = 0; q < 4; ++q) p[1][q] = pk2(__builtin_amdgcn_exp2f(c[8 + 2 * q]), __builtin_amdgcn_exp2f(c[8 + 2 * q + 1]));
          PIN(p[1]);
          SB();
          o = mfma(vf[it & 1][1], p[1], o); PIN(o);
          SB();
        });
      } else {
        const LAS char* kq = kw;
        const f32x16 z = {};
#pragma unroll
        for (int it = 0; it < 9; ++it) {
          if (tt - 8 + it >= 0) {
            f32x16 s = mfma(lds_rd(kq, it * 4096), qf[0], it == 0 ? co : it == 8 ? cd : z);
            s = mfma(lds_rd(kq, it * 4096 + 1024), qf[1], s);
#pragma unroll
            for (int i = 0; i < 16; i += 2) m = fmaxf(fmaxf(m, s[i]), s[i + 1]);
          }
        }
        m = -max32(m);
        f32x16 cm;
#pragma unroll
        for (int i = 0; i < 16; ++i) { cd[i] += m; cm[i] = m; }
#pragma unroll
        for (int it = 0; it < 9; ++it) {
          if (it >= 1 && tt - 8 + it >= 0) {
            f32x16 s = mfma(lds_rd(kq, it * 4096), qf[0], it == 8 ? cd : cm);
            s = mfma(lds_rd(kq, it * 4096 + 1024), qf[1], s);
            u32x4 p[2];
            PACK16(p, pk2(__builtin_amdgcn_exp2f(s[i0]), __builtin_amdgcn_exp2f(s[i1])));
            o = mfma(lds_rd(kq, it * 4096 + 2048), p[0], o);
            o = mfma(lds_rd(kq, it * 4096 + 3072), p[1], o);
          }
        }
      }
      FS(31);
      const unsigned lu = __float_as_uint(o[12]);
      auto rr = __builtin_amdgcn_permlane32_swap(lu, lu, false, false);
      const float inv = __builtin_amdgcn_rcpf(__uint_as_float(rr[1]));
      u32x4 ob[2];
      PACK16(ob, pk2(o[i0] * inv, o[i1] * inv));
      const unsigned oo = OFF_OA + l * SZ_OA + (tt * KSO + 2 * j) * 1024;
      hst(rs, voff, oo, ob[0]); hst(rs, voff + 1024, oo, ob[1]);
      FS(32);
      wait_vm_bar<0>();
      FS(33);
    }
    auto proj_init = [&](int xin) { IDS(); const unsigned xo = OFF_XA + xin * SZ_XA + (ttp * KSD + 4 * fs + 2 * kh) * 1024;
      u32x4 xr[2];
      if (xin == 0) {
        const unsigned short* xp = x + (size_t)(ttp * 32 + r) * D_ + 64 * fs + 32 * kh + 4 * h2;
#pragma unroll
        for (int g = 0; g < 4; ++g) { const u32x2 v = *(const GAS u32x2*)(xp + 8 * g); xr[g >> 1][(g & 1) * 2] = v.x; xr[g >> 1][(g & 1) * 2 + 1] = v.y; }
      } else { xr[0] = hld(rs, voff, xo); xr[1] = hld(rs, voff + 1024, xo); }
      f32x16 a;
#pragma unroll
      for (int q = 0; q < 8; ++q) { const unsigned xv = xr[q >> 2][q & 3]; a[2 * q] = bflo(xv); a[2 * q + 1] = bfhi(xv); }
      PIN(a);
      return a; };
    auto proj_finish = [&](f32x16 own, const f32x16& snd, int xout, bool final_) {
      IDS();
      u32x4 xres[2];
      LAS f32x4* xs = (LAS f32x4*)(aux) + (tt2 * 2 + kh) * 256 + lane;
      LAS f32x4* xrv = (LAS f32x4*)(aux) + (tt2 * 2 + (kh ^ 1)) * 256 + lane;
#pragma unroll
      for (int g = 0; g < 4; ++g) { f32x4 v = {snd[4 * g], snd[4 * g + 1], snd[4 * g + 2], snd[4 * g + 3]}; xs[g * 64] = v; }
      bar_lgkm();
#pragma unroll
      for (int g = 0; g < 4; ++g) { const f32x4 v = xrv[g * 64]; own[4 * g] += v.x; own[4 * g + 1] += v.y; own[4 * g + 2] += v.z; own[4 * g + 3] += v.w; }
#pragma unroll
      for (int q = 0; q < 8; ++q) xres[q >> 2][q & 3] = pk2(own[2 * q], own[2 * q + 1]);
      if (final_) {
        LAS char* img = aux + 16384;
#pragma unroll
        for (int g = 0; g < 4; ++g) { u32x2 v = {xres[g >> 1][(g & 1) * 2], xres[g >> 1][(g & 1) * 2 + 1]}; *(LAS u32x2*)(img + (tt2 * 32 + r) * 144 + kh * 64 + g * 16 + h2 * 8) = v; }
        bar_lgkm();
#pragma unroll
        for (int n = 0; n < 2; ++n) {
          const int row = w * 16 + n * 8 + (lane >> 3);
          const u32x4 v = *(const LAS u32x4*)(img + row * 144 + (lane & 7) * 16);
          *(GAS u32x4*)(out + (size_t)((4 * rp + 2 * th) * 32 + row) * D_ + 64 * fs + (lane & 7) * 8) = v;
        }
      } else {
        const unsigned xo = OFF_XA + xout * SZ_XA + (ttp * KSD + 4 * fs + 2 * kh) * 1024;
        hst(rs, voff, xo, xres[0]); hst(rs, voff + 1024, xo, xres[1]);
        float s1[4], s2[4];
#pragma unroll
        for (int g = 0; g < 4; ++g) {
          s1[g] = (own[4 * g] + own[4 * g + 1]) + (own[4 * g + 2] + own[4 * g + 3]);
          s2[g] = __builtin_fmaf(own[4 * g], own[4 * g], own[4 * g + 1] * own[4 * g + 1]) + __builtin_fmaf(own[4 * g + 2], own[4 * g + 2], own[4 * g + 3] * own[4 * g + 3]);
        }
        const float t1 = sum32((s1[0] + s1[1]) + (s1[2] + s1[3])), t2 = sum32((s2[0] + s2[1]) + (s2[2] + s2[3]));
        if (h2 == 0) { u32x2 v = {__float_as_uint(t1), __float_as_uint(t2)}; __builtin_amdgcn_raw_buffer_store_b64(v, rs, r * 8, OFF_ST + xout * SZ_ST + (ttp * 14 + 2 * fs + kh) * 256, 16); }
      }
    };
    if (active) {
      IDS();
      f32x16 own = proj_init(2 * l), snd = {};
      bar();
      FS(34);
      u32x4 af[16];
      const unsigned ao = OFF_OA + l * SZ_OA + (ttp * KSO + kh * 16) * 1024;
      constexpr int AH = 10;
      const unsigned oa = ao + 2 * fs * 1024, ob_ = oa - 16 * 1024;
#define OLD(i) hld(rs, voff + (i) * 1024, (i) + 2 * fs < 16 ? oa : ob_)
      static_for<AH>([&](auto I) { constexpr int i = I; af[i] = OLD(i); });
      const LAS char* wbo[3] = {opq(ring + 3 * CHB + kh * 3072), opq(ring + 0 * CHB + kh * 3072), opq((const LAS char*)aux + WOT_L + lane * 16 + kh * 3072)};
      const LAS char* wbs[3] = {opq(ring + 3 * CHB + kh * 2048 + (kh ^ 1) * 1024), opq(ring + 0 * CHB + kh * 2048 + (kh ^ 1) * 1024), opq((const LAS char*)aux + WOT_L + lane * 16 + kh * 2048 + (kh ^ 1) * 1024)};
      constexpr int PF = 2;
      u32x4 wo_[PF], wsn[PF];
#pragma unroll
      for (int i = 0; i < PF; ++i) { wo_[i] = lds_rd(wbo[0], i * 4096); wsn[i] = lds_rd(wbs[0], i * 4096); }
      SB();
      static_for<16>([&](auto I) {
        constexpr int i = I, n = i + PF;
        own = mfma(wo_[i % PF], af[i], own); PIN(own);
        if constexpr (n < 16) wo_[i % PF] = lds_rd(wbo[n / 7], (n % 7) * 4096);
        if constexpr (i + AH < 16) af[i + AH] = OLD(i + AH);
        SB();
        snd = mfma(wsn[i % PF], af[i], snd); PIN(snd);
        if constexpr (n < 16) wsn[i % PF] = lds_rd(wbs[n / 7], (n % 7) * 4096);
        SB();
      });
      FS(35);
      proj_finish(own, snd, 2 * l + 1, false);
      FS(38);
      wait_vm_bar<0>();
      FS(39);
    }
    {
      IDS();
      bar();
      FS(0);
      constexpr int AH = 10;
      u32x4 af[KSD];
      const unsigned xo = OFF_XA + (2 * l + 1) * SZ_XA + tt * (KSD * 1024);
      StatLd sl; stats_issue(sl, rs, OFF_ST + (2 * l + 1) * SZ_ST + tt * 3584, lane);
      Stats st;
      static_for<AH>([&](auto I) { constexpr int ks = I; af[ks] = XLD(ks); });
      const LAS float* csl = (const LAS float*)(aux + CSG_L) + 4 * h2;
      const unsigned ho = OFF_HA + l * SZ_HA + tt * HSTR + 7 * j * 1024;
      f32x16 prev = {}, acc = {};
      u32x4 wr[4];
      {
        const LAS char* w0 = slot(5); const LAS char* w1 = slot(6);
        constexpr int PF = 3;
        u32x4 r0[PF], r1[PF];
#pragma unroll
        for (int i = 0; i < PF; ++i) { r0[i] = lds_rd(w0, i * 1024); r1[i] = lds_rd(w1, i * 1024); }
        SB();
        FS(2);
        static_for<KSD>([&](auto I) {
          constexpr int ks = I;
          prev = mfma(r0[ks % PF], af[ks], prev); PIN(prev);
          if constexpr (ks + PF < KSD) r0[ks % PF] = lds_rd(w0, (ks + PF) * 1024);
          if constexpr (ks + AH < KSD) af[ks + AH] = XLD(ks + AH);
          if constexpr (ks == 20) { st = stats_finish(sl); PIN(st.mean); PIN(st.rs); }
          SB();
          acc = mfma(r1[ks % PF], af[ks], acc); PIN(acc);
          if constexpr (ks + PF < KSD) r1[ks % PF] = lds_rd(w1, (ks + PF) * 1024);
          SB();
        });
      }
      FS(3);
      const float c1 = -1.4426950408889634f * st.rs, vk = __builtin_amdgcn_rcpf(st.rs * st.rs);
      u32x4 ov;
      f32x4 cc[4];
      float ea[8], eb[8], ex[8];
#define GU_STAGE(P, v, S) do { \
        if constexpr ((S) == 0) { ea[v] = P[v] - st.mean * cc[(v) >> 2][(v) & 3]; PIN(ea[v]); eb[v] = P[(v) + 8] - st.mean * cc[2 + ((v) >> 2)][(v) & 3]; PIN(eb[v]); } \
        else if constexpr ((S) == 1) { ex[v] = c1 * ea[v]; PIN(ex[v]); eb[v] = ea[v] * eb[v]; PIN(eb[v]); } \
        else if constexpr ((S) == 2) { ex[v] = __builtin_amdgcn_exp2f(ex[v]); PIN(ex[v]); } \
        else if constexpr ((S) == 3) { ex[v] = __builtin_fmaf(ex[v], vk, vk); PIN(ex[v]); } \
        else if constexpr ((S) == 4) { ex[v] = __builtin_amdgcn_rcpf(ex[v]); PIN(ex[v]); } \
        else { eb[v] = eb[v] * ex[v]; PIN(eb[v]); if constexpr ((v) & 1) { ov[(v) >> 1] = pk2(eb[(v) - 1], eb[v]); PIN(ov); } } } while (0)
#define GU_GAP(P, g, base, sp) static_for<8>([&](auto V_) { constexpr int v_ = V_; constexpr int s_ = (g) - (base) - (sp) * v_; if constexpr (s_ >= 0 && s_ < 6) GU_STAGE(P, v_, s_); })
#define GU_CC(t) _Pragma("unroll") for (int g = 0; g < 4; ++g) cc[g] = *(const LAS f32x4*)(csl + (t) * 32 + 8 * g)
      GU_CC(0);
      bar();
      { const LAS char* w2 = slot(7);
#pragma unroll
        for (int i = 0; i < 4; ++i) wr[i] = lds_rd(w2, i * 1024); }
      SB();
      FS(4);
      f32x16 t1 = acc;
      static_for<5>([&](auto T) {
        constexpr int t = T + 2;
        const LAS char* wl = slot(5 + t);
        const LAS char* wn = slot(6 + t);
        acc = f32x16{};
        static_for<KSD>([&](auto I) {
          constexpr int ks = I;
          if constexpr (ks == 14) {
            if constexpr (t < 6) bar();
            else asm volatile("s_waitcnt vmcnt(1)\n\ts_barrier" ::: "memory");
            SB();
          }
          acc = mfma(wr[ks & 3], af[ks], acc); PIN(acc);
          if constexpr (ks + 4 < KSD) wr[ks & 3] = lds_rd(wl, (ks + 4) * 1024);
          else if constexpr (t < 6) wr[ks & 3] = lds_rd(wn, (ks + 4 - KSD) * 1024);
          if constexpr (t == 2) {
            if constexpr (ks < 14) { GU_GAP(prev, ks, 0, 1); }
            if constexpr (ks == 12) { GU_CC(1); }
            if constexpr (ks == 13) hst(rs, voff, ho, ov);
            if constexpr (ks >= 14) { GU_GAP(t1, ks, 15, 1); }
            if constexpr (ks == 27) { hst(rs, voff + 1024, ho, ov); GU_CC(2); }
          } else {
            GU_GAP(prev, ks, 1, 3);
            if constexpr (ks == 27) { hst(rs, voff + (t - 1) * 1024, ho, ov); GU_CC(t); }
          }
          SB();
        });
        prev = acc;
        FS(3 + t);
      });
      static_for<13>([&](auto G) { constexpr int g = G; GU_GAP(prev, g, 0, 1); });
      hst(rs, voff + 6 * 1024, ho, ov);
      FS(10);
      wait_vm_bar<0>();
      FS(11);
    }
    if (active) {
      IDS();
      f32x16 own = proj_init(2 * l + 1), snd = {};
      bar();
      FS(41);
      constexpr int WIN = DN_WIN;
      constexpr int NR = WIN + 2;
      u32x4 af[NR];
      const unsigned ao = OFF_HA + l * SZ_HA + ttp * HSTR + kh * 56 * 1024;
#define HLD(i) hld(rs, voff + (kk_of(i) % 7) * 1024, ao + (((kk_of(i) / 7) + fs) & 7) * 7168)
      static_for<WIN>([&](auto I) { constexpr int i = I; af[i] = HLD(i); });
      const int oo_ = kh * 3072, so_ = kh * 2048 + (kh ^ 1) * 1024;
      constexpr int PF = 2;
      u32x4 wo_[PF], wsn[PF];
      const LAS char* po = opq(ring + 0 * CHB + oo_); const LAS char* ps = opq(ring + 0 * CHB + so_);
#pragma unroll
      for (int i = 0; i < PF; ++i) { wo_[i] = lds_rd(po, i * 4096); wsn[i] = lds_rd(ps, i * 4096); }
      SB();
      FS(42);
      const LAS char* pon = po; const LAS char* psn = ps;
      static_for<56>([&](auto I) {
        constexpr int i = I, c = i / 7, s = i % 7, n = s + PF;
        if constexpr (s == 0) { po = pon; ps = psn; if constexpr (c < 7) { pon = opq(ring + ((13 + c) & 3) * CHB + oo_); psn = opq(ring + ((13 + c) & 3) * CHB + so_); } }
        if constexpr (i == 1) FS(43);
        if constexpr (i == 28) FS(44);
        if constexpr (s == 3) { bar(); SB(); }
        const u32x4 a = af[i % NR];
        own = mfma(wo_[i % PF], a, own); PIN(own);
        if constexpr (n < 7) wo_[i % PF] = lds_rd(po, n * 4096); else if constexpr (c < 7) wo_[i % PF] = lds_rd(pon, (n - 7) * 4096);
        SB();
        snd = mfma(wsn[i % PF], a, snd); PIN(snd);
        if constexpr (n < 7) wsn[i % PF] = lds_rd(ps, n * 4096); else if constexpr (c < 7) wsn[i % PF] = lds_rd(psn, (n - 7) * 4096);
        if constexpr (i + WIN < 56) af[(i + WIN) % NR] = HLD(i + WIN);
        SB();
      });
      FS(45);
      proj_finish(own, snd, 2, l == 1);
      FS(46);
      wait_vm_bar<0>();
      FS(47);
    }
    if (l == 0) FS(61);
  }
}

}

extern "C" size_t stamp_offset() { return OFF_STAMP; }

extern "C" void kernel_launch(void* const* d_in, const int* in_sizes, int n_in, void* d_out, int out_size, void* d_ws, size_t ws_size,
                              hipStream_t stream) {
  (void)hipFuncSetAttribute((const void*)k_main, hipFuncAttributeMaxDynamicSharedMemorySize, LDS_BYTES);
  k_main<<<256, 512, LDS_BYTES, stream>>>((char*)d_ws, (unsigned short*)d_out, (const unsigned short*)d_in[0], (const unsigned short*)d_in[1], (const unsigned short*)d_in[2],
                                          (const unsigned short*)d_in[4], (const unsigned short*)d_in[5], (const unsigned short*)d_in[6]);
}
```

```cpp
#include <hip/hip_runtime.h>
#include <stdint.h>
#include <utility>

typedef __attribute__((ext_vector_type(8))) __bf16 bf16x8;
typedef __attribute__((ext_vector_type(2))) __bf16 bf16x2;
typedef __attribute__((ext_vector_type(2))) float f32x2;
typedef __attribute__((ext_vector_type(4))) float f32x4;
typedef __attribute__((ext_vector_type(16))) float f32x16;
typedef __attribute__((ext_vector_type(4))) unsigned u32x4;
typedef __attribute__((ext_vector_type(2))) unsigned u32x2;
#define DEV __device__ __forceinline__
#define LAS __attribute__((address_space(3)))
#define GAS __attribute__((address_space(1)))
typedef GAS unsigned gu32;
#define RLX __ATOMIC_RELAXED, __HIP_MEMORY_SCOPE_AGENT

#define SB() __builtin_amdgcn_sched_barrier(0)
#define PIN(x) asm volatile("" : "+v"(x))

namespace {

constexpr int D_ = 448, F_ = 1792;
constexpr int NTT = 64;
constexpr int KSD = 28;
constexpr int KSO = 32;
constexpr int KSF = 112;
constexpr int CHB = 28672;

constexpr unsigned OFF_FLAG = 0;
constexpr int FL_GEN = 0;
constexpr int FL_STARTED = 1;
constexpr int FL_START = 16;
constexpr int FL_WG = FL_START + 256;
constexpr int FL_WP = FL_WG + 256;
constexpr int FL_KV = FL_WP + 512;
constexpr int FL_O = FL_KV + 1024;
constexpr int FL_X1 = FL_O + 512;
constexpr int FL_H = FL_X1 + 512;
constexpr int FL_X2 = FL_H + 1024;
constexpr int NFLAG = FL_X2 + 256;
constexpr unsigned OFF_WQKV = 576 * 1024;
constexpr unsigned OFF_WO   = OFF_WQKV + 16 * 3 * CHB;
constexpr unsigned OFF_WGU  = OFF_WO + 7 * 64 * 1024;
constexpr unsigned OFF_WDN  = OFF_WGU + 16 * 7 * CHB;
constexpr unsigned OFF_CSQ  = OFF_WDN + 7 * 224 * 1024;
constexpr unsigned OFF_CSG  = OFF_CSQ + 48 * 512;
constexpr unsigned SZ_XA    = NTT * KSD * 1024;
constexpr unsigned OFF_XA   = OFF_CSG + 112 * 512;
constexpr unsigned SZ_ST    = NTT * 14 * 32 * 8;
constexpr unsigned OFF_ST   = OFF_XA + 4 * SZ_XA;
constexpr unsigned SZ_KV    = NTT * 16 * 4096;
constexpr unsigned OFF_KV   = OFF_ST + 4 * SZ_ST;
constexpr unsigned SZ_OA    = NTT * KSO * 1024;
constexpr unsigned OFF_OA   = OFF_KV + 2 * SZ_KV;
constexpr unsigned HSTR = KSF * 1024;
constexpr unsigned SZ_HA    = NTT * HSTR;
constexpr unsigned OFF_HA   = OFF_OA + 2 * SZ_OA;
constexpr unsigned WS_END   = OFF_HA + 2 * SZ_HA;
static_assert(NFLAG * 128 <= OFF_WQKV, "flags");
static_assert(OFF_XA % 128 == 0 && OFF_ST % 128 == 0 && OFF_KV % 128 == 0 && OFF_OA % 128 == 0 && OFF_HA % 128 == 0, "lines");

template <class F, int... I> DEV void static_for_impl(F&& f, std::integer_sequence<int, I...>) { (f(std::integral_constant<int, I>{}), ...); }
template <int N, class F> DEV void static_for(F&& f) { static_for_impl(f, std::make_integer_sequence<int, N>{}); }

DEV int lane_id() { int l; asm volatile("v_mbcnt_lo_u32_b32 %0, -1, 0\n\tv_mbcnt_hi_u32_b32 %0, -1, %0" : "=v"(l)); return l; }
DEV unsigned pk2(float a, float b) { f32x2 v = {a, b}; return __builtin_bit_cast(unsigned, __builtin_convertvector(v, bf16x2)); }
DEV f32x16 mfma(u32x4 a, u32x4 b, f32x16 c) {
  return __builtin_amdgcn_mfma_f32_32x32x16_bf16(__builtin_bit_cast(bf16x8, a), __builtin_bit_cast(bf16x8, b), c, 0, 0, 0);
}
DEV float bflo(unsigned u) { return __uint_as_float(u << 16); }
DEV float bfhi(unsigned u) { return __uint_as_float(u & 0xffff0000u); }
DEV float sum32(float v) {
  const unsigned u = __float_as_uint(v);
  auto rr = __builtin_amdgcn_permlane32_swap(u, u, false, false);
  return __uint_as_float(rr[0]) + __uint_as_float(rr[1]);
}
DEV float max32(float v) {
  const unsigned u = __float_as_uint(v);
  auto rr = __builtin_amdgcn_permlane32_swap(u, u, false, false);
  return fmaxf(__uint_as_float(rr[0]), __uint_as_float(rr[1]));
}
template <int N> DEV void wait_vm() { asm volatile("s_waitcnt vmcnt(%0)" ::"n"(N) : "memory"); }
template <int N> DEV void wait_vm_bar() { asm volatile("s_waitcnt vmcnt(%0) lgkmcnt(0)\n\ts_barrier" ::"n"(N) : "memory"); }
DEV void bar_lgkm() { asm volatile("s_waitcnt lgkmcnt(0)\n\ts_barrier" ::: "memory"); }
DEV u32x4 lds_rd(const LAS char* base, int off) { return *(const LAS u32x4*)(base + off); }
DEV void lds_wr(LAS char* base, int off, u32x4 v) { *(LAS u32x4*)(base + off) = v; }
DEV u32x4 gld(const char* p) { return *(const GAS u32x4*)(p); }
typedef __amdgpu_buffer_rsrc_t rsrc_t;
DEV u32x4 hld(rsrc_t rs, unsigned voff, unsigned soff) { return __builtin_amdgcn_raw_buffer_load_b128(rs, voff, soff, 16); }
DEV void hst(rsrc_t rs, unsigned voff, unsigned soff, u32x4 v) { __builtin_amdgcn_raw_buffer_store_b128(v, rs, voff, soff, 16); asm volatile("s_nop 1" ::"v"(v) : "memory"); }

DEV void poll(const char* ws, unsigned vo, unsigned T) {
  unsigned cnt, v;
  asm volatile(
      "s_mov_b32 %1, 0\n"
      "1:\n\t"
      "global_load_dword %0, %2, %3 sc1\n\t"
      "s_waitcnt vmcnt(0)\n\tv_cmp_ne_u32 vcc, %4, %0\n\ts_nop 1\n\ts_cbranch_vccz 2f\n\ts_sleep 1\n\t"
      "s_add_u32 %1, %1, 1\n\ts_cmp_lt_u32 %1, 0x8000\n\ts_cbranch_scc1 1b\n"
      "2:"
      : "=&v"(v), "=&s"(cnt) : "v"(vo), "s"(ws), "s"(T) : "memory", "scc", "vcc");
}
DEV void poll_line(const char* ws, int line, int nwords, unsigned T, int lane) { poll(ws, OFF_FLAG + line * 128 + min(lane, nwords - 1) * 4, T); }
DEV void put(char* ws, unsigned off, unsigned T) { __hip_atomic_store((gu32*)(ws + off), T, RLX); }
DEV void sig16(char* ws, int line0, int stride, int word, unsigned T, int lane) { if (lane < 16) put(ws, OFF_FLAG + (line0 + lane * stride) * 128 + word * 4, T); }

struct Stats { float mean, rs; };
constexpr int G1T = 4, G1S = 8 * G1T;
__host__ __device__ constexpr int kk_of(int i) { return i < G1S ? 7 * (i / G1T) + i % G1T : 7 * ((i - G1S) / (7 - G1T)) + G1T + (i - G1S) % (7 - G1T); }

typedef short s16x4 __attribute__((ext_vector_type(4)));
DEV u32x2 tr_read(const LAS char* p) { return __builtin_bit_cast(u32x2, __builtin_amdgcn_ds_read_tr16_b64_v4i16((LAS s16x4*)p)); }
DEV u32x4 tr_frag(const LAS char* scr, int lane) {
  const LAS char* rd = scr + (4 * (lane >> 5) + ((lane & 15) >> 2)) * 64 + ((lane >> 4) & 1) * 32 + (lane & 3) * 8;
  const u32x2 lo = tr_read(rd), hi = tr_read(rd + 512);
  return u32x4{lo.x, lo.y, hi.x, hi.y};
}
DEV float frag_sum(u32x4 v) { float t = 0.f;
#pragma unroll
  for (int e = 0; e < 4; ++e) t += bflo(v[e]) + bfhi(v[e]);
  return t; }
template <int KIND>
DEV void unit_load(u32x4 (&v)[8], const unsigned short* __restrict__ src, const unsigned short* __restrict__ src2, int ld, int col, int a2, int q, int lane) {
  constexpr int NI = KIND == 2 ? 8 : 7;
  const int p = lane & 3, rw = lane >> 2;
  const unsigned short* sp = KIND == 1 ? (p < 2 ? src + col + 8 * p : src2 + col + 8 * (p - 2)) : src + col + 8 * p;
#pragma unroll
  for (int n = 0; n < NI; ++n) {
    const int ks = q + 4 * n;
    int row; bool ok = true;
    if (KIND == 2) { const int d = 16 * (ks & 1) + rw; ok = d < 28; row = 28 * (ks >> 1) + (ok ? d : 0); }
    else row = (KIND == 3 ? 448 * (a2 & 3) : 0) + 16 * ks + rw;
    const u32x4 t = *(const GAS u32x4*)(sp + (size_t)row * ld);
    v[n] = ok ? t : u32x4{0u, 0u, 0u, 0u};
  }
}
template <int KIND>
DEV void unit_finish(const u32x4 (&v)[8], rsrc_t rs, unsigned dst, unsigned cs, int a1, int a2, LAS char* scr, int q, int lane) {
  constexpr int NI = KIND == 2 ? 8 : 7;
  float sum = 0.f;
#pragma unroll
  for (int h = 0; h < 2; ++h) {
#pragma unroll
    for (int n = 4 * h; n < 4 * h + 4; ++n) if (n < NI) *(LAS u32x4*)(scr + (n & 3) * 1024 + lane * 16) = v[n];
    asm volatile("" ::: "memory");
#pragma unroll
    for (int n = 4 * h; n < 4 * h + 4; ++n) {
      if (n < NI) {
        const int i = q + 4 * n;
        const u32x4 f = tr_frag(scr + (n & 3) * 1024, lane);
        unsigned doff;
        if (KIND == 1) doff = i * 1024;
        else if (KIND == 2) doff = ((((i - 2 * a2) & 15) * 2 + (i >> 4)) * 2 + a1) * 1024;
        else {
          const int ks = 28 * (a2 & 3) + i, kh = ks / 56, kk = ks % 56, j8 = (kk / 7 - (a2 >> 2)) & 7, t = kk % 7;
          const int step = t < G1T ? G1T * j8 + t : G1S + (7 - G1T) * j8 + (t - G1T);
          doff = ((step * 2 + kh) * 2 + a1) * 1024;
        }
        hst(rs, lane * 16, dst + doff, f);
        if (KIND == 1) sum += frag_sum(f);
      }
    }
    asm volatile("s_waitcnt lgkmcnt(0)" ::: "memory");
  }
  if (KIND == 1) {
    sum = sum32(sum);
    if (lane < 32) __builtin_amdgcn_raw_buffer_store_b32(__float_as_uint(sum), rs, lane * 4, cs + q * 128, 16);
  }
}

constexpr int AUX = 4 * CHB;
constexpr int LDS_BYTES = AUX + 49152;
#define PACK16(o, expr) _Pragma("unroll") for (int q_ = 0; q_ < 8; ++q_) { const int i0 = 2 * q_, i1 = 2 * q_ + 1; (void)i0; (void)i1; o[q_ >> 2][q_ & 3] = expr; }
struct StatLd { u32x2 v[7]; };
DEV void stats_issue(StatLd& s, rsrc_t rs, unsigned soff, int lane) {
  const unsigned vo = (lane >> 5) * 1792 + (lane & 31) * 8;
#pragma unroll
  for (int i = 0; i < 7; ++i) s.v[i] = __builtin_amdgcn_raw_buffer_load_b64(rs, vo + i * 256, soff, 16);
}
DEV Stats stats_finish(const StatLd& s) {
  float s1 = 0.f, s2 = 0.f;
#pragma unroll
  for (int i = 0; i < 7; ++i) { s1 += __uint_as_float(s.v[i].x); s2 += __uint_as_float(s.v[i].y); }
  s1 = sum32(s1); s2 = sum32(s2);
  Stats o; o.mean = s1 * (1.0f / D_);
  const float var = fmaxf(s2 * (1.0f / D_) - o.mean * o.mean, 0.f);
  o.rs = __builtin_amdgcn_rsqf(var + 1e-5f);
  return o;
}

template <int N> DEV void dma_n(rsrc_t rs, unsigned voff, unsigned lds, unsigned soff) {
  if constexpr (N == 4)
    asm volatile("s_mov_b32 m0, %0\n\ts_nop 0\n\tbuffer_load_dwordx4 %1, %2, %3 offen lds\n\tbuffer_load_dwordx4 %1, %2, %3 offen offset:1024 lds\n\t"
                 "buffer_load_dwordx4 %1, %2, %3 offen offset:2048 lds\n\tbuffer_load_dwordx4 %1, %2, %3 offen offset:3072 lds" ::"s"(lds), "v"(voff), "s"(rs), "s"(soff) : "memory");
  else
    asm volatile("s_mov_b32 m0, %0\n\ts_nop 0\n\tbuffer_load_dwordx4 %1, %2, %3 offen lds\n\tbuffer_load_dwordx4 %1, %2, %3 offen offset:1024 lds" ::"s"(lds), "v"(voff), "s"(rs), "s"(soff) : "memory");
}
DEV void dma_chunk(rsrc_t rs, unsigned voff, unsigned lds, unsigned soff, int w) {
  const unsigned o = (w - 1) * 9216;
  dma_n<4>(rs, voff, lds + o, soff + o); dma_n<4>(rs, voff, lds + o + 4096, soff + o + 4096);
  if (w == 3) dma_n<2>(rs, voff, lds + o + 8192, soff + o + 8192);
  else asm volatile("s_mov_b32 m0, %0\n\ts_nop 0\n\tbuffer_load_dwordx4 %1, %2, %3 offen lds" ::"s"(lds + o + 8192), "v"(voff), "s"(rs), "s"(soff + o + 8192) : "memory");
}
DEV void bar() { asm volatile("s_barrier" ::: "memory"); }
DEV const LAS char* opq(const LAS char* p) { asm volatile("" : "+v"(p)); return p; }

constexpr int DN_WIN = 14;
static_assert(G1S - DN_WIN >= 17, "group 2 is confirmed at D2");

__global__ __launch_bounds__(512, 2) void k_main(char* __restrict__ ws, unsigned short* __restrict__ out, const unsigned short* __restrict__ x, const unsigned short* __restrict__ wqkv,
                                                  const unsigned short* __restrict__ wo, const unsigned short* __restrict__ w1, const unsigned short* __restrict__ w3, const unsigned short* __restrict__ w2) {
  extern __shared__ __attribute__((aligned(16))) char smem[];
  const int w8_ = __builtin_amdgcn_readfirstlane(threadIdx.x >> 6);
  const bool helper = w8_ >= 4;
  const int w_ = w8_ & 3;
  const int b = blockIdx.x, idx = b >> 3;
  const int x_ = b & 7;
  const int rb_ = 4 * (x_ & 3) + (idx >> 3), j_ = 8 * (x_ >> 2) + (idx & 7);
  const int rp_ = 2 * x_ + (idx >> 4), m_ = idx & 15;
  const bool active = m_ < 14;
  const unsigned lds0 = __builtin_amdgcn_readfirstlane((unsigned)(uintptr_t)smem);
  const rsrc_t rs = __builtin_amdgcn_make_buffer_rsrc(ws, 0, WS_END, 0x00020000);
#define IDS() \
  int w = w_, rb = rb_, j = j_, rp = rp_, mm = m_; asm volatile("" : "+s"(w), "+s"(rb), "+s"(j), "+s"(rp), "+s"(mm)); \
  const int lane = lane_id(), r = lane & 31, h2 = lane >> 5; \
  const int th = mm >= 7 ? 1 : 0, fs = mm - 7 * th, tt2 = w & 1, kh = w >> 1; \
  const int tt = 4 * rb + w, ttp = 4 * rp + 2 * th + tt2; \
  const unsigned voff = lane * 16; \
  const LAS char* ring = (const LAS char*)smem + lane * 16; \
  LAS char* aux = (LAS char*)smem + AUX; \
  (void)r; (void)h2; (void)fs; (void)kh; (void)tt; (void)ttp; (void)voff; (void)ring; (void)aux; (void)tt2; (void)rp;
#define slot(c) opq(ring + ((c) & 3) * CHB)
#define XLD(p) hld(rs, voff + (p) * 1024, xo)
  constexpr int CSQ_L = 0;
  constexpr int CSG_L = 16384;
  constexpr int WOT_L = 24576;
  if (helper) {
    auto issue = [&](int c, int l) {
      const int sl = c & 3;
      if (c >= 20) { c -= 20; ++l; }
      const bool proj = (c >= 3 && c < 5) || c >= 12;
      int w = w_, j = j_, mm = m_; asm volatile("" : "+s"(w), "+s"(j), "+s"(mm));
      const int fs = mm >= 7 ? mm - 7 : mm;
      unsigned src;
      if (c < 3) src = OFF_WQKV + (j * 3 + c) * CHB;
      else if (c < 5) src = OFF_WO + fs * 65536 + (c - 3) * CHB;
      else if (c < 12) src = OFF_WGU + (j * 7 + c - 5) * CHB;
      else src = OFF_WDN + fs * 229376 + (c - 12) * CHB;
      if (!(l < 2 && (active || !proj))) src = WS_END;
      if (w != 0) dma_chunk(rs, lane_id() * 16, lds0 + sl * CHB, src, w);
    };
    unsigned tv = __hip_atomic_load((gu32*)(ws + OFF_FLAG + FL_GEN * 128), RLX);
    {
      const int lane = lane_id(), q = w_, j = j_;
      const rsrc_t rq = __builtin_amdgcn_make_buffer_rsrc((void*)wqkv, 0, D_ * 3 * D_ * 2, 0x00020000);
      const unsigned wvo = (lane >> 2) * (3 * D_ * 2) + (lane & 3) * 16;
#pragma unroll
      for (int p = 0; p < 3; ++p)
#pragma unroll
        for (int n = 0; n < 7; ++n)
          asm volatile("s_mov_b32 m0, %0\n\ts_nop 0\n\tbuffer_load_dwordx4 %1, %2, %3 offen lds"
                       ::"s"(lds0 + p * CHB + (q + 4 * n) * 1024), "v"(wvo), "s"(rq), "s"(16 * (q + 4 * n) * (3 * D_ * 2) + 56 * j - 8 * (j & 1) + (p == 2 ? 0 : (p + 1) * D_ * 2)) : "memory");
    }
    auto qkv_tiles = [&](int p) {
      const int lane = lane_id(), q = w_, j = j_;
      float sum = 0.f;
      u32x4 fr[7];
#pragma unroll
      for (int n = 0; n < 7; ++n) fr[n] = tr_frag((const LAS char*)smem + p * CHB + (q + 4 * n) * 1024 + 8 * (j & 1), lane);
      asm volatile("s_waitcnt lgkmcnt(0)" ::: "memory");
#pragma unroll
      for (int n = 0; n < 7; ++n) {
        u32x4 f = fr[n];
        if ((lane & 31) >= 28) f = u32x4{0u, 0u, 0u, 0u};
        *(LAS u32x4*)((LAS char*)smem + p * CHB + (q + 4 * n) * 1024 + lane * 16) = f;
        if (rb_ == 0) hst(rs, lane * 16, OFF_WQKV + (j * 3 + p) * CHB + (q + 4 * n) * 1024, f);
        sum += frag_sum(f);
      }
      sum = sum32(sum);
      if (lane < 32) { *(LAS float*)((LAS char*)smem + AUX + 512 + (p * 4 + q) * 128 + lane * 4) = sum;
        if (rb_ == 0) __builtin_amdgcn_raw_buffer_store_b32(__float_as_uint(sum), rs, lane * 4, OFF_CSQ + (j * 3 + p) * 512 + q * 128, 16); }
    };
    wait_vm<7>();
    qkv_tiles(0); qkv_tiles(1);
    PIN(tv);
    const unsigned T = __builtin_amdgcn_readfirstlane(tv) + 1u;
    auto stage_cs = [&](unsigned src, int n16, int dst) {
      const int lane = lane_id();
      if (lane < n16) {
        f32x4 a = {0.f, 0.f, 0.f, 0.f};
#pragma unroll
        for (int q = 0; q < 4; ++q) a += __builtin_bit_cast(f32x4, hld(rs, (lane >> 3) * 512 + (lane & 7) * 16 + q * 128, src));
        *(LAS f32x4*)((LAS char*)smem + AUX + dst + lane * 16) = a;
      }
    };
    auto stage_csq = [&]() { if (w_ == 3) stage_cs(OFF_CSQ + j_ * 3 * 512, 24, CSQ_L); };
    u32x4 hd[4][10];
#pragma unroll
    for (int c = 0; c < 4; ++c)
#pragma unroll
      for (int p = 0; p < 10; ++p) hd[c][p] = u32x4{0u, 0u, 0u, 0u};
    auto hold = [&](auto C) {
      constexpr int c = C;
      int w = w_, mm = m_; asm volatile("" : "+s"(w), "+s"(mm));
      if (w == 0 || !active) return;
      const int fs = mm >= 7 ? mm - 7 : mm;
      const char* src = ws + OFF_WDN + (size_t)fs * 229376 + (4 + c) * CHB + (w - 1) * 9216 + lane_id() * 16;
#pragma unroll
      for (int p = 0; p < 9; ++p) hd[c][p] = gld(src + p * 1024);
      if (w == 3) hd[c][9] = gld(src + 9 * 1024);
      SB();
    };
    auto drop = [&](auto C) {
      constexpr int c = C;
      int w = w_; asm volatile("" : "+s"(w));
      if (w == 0) return;
      LAS char* dst = (LAS char*)smem + (c & 3) * CHB + (w - 1) * 9216 + lane_id() * 16;
#pragma unroll
      for (int p = 0; p < 9; ++p) lds_wr(dst, p * 1024, hd[c][p]);
      if (w == 3) lds_wr(dst, 9 * 1024, hd[c][9]);
    };
    using I0 = std::integral_constant<int, 0>; using I1 = std::integral_constant<int, 1>; using I2 = std::integral_constant<int, 2>; using I3 = std::integral_constant<int, 3>;
#pragma unroll 1
    for (int l = 0; l < 2; ++l) {
      IDS();
      if (l == 0) {
        bar_lgkm();
        wait_vm<0>();
        qkv_tiles(2);
        if (w == 1 && lane == 0) put(ws, OFF_FLAG + FL_STARTED * 128 + b * 4, T);
        bar_lgkm();
        if (w == 3 && lane < 24) {
          const LAS char* ps = (const LAS char*)aux + 512 + (lane >> 3) * 512 + (lane & 7) * 16;
          const f32x4 a = (*(const LAS f32x4*)(ps) + *(const LAS f32x4*)(ps + 128)) + (*(const LAS f32x4*)(ps + 256) + *(const LAS f32x4*)(ps + 384));
          *(LAS f32x4*)(aux + CSQ_L + lane * 16) = a;
        }
        bar_lgkm();
        wait_vm<0>();
        if (rb == 0) { if (lane < 48) put(ws, OFF_FLAG + (FL_START + (lane & 15) * 16 + j) * 128 + ((lane >> 4) * 4 + w) * 4, T); }
        if (w == 2) {
          poll(ws, OFF_FLAG + (lane < 12 ? (FL_START + rb * 16 + j) * 128 + lane * 4 : (FL_WG + rb * 16 + j) * 128 + min(lane - 12, 27) * 4), T);
          if (active) poll(ws, OFF_FLAG + (FL_WP + (rp * 16 + mm) * 2) * 128 + (lane < 32 ? lane * 4 : 128 + min(lane - 32, 7) * 4), T);
          __builtin_amdgcn_fence(__ATOMIC_ACQUIRE, "agent");
        }
      } else {
        if (w == 0) poll_line(ws, FL_X2 + rb * 16 + j, 14, T, lane); else wait_vm<0>();
        bar_lgkm();
        bar();
        bar();
      }
      bar();
      if (w == 1 && lane < 4) put(ws, OFF_FLAG + (FL_KV + ((l * 16 + rb) * 16 + j) * 2 + (lane >> 1)) * 128 + (lane & 1) * 4, T);
      if (w == 0) {
        if (rb >= 1) {
          const int far = (lane & 2) && rb >= 2;
          poll(ws, OFF_FLAG + (FL_KV + ((l * 16 + rb - 1 - far) * 16 + j) * 2 + far) * 128 + (lane & 1) * 4, T);
        }
      }
      bar();
      if (rb >= 1) {
        u32x4 hv[8];
#pragma unroll
        for (int n = 0; n < 2; ++n) {
          const int rel = 2 * w + n, kt = max(4 * rb - 8 + rel, 0);
#pragma unroll
          for (int q = 0; q < 4; ++q) hv[n * 4 + q] = hld(rs, voff + q * 1024, OFF_KV + l * SZ_KV + (kt * 16 + j) * 4096);
        }
        SB();
#pragma unroll
        for (int n = 0; n < 8; ++n) lds_wr(aux + lane * 16, (2 * w) * 4096 + n * 1024, hv[n]);
      }
      if (w == 2) wait_vm<0>();
      bar_lgkm();
      issue(3, l); issue(4, l); issue(5, l); issue(6, l);
      bar();
      if (w == 1) sig16(ws, FL_O + (l * 16 + rb) * 16, 1, j, T, lane);
      if (w == 3) stage_cs(OFF_CSG + j * 7 * 512, 56, CSG_L);
      if (active) {
        if (w == 1 || w == 2) dma_n<4>(rs, voff, lds0 + AUX + WOT_L + (w - 1) * 4096, OFF_WO + fs * 65536 + 2 * CHB + (w - 1) * 4096);
        if (w == 0) poll_line(ws, FL_O + (l * 16 + rp) * 16 + mm, 16, T, lane);
        if (w != 0) wait_vm<0>();
        bar_lgkm();
        bar();
        bar();
        if (w == 1) sig16(ws, FL_X1 + (l * 16 + rp) * 16, 1, mm, T, lane);
      }
      issue(7, l); issue(8, l);
      if (w == 0) poll_line(ws, FL_X1 + (l * 16 + rb) * 16 + j, 14, T, lane);
      if (w != 0) wait_vm<18>();
      bar_lgkm();
      wait_vm_bar<9>();
      issue(9, l); issue(10, l);
      wait_vm_bar<18>();
      const bool first = l == 0 && active;
      wait_vm_bar<9>(); issue(11, l);
      if (l == 0) hold(I0{});
      if (first) wait_vm<18>(); else wait_vm<9>();
      bar(); issue(12, l);
      if (l == 0) hold(I1{});
      if (first) wait_vm<27>(); else wait_vm<9>();
      bar(); issue(13, l);
      if (l == 0) hold(I2{});
      bar();
      if (w == 1) sig16(ws, FL_H + (l * 16 + rb) * 16, 1, j, T, lane);
      issue(14, l);
      if (l == 0) hold(I3{});
      bar();
      if (w == 1) sig16(ws, FL_H + 512 + (l * 16 + rb) * 16, 1, j, T, lane);
      if (active) {
        issue(15, l);
        if (w == 0) poll_line(ws, FL_H + (l * 16 + rp) * 16 + mm, 16, T, lane);
        if (first) wait_vm<36>(); else wait_vm<18>();
        bar();
        bar();
        if (first) wait_vm<18>(); else wait_vm<9>();
        bar();
        drop(I0{});
        if (w == 0) poll_line(ws, FL_H + 512 + (l * 16 + rp) * 16 + mm, 16, T, lane);
        if (w != 0) wait_vm<0>();
        bar_lgkm();
        drop(I1{});
        bar_lgkm();
        drop(I2{});
        bar_lgkm();
        drop(I3{});
        bar_lgkm();
        issue(20, l);
        bar_lgkm();
        issue(21, l);
        bar();
        issue(22, l);
        bar();
        if (l == 1) bar();
        if (l == 0) bar();
        if (l == 0 && w == 1) sig16(ws, FL_X2 + rp * 16, 1, mm, T, lane);
        if (l == 0) stage_csq();
      } else if (l == 0) { stage_csq(); issue(20, l); issue(21, l); issue(22, l); }
      else if (b == 112 && w == 0) {
#pragma unroll 1
        for (int n = 0; n < 4; ++n) poll(ws, OFF_FLAG + FL_STARTED * 128 + (n * 64 + lane) * 4, T);
        if (lane == 0) put(ws, OFF_FLAG + FL_GEN * 128, T);
      }
    }
    return;
  }

#pragma unroll 1
  for (int l = 0; l < 2; ++l) {
    u32x4 qf[2];
    {
      IDS();
      if (l == 1) bar();
      constexpr int AH = 10;
      u32x4 af[KSD];
      const unsigned xo = OFF_XA + 2 * l * SZ_XA + tt * (KSD * 1024);
      Stats st;
      const LAS char* wk = slot(0); const LAS char* wv = slot(1); const LAS char* wq = slot(2);
      f32x16 ak = {}, av = {};
      u32x4 rq[4];
      const LAS float* csl = (const LAS float*)(aux + CSQ_L);
      LAS float* stl = (LAS float*)(aux + 8 * 4096 + w * 4096);
      f32x4 ck[4], srs[4], smn[4];
      float csv;
      auto passA = [&](auto FIRST) {
        StatLd sl;
        unsigned tv = 0;
        const int uu = x_ * 14 + idx, uj = uu / 7, ut = uu % 7;
        const int ukind = idx < 14 ? 1 : (x_ < 7 && idx < 22) ? 3 : (x_ < 7 && idx < 24) ? 2 : 0;
        if constexpr (FIRST) {
#pragma unroll
          for (int m = 0; m < 3; ++m)
#pragma unroll
            for (int n = 0; n < 8; ++n) af[8 * m + n] = *(const GAS u32x4*)(x + (size_t)(tt * 32 + 4 * n + (lane >> 4)) * D_ + 128 * m + (lane & 15) * 8);
#pragma unroll
          for (int n = 0; n < 4; ++n) af[24 + n] = *(const GAS u32x4*)(x + (size_t)(tt * 32 + 8 * n + (lane >> 3)) * D_ + 384 + (lane & 7) * 8);
          tv = __hip_atomic_load((gu32*)(ws + OFF_FLAG + FL_GEN * 128), RLX);
          u32x4 uv[8];
#pragma unroll
          for (int n = 0; n < 8; ++n) uv[n] = u32x4{0u, 0u, 0u, 0u};
          if (ukind == 1) unit_load<1>(uv, w1, w3, F_, 112 * uj + 16 * ut, 0, w, lane);
          else if (ukind == 3) unit_load<3>(uv, w2, nullptr, D_, 64 * x_ + 32 * ((idx - 14) & 1), (idx - 14) >> 1, w, lane);
          else if (ukind == 2) unit_load<2>(uv, wo, nullptr, D_, 64 * x_ + 32 * (idx - 22), 0, w, lane);
          SB();
          LAS char* scr = w < 3 ? (LAS char*)smem + 3 * CHB + w * 8704 : aux + 39936;
          float s1a = 0.f, s1b = 0.f, s2a = 0.f, s2b = 0.f;
          static_for<4>([&](auto M) {
            constexpr int m = M, NK = m < 3 ? 8 : 4;
#pragma unroll
            for (int n = 0; n < NK; ++n) {
              if (m < 3) *(LAS u32x4*)(scr + (4 * n + (lane >> 4)) * 272 + (lane & 15) * 16) = af[8 * m + n];
              else *(LAS u32x4*)(scr + (8 * n + (lane >> 3)) * 272 + (lane & 7) * 16) = af[8 * m + n];
            }
            asm volatile("" ::: "memory");
#pragma unroll
            for (int n = 0; n < NK; ++n) {
              const u32x2 a = *(const LAS u32x2*)(scr + r * 272 + n * 32 + 8 * h2), c = *(const LAS u32x2*)(scr + r * 272 + n * 32 + 16 + 8 * h2);
              af[8 * m + n] = u32x4{a.x, a.y, c.x, c.y};
            }
            asm volatile("s_waitcnt lgkmcnt(0)" ::: "memory");
#pragma unroll
            for (int n = 0; n < NK; ++n)
#pragma unroll
              for (int e = 0; e < 4; ++e) { const float lo = bflo(af[8 * m + n][e]), hi = bfhi(af[8 * m + n][e]); s1a += lo; s1b += hi; s2a = __builtin_fmaf(lo, lo, s2a); s2b = __builtin_fmaf(hi, hi, s2b); }
            SB();
          });
          const float s1 = sum32(s1a + s1b), s2 = sum32(s2a + s2b);
          st.mean = s1 * (1.0f / D_);
          st.rs = __builtin_amdgcn_rsqf(fmaxf(s2 * (1.0f / D_) - st.mean * st.mean, 0.f) + 1e-5f);
          if (ukind == 1) unit_finish<1>(uv, rs, OFF_WGU + uu * CHB, OFF_CSG + uu * 512, 0, 0, scr, w, lane);
          else if (ukind == 3) unit_finish<3>(uv, rs, OFF_WDN + x_ * 229376, 0, (idx - 14) & 1, ((idx - 14) >> 1) + 4 * x_, scr, w, lane);
          else if (ukind == 2) unit_finish<2>(uv, rs, OFF_WO + x_ * 65536, 0, idx - 22, x_, scr, w, lane);
          PIN(tv);
          bar_lgkm();
        } else {
          static_for<AH>([&](auto I) { constexpr int ks = I; af[ks] = XLD(ks); });
          stats_issue(sl, rs, OFF_ST + 2 * l * SZ_ST + tt * 3584, lane);
        }
        constexpr int PF = 3;
        u32x4 rk[PF], rv[PF];
#pragma unroll
        for (int i = 0; i < PF; ++i) { rk[i] = lds_rd(wk, i * 1024); rv[i] = lds_rd(wv, i * 1024); }
        SB();
        static_for<KSD>([&](auto I) {
          constexpr int ks = I;
          if constexpr (FIRST && ks == 8) {
            if (ukind != 0) {
              wait_vm<0>();
              const unsigned T = __builtin_amdgcn_readfirstlane(tv) + 1u;
              if (ukind == 1) sig16(ws, FL_WG + uj, 16, ut * 4 + w, T, lane);
              else {
                const int u8 = idx - 14, line = (ukind == 3 && u8 >= 6) ? 1 : 0, word = (ukind == 2 ? (idx - 22) * 4 : u8 < 6 ? 8 + u8 * 4 : (u8 - 6) * 4) + w;
                if (lane < 32) put(ws, OFF_FLAG + (FL_WP + ((lane >> 1) * 16 + x_ + 7 * (lane & 1)) * 2 + line) * 128 + word * 4, T);
              }
            }
            SB();
          }
          if constexpr (ks == 14 || ks == 21) { bar(); SB(); }
          ak = mfma(rk[ks % PF], af[ks], ak); PIN(ak);
          if constexpr (ks + PF < KSD) rk[ks % PF] = lds_rd(wk, (ks + PF) * 1024);
          if constexpr (!FIRST && ks + AH < KSD) af[ks + AH] = XLD(ks + AH);
          if constexpr (!FIRST && ks == 20) { st = stats_finish(sl); PIN(st.mean); PIN(st.rs); }
          SB();
          av = mfma(af[ks], rv[ks % PF], av); PIN(av);
          if constexpr (ks + PF < KSD) rv[ks % PF] = lds_rd(wv, (ks + PF) * 1024);
          if constexpr (ks >= KSD - 4) rq[ks - (KSD - 4)] = lds_rd(wq, (ks - (KSD - 4)) * 1024);
          if constexpr (ks == 21) { if (h2 == 0) stl[r] = st.rs; else stl[32 + r] = st.mean; }
          if constexpr (ks == 22) {
#pragma unroll
            for (int g = 0; g < 4; ++g) ck[g] = *(const LAS f32x4*)(csl + 8 * g + 4 * h2);
            csv = csl[32 + r];
          }
          if constexpr (ks >= 23 && ks < 27) { constexpr int g = ks - 23; srs[g] = *(const LAS f32x4*)(stl + 8 * g + 4 * h2); smn[g] = *(const LAS f32x4*)(stl + 32 + 8 * g + 4 * h2); }
          SB();
        });
      };
      if (l == 0) passA(std::true_type{}); else passA(std::false_type{});
      f32x16 aq = {};
      u32x4 ko[2], vo[2];
      f32x4 cq[4];
      const unsigned kvo = OFF_KV + l * SZ_KV + (tt * 16 + j) * 4096;
      static_for<KSD>([&](auto I) {
        constexpr int ks = I;
        aq = mfma(rq[ks & 3], af[ks], aq); PIN(aq);
        if constexpr (ks + 4 < KSD) rq[ks & 3] = lds_rd(wq, (ks + 4) * 1024);
        if constexpr (ks < 2) {
#pragma unroll
          for (int g = 2 * ks; g < 2 * ks + 2; ++g) {
            float v[4];
#pragma unroll
            for (int e = 0; e < 4; ++e) { v[e] = ak[4 * g + e] - st.mean * ck[g][e]; PIN(v[e]); }
#pragma unroll
            for (int e = 0; e < 4; ++e) { v[e] *= st.rs; PIN(v[e]); }
            ko[g >> 1][(g & 1) * 2] = pk2(v[0], v[1]); ko[g >> 1][(g & 1) * 2 + 1] = pk2(v[2], v[3]);
          }
          PIN(ko[ks]);
        }
        if constexpr (ks == 2) { hst(rs, voff, kvo, ko[0]); hst(rs, voff + 1024, kvo, ko[1]); }
        if constexpr (ks >= 2 && ks < 6) {
          constexpr int g = ks - 2;
          float v[4];
#pragma unroll
          for (int e = 0; e < 4; ++e) { v[e] = av[4 * g + e] - smn[g][e] * csv; PIN(v[e]); }
#pragma unroll
          for (int e = 0; e < 4; ++e) { v[e] *= srs[g][e]; PIN(v[e]); }
          const unsigned p0 = pk2(v[0], v[1]), p1 = pk2(v[2], v[3]);
          vo[g >> 1][(g & 1) * 2] = r == 28 ? 0x3f803f80u : p0; vo[g >> 1][(g & 1) * 2 + 1] = r == 28 ? 0x3f803f80u : p1;
          PIN(vo[g >> 1]);
        }
        if constexpr (ks == 6) { hst(rs, voff + 2048, kvo, vo[0]); hst(rs, voff + 3072, kvo, vo[1]); }
        if constexpr (ks == 7) { LAS char* kvl = aux + (8 + w) * 4096 + lane * 16; lds_wr(kvl, 0, ko[0]); lds_wr(kvl, 1024, ko[1]); lds_wr(kvl, 2048, vo[0]); lds_wr(kvl, 3072, vo[1]); }
        if constexpr (ks >= 16 && ks < 20) cq[ks - 16] = *(const LAS f32x4*)(csl + 64 + 8 * (ks - 16) + 4 * h2);
        if constexpr (ks == 24) { wait_vm_bar<0>(); }
        SB();
      });
      const float qs = st.rs * (0.18898223650461362f * 1.4426950408889634f);
#pragma unroll
      for (int g = 0; g < 4; ++g) {
        float v[4];
#pragma unroll
        for (int e = 0; e < 4; ++e) { v[e] = aq[4 * g + e] - st.mean * cq[g][e]; PIN(v[e]); v[e] *= qs; PIN(v[e]); }
        qf[g >> 1][(g & 1) * 2] = pk2(v[0], v[1]); qf[g >> 1][(g & 1) * 2 + 1] = pk2(v[2], v[3]);
      }
    }
    {
      IDS();
      const float NEG = -1e30f;
      f32x16 cd, co;
#pragma unroll
      for (int i = 0; i < 16; ++i) { const int key = (i & 3) + 8 * (i >> 2) + 4 * h2; cd[i] = key <= r ? 0.f : NEG; co[i] = key > r ? 0.f : NEG; }
      PIN(cd); PIN(co);
      bar();
      const LAS char* kw = opq((const LAS char*)aux + lane * 16 + w * 4096);
      bar();
      float m = NEG;
      f32x16 o = {};
      if (rb >= 2) {
        u32x4 kf[9][2];
#pragma unroll
        for (int it = 0; it < 9; ++it) { kf[it][0] = lds_rd(kw, it * 4096); kf[it][1] = lds_rd(kw, it * 4096 + 1024); }
        SB();
        {
          const f32x16 z = {};
          f32x16 s[3];
          static_for<11>([&](auto IT) {
            constexpr int it = IT;
            if constexpr (it < 9) { s[it % 3] = mfma(kf[it][0], qf[0], it == 0 ? co : it == 8 ? cd : z); s[it % 3] = mfma(kf[it][1], qf[1], s[it % 3]); PIN(s[it % 3]); }
            if constexpr (it >= 2) {
              const f32x16& t = s[(it - 2) % 3];
              float ma = fmaxf(fmaxf(t[0], t[1]), t[2]), mb = fmaxf(fmaxf(t[3], t[4]), t[5]);
              ma = fmaxf(fmaxf(ma, t[6]), t[7]); mb = fmaxf(fmaxf(mb, t[8]), t[9]);
              ma = fmaxf(fmaxf(ma, t[10]), t[11]); mb = fmaxf(fmaxf(mb, t[12]), t[13]);
              ma = fmaxf(fmaxf(ma, t[14]), t[15]);
              m = fmaxf(fmaxf(m, ma), mb);
              PIN(m);
            }
            SB();
          });
        }
        m = -max32(m);
        PIN(m);
        f32x16 cm;
#pragma unroll
        for (int i = 0; i < 16; ++i) { cm[i] = m; co[i] = co[i] + m; }
        u32x4 vf[2][2];
        vf[0][0] = lds_rd(kw, 2048); vf[0][1] = lds_rd(kw, 3072);
        f32x16 s[3];
        s[0] = mfma(kf[0][0], qf[0], co); s[0] = mfma(kf[0][1], qf[1], s[0]);
        s[1] = mfma(kf[1][0], qf[0], cm); s[1] = mfma(kf[1][1], qf[1], s[1]);
#pragma unroll
        for (int i = 0; i < 16; ++i) cd[i] = cd[i] + m;
        SB();
        static_for<9>([&](auto IT) {
          constexpr int it = IT;
          if constexpr (it < 8) { vf[(it + 1) & 1][0] = lds_rd(kw, (it + 1) * 4096 + 2048); vf[(it + 1) & 1][1] = lds_rd(kw, (it + 1) * 4096 + 3072); }
          u32x4 p[2];
          const f32x16& c = s[it % 3];
          if constexpr (it + 2 < 9) { s[(it + 2) % 3] = mfma(kf[it + 2][0], qf[0], it + 2 == 8 ? cd : cm); }
#pragma unroll
          for (int q = 0; q < 4; ++q) p[0][q] = pk2(__builtin_amdgcn_exp2f(c[2 * q]), __builtin_amdgcn_exp2f(c[2 * q + 1]));
          PIN(p[0]);
          SB();
          if constexpr (it + 2 < 9) { s[(it + 2) % 3] = mfma(kf[it + 2][1], qf[1], s[(it + 2) % 3]); PIN(s[(it + 2) % 3]); }
          o = mfma(vf[it & 1][0], p[0], o);
#pragma unroll
          for (int q = 0; q < 4; ++q) p[1][q] = pk2(__builtin_amdgcn_exp2f(c[8 + 2 * q]), __builtin_amdgcn_exp2f(c[8 + 2 * q + 1]));
          PIN(p[1]);
          SB();
          o = mfma(vf[it & 1][1], p[1], o); PIN(o);
          SB();
        });
      } else {
        const LAS char* kq = kw;
        const f32x16 z = {};
#pragma unroll
        for (int it = 0; it < 9; ++it) {
          if (tt - 8 + it >= 0) {
            f32x16 s = mfma(lds_rd(kq, it * 4096), qf[0], it == 0 ? co : it == 8 ? cd : z);
            s = mfma(lds_rd(kq, it * 4096 + 1024), qf[1], s);
#pragma unroll
            for (int i = 0; i < 16; i += 2) m = fmaxf(fmaxf(m, s[i]), s[i + 1]);
          }
        }
        m = -max32(m);
        f32x16 cm;
#pragma unroll
        for (int i = 0; i < 16; ++i) { cd[i] += m; cm[i] = m; }
#pragma unroll
        for (int it = 0; it < 9; ++it) {
          if (it >= 1 && tt - 8 + it >= 0) {
            f32x16 s = mfma(lds_rd(kq, it * 4096), qf[0], it == 8 ? cd : cm);
            s = mfma(lds_rd(kq, it * 4096 + 1024), qf[1], s);
            u32x4 p[2];
            PACK16(p, pk2(__builtin_amdgcn_exp2f(s[i0]), __builtin_amdgcn_exp2f(s[i1])));
            o = mfma(lds_rd(kq, it * 4096 + 2048), p[0], o);
            o = mfma(lds_rd(kq, it * 4096 + 3072), p[1], o);
          }
        }
      }
      const unsigned lu = __float_as_uint(o[12]);
      auto rr = __builtin_amdgcn_permlane32_swap(lu, lu, false, false);
      const float inv = __builtin_amdgcn_rcpf(__uint_as_float(rr[1]));
      u32x4 ob[2];
      PACK16(ob, pk2(o[i0] * inv, o[i1] * inv));
      const unsigned oo = OFF_OA + l * SZ_OA + (tt * KSO + 2 * j) * 1024;
      hst(rs, voff, oo, ob[0]); hst(rs, voff + 1024, oo, ob[1]);
      wait_vm_bar<0>();
    }
    auto proj_init = [&](int xin) { IDS(); const unsigned xo = OFF_XA + xin * SZ_XA + (ttp * KSD + 4 * fs + 2 * kh) * 1024;
      u32x4 xr[2];
      if (xin == 0) {
        const unsigned short* xp = x + (size_t)(ttp * 32 + r) * D_ + 64 * fs + 32 * kh + 4 * h2;
#pragma unroll
        for (int g = 0; g < 4; ++g) { const u32x2 v = *(const GAS u32x2*)(xp + 8 * g); xr[g >> 1][(g & 1) * 2] = v.x; xr[g >> 1][(g & 1) * 2 + 1] = v.y; }
      } else { xr[0] = hld(rs, voff, xo); xr[1] = hld(rs, voff + 1024, xo); }
      f32x16 a;
#pragma unroll
      for (int q = 0; q < 8; ++q) { const unsigned xv = xr[q >> 2][q & 3]; a[2 * q] = bflo(xv); a[2 * q + 1] = bfhi(xv); }
      PIN(a);
      return a; };
    auto proj_finish = [&](f32x16 own, const f32x16& snd, int xout, bool final_) {
      IDS();
      u32x4 xres[2];
      LAS f32x4* xs = (LAS f32x4*)(aux) + (tt2 * 2 + kh) * 256 + lane;
      LAS f32x4* xrv = (LAS f32x4*)(aux) + (tt2 * 2 + (kh ^ 1)) * 256 + lane;
#pragma unroll
      for (int g = 0; g < 4; ++g) { f32x4 v = {snd[4 * g], snd[4 * g + 1], snd[4 * g + 2], snd[4 * g + 3]}; xs[g * 64] = v; }
      bar_lgkm();
#pragma unroll
      for (int g = 0; g < 4; ++g) { const f32x4 v = xrv[g * 64]; own[4 * g] += v.x; own[4 * g + 1] += v.y; own[4 * g + 2] += v.z; own[4 * g + 3] += v.w; }
#pragma unroll
      for (int q = 0; q < 8; ++q) xres[q >> 2][q & 3] = pk2(own[2 * q], own[2 * q + 1]);
      if (final_) {
        LAS char* img = aux + 16384;
#pragma unroll
        for (int g = 0; g < 4; ++g) { u32x2 v = {xres[g >> 1][(g & 1) * 2], xres[g >> 1][(g & 1) * 2 + 1]}; *(LAS u32x2*)(img + (tt2 * 32 + r) * 144 + kh * 64 + g * 16 + h2 * 8) = v; }
        bar_lgkm();
#pragma unroll
        for (int n = 0; n < 2; ++n) {
          const int row = w * 16 + n * 8 + (lane >> 3);
          const u32x4 v = *(const LAS u32x4*)(img + row * 144 + (lane & 7) * 16);
          *(GAS u32x4*)(out + (size_t)((4 * rp + 2 * th) * 32 + row) * D_ + 64 * fs + (lane & 7) * 8) = v;
        }
      } else {
        const unsigned xo = OFF_XA + xout * SZ_XA + (ttp * KSD + 4 * fs + 2 * kh) * 1024;
        hst(rs, voff, xo, xres[0]); hst(rs, voff + 1024, xo, xres[1]);
        float s1[4], s2[4];
#pragma unroll
        for (int g = 0; g < 4; ++g) {
          s1[g] = (own[4 * g] + own[4 * g + 1]) + (own[4 * g + 2] + own[4 * g + 3]);
          s2[g] = __builtin_fmaf(own[4 * g], own[4 * g], own[4 * g + 1] * own[4 * g + 1]) + __builtin_fmaf(own[4 * g + 2], own[4 * g + 2], own[4 * g + 3] * own[4 * g + 3]);
        }
        const float t1 = sum32((s1[0] + s1[1]) + (s1[2] + s1[3])), t2 = sum32((s2[0] + s2[1]) + (s2[2] + s2[3]));
        if (h2 == 0) { u32x2 v = {__float_as_uint(t1), __float_as_uint(t2)}; __builtin_amdgcn_raw_buffer_store_b64(v, rs, r * 8, OFF_ST + xout * SZ_ST + (ttp * 14 + 2 * fs + kh) * 256, 16); }
      }
    };
    if (active) {
      IDS();
      f32x16 own = proj_init(2 * l), snd = {};
      bar();
      u32x4 af[16];
      const unsigned ao = OFF_OA + l * SZ_OA + (ttp * KSO + kh * 16) * 1024;
      constexpr int AH = 10;
      const unsigned oa = ao + 2 * fs * 1024, ob_ = oa - 16 * 1024;
#define OLD(i) hld(rs, voff + (i) * 1024, (i) + 2 * fs < 16 ? oa : ob_)
      static_for<AH>([&](auto I) { constexpr int i = I; af[i] = OLD(i); });
      const LAS char* wbo[3] = {opq(ring + 3 * CHB + kh * 3072), opq(ring + 0 * CHB + kh * 3072), opq((const LAS char*)aux + WOT_L + lane * 16 + kh * 3072)};
      const LAS char* wbs[3] = {opq(ring + 3 * CHB + kh * 2048 + (kh ^ 1) * 1024), opq(ring + 0 * CHB + kh * 2048 + (kh ^ 1) * 1024), opq((const LAS char*)aux + WOT_L + lane * 16 + kh * 2048 + (kh ^ 1) * 1024)};
      constexpr int PF = 2;
      u32x4 wo_[PF], wsn[PF];
#pragma unroll
      for (int i = 0; i < PF; ++i) { wo_[i] = lds_rd(wbo[0], i * 4096); wsn[i] = lds_rd(wbs[0], i * 4096); }
      SB();
      static_for<16>([&](auto I) {
        constexpr int i = I, n = i + PF;
        own = mfma(wo_[i % PF], af[i], own); PIN(own);
        if constexpr (n < 16) wo_[i % PF] = lds_rd(wbo[n / 7], (n % 7) * 4096);
        if constexpr (i + AH < 16) af[i + AH] = OLD(i + AH);
        SB();
        snd = mfma(wsn[i % PF], af[i], snd); PIN(snd);
        if constexpr (n < 16) wsn[i % PF] = lds_rd(wbs[n / 7], (n % 7) * 4096);
        SB();
      });
      proj_finish(own, snd, 2 * l + 1, false);
      wait_vm_bar<0>();
    }
    {
      IDS();
      bar();
      constexpr int AH = 10;
      u32x4 af[KSD];
      const unsigned xo = OFF_XA + (2 * l + 1) * SZ_XA + tt * (KSD * 1024);
      StatLd sl;
      Stats st;
      static_for<AH>([&](auto I) { constexpr int ks = I; af[ks] = XLD(ks); });
      stats_issue(sl, rs, OFF_ST + (2 * l + 1) * SZ_ST + tt * 3584, lane);
      const LAS float* csl = (const LAS float*)(aux + CSG_L) + 4 * h2;
      const unsigned ho = OFF_HA + l * SZ_HA + tt * HSTR + 7 * j * 1024;
      f32x16 prev = {}, acc = {};
      u32x4 wr[4];
      {
        const LAS char* w0 = slot(5); const LAS char* w1 = slot(6);
        constexpr int PF = 3;
        u32x4 r0[PF], r1[PF];
#pragma unroll
        for (int i = 0; i < PF; ++i) { r0[i] = lds_rd(w0, i * 1024); r1[i] = lds_rd(w1, i * 1024); }
        SB();
        static_for<KSD>([&](auto I) {
          constexpr int ks = I;
          prev = mfma(r0[ks % PF], af[ks], prev); PIN(prev);
          if constexpr (ks + PF < KSD) r0[ks % PF] = lds_rd(w0, (ks + PF) * 1024);
          if constexpr (ks + AH < KSD) af[ks + AH] = XLD(ks + AH);
          if constexpr (ks == 20) { st = stats_finish(sl); PIN(st.mean); PIN(st.rs); }
          SB();
          acc = mfma(r1[ks % PF], af[ks], acc); PIN(acc);
          if constexpr (ks + PF < KSD) r1[ks % PF] = lds_rd(w1, (ks + PF) * 1024);
          SB();
        });
      }
      const float c1 = -1.4426950408889634f * st.rs, vk = __builtin_amdgcn_rcpf(st.rs * st.rs);
      u32x4 ov;
      f32x4 cc[4];
      float ea[8], eb[8], ex[8];
#define GU_STAGE(P, v, S) do { \
        if constexpr ((S) == 0) { ea[v] = P[v] - st.mean * cc[(v) >> 2][(v) & 3]; PIN(ea[v]); eb[v] = P[(v) + 8] - st.mean * cc[2 + ((v) >> 2)][(v) & 3]; PIN(eb[v]); } \
        else if constexpr ((S) == 1) { ex[v] = c1 * ea[v]; PIN(ex[v]); eb[v] = ea[v] * eb[v]; PIN(eb[v]); } \
        else if constexpr ((S) == 2) { ex[v] = __builtin_amdgcn_exp2f(ex[v]); PIN(ex[v]); } \
        else if constexpr ((S) == 3) { ex[v] = __builtin_fmaf(ex[v], vk, vk); PIN(ex[v]); } \
        else if constexpr ((S) == 4) { ex[v] = __builtin_amdgcn_rcpf(ex[v]); PIN(ex[v]); } \
        else { eb[v] = eb[v] * ex[v]; PIN(eb[v]); if constexpr ((v) & 1) { ov[(v) >> 1] = pk2(eb[(v) - 1], eb[v]); PIN(ov); } } } while (0)
#define GU_GAP(P, g, base, sp) static_for<8>([&](auto V_) { constexpr int v_ = V_; constexpr int s_ = (g) - (base) - (sp) * v_; if constexpr (s_ >= 0 && s_ < 6) GU_STAGE(P, v_, s_); })
#define GU_CC(t) _Pragma("unroll") for (int g = 0; g < 4; ++g) cc[g] = *(const LAS f32x4*)(csl + (t) * 32 + 8 * g)
      GU_CC(0);
      bar();
      { const LAS char* w2 = slot(7);
#pragma unroll
        for (int i = 0; i < 4; ++i) wr[i] = lds_rd(w2, i * 1024); }
      SB();
      f32x16 t1 = acc;
      static_for<5>([&](auto T) {
        constexpr int t = T + 2;
        const LAS char* wl = slot(5 + t);
        const LAS char* wn = slot(6 + t);
        acc = f32x16{};
        static_for<KSD>([&](auto I) {
          constexpr int ks = I;
          if constexpr (ks == 14) {
            if constexpr (t < 6) bar();
            else asm volatile("s_waitcnt vmcnt(1)\n\ts_barrier" ::: "memory");
            SB();
          }
          acc = mfma(wr[ks & 3], af[ks], acc); PIN(acc);
          if constexpr (ks + 4 < KSD) wr[ks & 3] = lds_rd(wl, (ks + 4) * 1024);
          else if constexpr (t < 6) wr[ks & 3] = lds_rd(wn, (ks + 4 - KSD) * 1024);
          if constexpr (t == 2) {
            if constexpr (ks < 14) { GU_GAP(prev, ks, 0, 1); }
            if constexpr (ks == 12) { GU_CC(1); }
            if constexpr (ks == 13) hst(rs, voff, ho, ov);
            if constexpr (ks >= 14) { GU_GAP(t1, ks, 15, 1); }
            if constexpr (ks == 27) { hst(rs, voff + 1024, ho, ov); GU_CC(2); }
          } else {
            GU_GAP(prev, ks, 1, 3);
            if constexpr (ks == 27) { hst(rs, voff + (t - 1) * 1024, ho, ov); GU_CC(t); }
          }
          SB();
        });
        prev = acc;
      });
      static_for<13>([&](auto G) { constexpr int g = G; GU_GAP(prev, g, 0, 1); });
      hst(rs, voff + 6 * 1024, ho, ov);
      wait_vm_bar<0>();
    }
    if (active) {
      IDS();
      f32x16 own = proj_init(2 * l + 1), snd = {};
      bar();
      constexpr int WIN = DN_WIN;
      constexpr int NR = WIN + 2;
      u32x4 af[NR];
      const unsigned ao = OFF_HA + l * SZ_HA + ttp * HSTR + kh * 56 * 1024;
#define HLD(i) hld(rs, voff + (kk_of(i) % 7) * 1024, ao + (((kk_of(i) / 7) + fs) & 7) * 7168)
      static_for<WIN>([&](auto I) { constexpr int i = I; af[i] = HLD(i); });
      const int oo_ = kh * 3072, so_ = kh * 2048 + (kh ^ 1) * 1024;
      constexpr int PF = 2;
      u32x4 wo_[PF], wsn[PF];
      const LAS char* po = opq(ring + 0 * CHB + oo_); const LAS char* ps = opq(ring + 0 * CHB + so_);
#pragma unroll
      for (int i = 0; i < PF; ++i) { wo_[i] = lds_rd(po, i * 4096); wsn[i] = lds_rd(ps, i * 4096); }
      SB();
      const LAS char* pon = po; const LAS char* psn = ps;
      static_for<56>([&](auto I) {
        constexpr int i = I, c = i / 7, s = i % 7, n = s + PF;
        if constexpr (s == 0) { po = pon; ps = psn; if constexpr (c < 7) { pon = opq(ring + ((13 + c) & 3) * CHB + oo_); psn = opq(ring + ((13 + c) & 3) * CHB + so_); } }
        if constexpr (s == 3) { bar(); SB(); }
        const u32x4 a = af[i % NR];
        own = mfma(wo_[i % PF], a, own); PIN(own);
        if constexpr (n < 7) wo_[i % PF] = lds_rd(po, n * 4096); else if constexpr (c < 7) wo_[i % PF] = lds_rd(pon, (n - 7) * 4096);
        SB();
        snd = mfma(wsn[i % PF], a, snd); PIN(snd);
        if constexpr (n < 7) wsn[i % PF] = lds_rd(ps, n * 4096); else if constexpr (c < 7) wsn[i % PF] = lds_rd(psn, (n - 7) * 4096);
        if constexpr (i + WIN < 56) af[(i + WIN) % NR] = HLD(i + WIN);
        SB();
      });
      proj_finish(own, snd, 2, l == 1);
      if (l == 0) wait_vm_bar<0>();
    }
  }
}

}

extern "C" void kernel_launch(void* const* d_in, const int* in_sizes, int n_in, void* d_out, int out_size, void* d_ws, size_t ws_size,
                              hipStream_t stream) {
  (void)hipFuncSetAttribute((const void*)k_main, hipFuncAttributeMaxDynamicSharedMemorySize, LDS_BYTES);
  k_main<<<256, 512, LDS_BYTES, stream>>>((char*)d_ws, (unsigned short*)d_out, (const unsigned short*)d_in[0], (const unsigned short*)d_in[1], (const unsigned short*)d_in[2],
                                          (const unsigned short*)d_in[4], (const unsigned short*)d_in[5], (const unsigned short*)d_in[6]);
}
```

```cpp
#include <hip/hip_runtime.h>
#include <stdint.h>
#include <utility>

typedef __attribute__((ext_vector_type(8))) __bf16 bf16x8;
typedef __attribute__((ext_vector_type(2))) __bf16 bf16x2;
typedef __attribute__((ext_vector_type(2))) float f32x2;
typedef __attribute__((ext_vector_type(4))) float f32x4;
typedef __attribute__((ext_vector_type(16))) float f32x16;
typedef __attribute__((ext_vector_type(4))) unsigned u32x4;
typedef __attribute__((ext_vector_type(2))) unsigned u32x2;
#define DEV __device__ __forceinline__
#define LAS __attribute__((address_space(3)))
#define GAS __attribute__((address_space(1)))
typedef GAS unsigned gu32;
#define RLX __ATOMIC_RELAXED, __HIP_MEMORY_SCOPE_AGENT

#define SB() __builtin_amdgcn_sched_barrier(0)
#define PIN(x) asm volatile("" : "+v"(x))

namespace {

constexpr int D_ = 448, F_ = 1792;
constexpr int NTT = 64;
constexpr int KSD = 28;
constexpr int KSO = 32;
constexpr int KSF = 112;
constexpr int CHB = 28672;

constexpr unsigned OFF_FLAG = 0;
constexpr int FL_GEN = 0;
constexpr int FL_STARTED = 1;
constexpr int FL_START = 16;
constexpr int FL_WG = FL_START + 256;
constexpr int FL_WP = FL_WG + 256;
constexpr int FL_KV = FL_WP + 512;
constexpr int FL_O = FL_KV + 1024;
constexpr int FL_X1 = FL_O + 512;
constexpr int FL_H = FL_X1 + 512;
constexpr int FL_X2 = FL_H + 1024;
constexpr int NFLAG = FL_X2 + 256;
constexpr unsigned OFF_WQKV = 576 * 1024;
constexpr unsigned OFF_WO   = OFF_WQKV + 16 * 3 * CHB;
constexpr unsigned OFF_WGU  = OFF_WO + 7 * 64 * 1024;
constexpr unsigned OFF_WDN  = OFF_WGU + 16 * 7 * CHB;
constexpr unsigned OFF_CSQ  = OFF_WDN + 7 * 224 * 1024;
constexpr unsigned OFF_CSG  = OFF_CSQ + 48 * 512;
constexpr unsigned SZ_XA    = NTT * KSD * 1024;
constexpr unsigned OFF_XA   = OFF_CSG + 112 * 512;
constexpr unsigned SZ_ST    = NTT * 14 * 32 * 8;
constexpr unsigned OFF_ST   = OFF_XA + 4 * SZ_XA;
constexpr unsigned SZ_KV    = NTT * 16 * 4096;
constexpr unsigned OFF_KV   = OFF_ST + 4 * SZ_ST;
constexpr unsigned SZ_OA    = NTT * KSO * 1024;
constexpr unsigned OFF_OA   = OFF_KV + 2 * SZ_KV;
constexpr unsigned HSTR = KSF * 1024;
constexpr unsigned SZ_HA    = NTT * HSTR;
constexpr unsigned OFF_HA   = OFF_OA + 2 * SZ_OA;
constexpr unsigned WS_END   = OFF_HA + 2 * SZ_HA;

template <class F, int... I> DEV void static_for_impl(F&& f, std::integer_sequence<int, I...>) { (f(std::integral_constant<int, I>{}), ...); }
template <int N, class F> DEV void static_for(F&& f) { static_for_impl(f, std::make_integer_sequence<int, N>{}); }

DEV int lane_id() { int l; asm volatile("v_mbcnt_lo_u32_b32 %0, -1, 0\n\tv_mbcnt_hi_u32_b32 %0, -1, %0" : "=v"(l)); return l; }
DEV unsigned pk2(float a, float b) { f32x2 v = {a, b}; return __builtin_bit_cast(unsigned, __builtin_convertvector(v, bf16x2)); }
DEV f32x16 mfma(u32x4 a, u32x4 b, f32x16 c) {
  return __builtin_amdgcn_mfma_f32_32x32x16_bf16(__builtin_bit_cast(bf16x8, a), __builtin_bit_cast(bf16x8, b), c, 0, 0, 0);
}
DEV float bflo(unsigned u) { return __uint_as_float(u << 16); }
DEV float bfhi(unsigned u) { return __uint_as_float(u & 0xffff0000u); }
DEV float sum32(float v) {
  const unsigned u = __float_as_uint(v);
  auto rr = __builtin_amdgcn_permlane32_swap(u, u, false, false);
  return __uint_as_float(rr[0]) + __uint_as_float(rr[1]);
}
DEV float max32(float v) {
  const unsigned u = __float_as_uint(v);
  auto rr = __builtin_amdgcn_permlane32_swap(u, u, false, false);
  return fmaxf(__uint_as_float(rr[0]), __uint_as_float(rr[1]));
}
template <int N> DEV void wait_vm() { asm volatile("s_waitcnt vmcnt(%0)" ::"n"(N) : "memory"); }
template <int N> DEV void wait_vm_bar() { asm volatile("s_waitcnt vmcnt(%0) lgkmcnt(0)\n\ts_barrier" ::"n"(N) : "memory"); }
DEV void bar_lgkm() { asm volatile("s_waitcnt lgkmcnt(0)\n\ts_barrier" ::: "memory"); }
DEV u32x4 lds_rd(const LAS char* base, int off) { return *(const LAS u32x4*)(base + off); }
DEV void lds_wr(LAS char* base, int off, u32x4 v) { *(LAS u32x4*)(base + off) = v; }
DEV u32x4 gld(const char* p) { return *(const GAS u32x4*)(p); }
typedef __amdgpu_buffer_rsrc_t rsrc_t;
DEV u32x4 hld(rsrc_t rs, unsigned voff, unsigned soff) { return __builtin_amdgcn_raw_buffer_load_b128(rs, voff, soff, 16); }
DEV void hst(rsrc_t rs, unsigned voff, unsigned soff, u32x4 v) { __builtin_amdgcn_raw_buffer_store_b128(v, rs, voff, soff, 16); asm volatile("s_nop 1" ::"v"(v) : "memory"); }

DEV void poll(const char* ws, unsigned vo, unsigned T) {
  unsigned cnt, v;
  asm volatile(
      "s_mov_b32 %1, 0\n"
      "1:\n\t"
      "global_load_dword %0, %2, %3 sc1\n\t"
      "s_waitcnt vmcnt(0)\n\tv_cmp_ne_u32 vcc, %4, %0\n\ts_nop 1\n\ts_cbranch_vccz 2f\n\ts_sleep 1\n\t"
      "s_add_u32 %1, %1, 1\n\ts_cmp_lt_u32 %1, 0x8000\n\ts_cbranch_scc1 1b\n"
      "2:"
      : "=&v"(v), "=&s"(cnt) : "v"(vo), "s"(ws), "s"(T) : "memory", "scc", "vcc");
}
DEV void poll_line(const char* ws, int line, int nwords, unsigned T, int lane) { poll(ws, OFF_FLAG + line * 128 + min(lane, nwords - 1) * 4, T); }
DEV void put(char* ws, unsigned off, unsigned T) { __hip_atomic_store((gu32*)(ws + off), T, RLX); }
DEV void sig16(char* ws, int line0, int stride, int word, unsigned T, int lane) { if (lane < 16) put(ws, OFF_FLAG + (line0 + lane * stride) * 128 + word * 4, T); }

struct Stats { float mean, rs; };
constexpr int G1T = 4, G1S = 8 * G1T;
__host__ __device__ constexpr int kk_of(int i) { return i < G1S ? 7 * (i / G1T) + i % G1T : 7 * ((i - G1S) / (7 - G1T)) + G1T + (i - G1S) % (7 - G1T); }

typedef short s16x4 __attribute__((ext_vector_type(4)));
DEV u32x2 tr_read(const LAS char* p) { return __builtin_bit_cast(u32x2, __builtin_amdgcn_ds_read_tr16_b64_v4i16((LAS s16x4*)p)); }
DEV u32x4 tr_frag(const LAS char* scr, int lane) {
  const LAS char* rd = scr + (4 * (lane >> 5) + ((lane & 15) >> 2)) * 64 + ((lane >> 4) & 1) * 32 + (lane & 3) * 8;
  const u32x2 lo = tr_read(rd), hi = tr_read(rd + 512);
  return u32x4{lo.x, lo.y, hi.x, hi.y};
}
DEV float frag_sum(u32x4 v) { float t = 0.f;
#pragma unroll
  for (int e = 0; e < 4; ++e) t += bflo(v[e]) + bfhi(v[e]);
  return t; }
template <int KIND>
DEV void unit_load(u32x4 (&v)[8], const unsigned short* __restrict__ src, const unsigned short* __restrict__ src2, int ld, int col, int a2, int q, int lane) {
  constexpr int NI = KIND == 2 ? 8 : 7;
  const int p = lane & 3, rw = lane >> 2;
  const unsigned short* sp = KIND == 1 ? (p < 2 ? src + col + 8 * p : src2 + col + 8 * (p - 2)) : src + col + 8 * p;
#pragma unroll
  for (int n = 0; n < NI; ++n) {
    const int ks = q + 4 * n;
    int row; bool ok = true;
    if (KIND == 2) { const int d = 16 * (ks & 1) + rw; ok = d < 28; row = 28 * (ks >> 1) + (ok ? d : 0); }
    else row = (KIND == 3 ? 448 * (a2 & 3) : 0) + 16 * ks + rw;
    const u32x4 t = *(const GAS u32x4*)(sp + (size_t)row * ld);
    v[n] = ok ? t : u32x4{0u, 0u, 0u, 0u};
  }
}
template <int KIND>
DEV void unit_finish(const u32x4 (&v)[8], rsrc_t rs, unsigned dst, unsigned cs, int a1, int a2, LAS char* scr, int q, int lane) {
  constexpr int NI = KIND == 2 ? 8 : 7;
  float sum = 0.f;
#pragma unroll
  for (int h = 0; h < 2; ++h) {
#pragma unroll
    for (int n = 4 * h; n < 4 * h + 4; ++n) if (n < NI) *(LAS u32x4*)(scr + (n & 3) * 1024 + lane * 16) = v[n];
    asm volatile("" ::: "memory");
#pragma unroll
    for (int n = 4 * h; n < 4 * h + 4; ++n) {
      if (n < NI) {
        const int i = q + 4 * n;
        const u32x4 f = tr_frag(scr + (n & 3) * 1024, lane);
        unsigned doff;
        if (KIND == 1) doff = i * 1024;
        else if (KIND == 2) doff = ((((i - 2 * a2) & 15) * 2 + (i >> 4)) * 2 + a1) * 1024;
        else {
          const int ks = 28 * (a2 & 3) + i, kh = ks / 56, kk = ks % 56, j8 = (kk / 7 - (a2 >> 2)) & 7, t = kk % 7;
          const int step = t < G1T ? G1T * j8 + t : G1S + (7 - G1T) * j8 + (t - G1T);
          doff = ((step * 2 + kh) * 2 + a1) * 1024;
        }
        hst(rs, lane * 16, dst + doff, f);
        if (KIND == 1) sum += frag_sum(f);
      }
    }
    asm volatile("s_waitcnt lgkmcnt(0)" ::: "memory");
  }
  if (KIND == 1) {
    sum = sum32(sum);
    if (lane < 32) __builtin_amdgcn_raw_buffer_store_b32(__float_as_uint(sum), rs, lane * 4, cs + q * 128, 16);
  }
}

constexpr int AUX = 4 * CHB;
constexpr int LDS_BYTES = AUX + 49152;
#define PACK16(o, expr) _Pragma("unroll") for (int q_ = 0; q_ < 8; ++q_) { const int i0 = 2 * q_, i1 = 2 * q_ + 1; (void)i0; (void)i1; o[q_ >> 2][q_ & 3] = expr; }
struct StatLd { u32x2 v[7]; };
DEV void stats_issue(StatLd& s, rsrc_t rs, unsigned soff, int lane) {
  const unsigned vo = (lane >> 5) * 1792 + (lane & 31) * 8;
#pragma unroll
  for (int i = 0; i < 7; ++i) s.v[i] = __builtin_amdgcn_raw_buffer_load_b64(rs, vo + i * 256, soff, 16);
}
DEV Stats stats_finish(const StatLd& s) {
  float s1 = 0.f, s2 = 0.f;
#pragma unroll
  for (int i = 0; i < 7; ++i) { s1 += __uint_as_float(s.v[i].x); s2 += __uint_as_float(s.v[i].y); }
  s1 = sum32(s1); s2 = sum32(s2);
  Stats o; o.mean = s1 * (1.0f / D_);
  const float var = fmaxf(s2 * (1.0f / D_) - o.mean * o.mean, 0.f);
  o.rs = __builtin_amdgcn_rsqf(var + 1e-5f);
  return o;
}

template <int N> DEV void dma_n(rsrc_t rs, unsigned voff, unsigned lds, unsigned soff) {
  if constexpr (N == 4)
    asm volatile("s_mov_b32 m0, %0\n\ts_nop 0\n\tbuffer_load_dwordx4 %1, %2, %3 offen lds\n\tbuffer_load_dwordx4 %1, %2, %3 offen offset:1024 lds\n\t"
                 "buffer_load_dwordx4 %1, %2, %3 offen offset:2048 lds\n\tbuffer_load_dwordx4 %1, %2, %3 offen offset:3072 lds" ::"s"(lds), "v"(voff), "s"(rs), "s"(soff) : "memory");
  else
    asm volatile("s_mov_b32 m0, %0\n\ts_nop 0\n\tbuffer_load_dwordx4 %1, %2, %3 offen lds\n\tbuffer_load_dwordx4 %1, %2, %3 offen offset:1024 lds" ::"s"(lds), "v"(voff), "s"(rs), "s"(soff) : "memory");
}
DEV void dma_chunk(rsrc_t rs, unsigned voff, unsigned lds, unsigned soff, int w) {
  const unsigned o = (w - 1) * 9216;
  dma_n<4>(rs, voff, lds + o, soff + o); dma_n<4>(rs, voff, lds + o + 4096, soff + o + 4096);
  if (w == 3) dma_n<2>(rs, voff, lds + o + 8192, soff + o + 8192);
  else asm volatile("s_mov_b32 m0, %0\n\ts_nop 0\n\tbuffer_load_dwordx4 %1, %2, %3 offen lds" ::"s"(lds + o + 8192), "v"(voff), "s"(rs), "s"(soff + o + 8192) : "memory");
}
DEV void bar() { asm volatile("s_barrier" ::: "memory"); }
DEV const LAS char* opq(const LAS char* p) { asm volatile("" : "+v"(p)); return p; }

constexpr int DN_WIN = 14;

__global__ __launch_bounds__(512, 2) void k_main(char* __restrict__ ws, unsigned short* __restrict__ out, const unsigned short* __restrict__ x, const unsigned short* __restrict__ wqkv,
                                                  const unsigned short* __restrict__ wo, const unsigned short* __restrict__ w1, const unsigned short* __restrict__ w3, const unsigned short* __restrict__ w2) {
  extern __shared__ __attribute__((aligned(16))) char smem[];
  const int w8_ = __builtin_amdgcn_readfirstlane(threadIdx.x >> 6);
  const bool helper = w8_ >= 4;
  const int w_ = w8_ & 3;
  const int b = blockIdx.x, idx = b >> 3;
  const int x_ = b & 7;
  const int rb_ = 4 * (x_ & 3) + (idx >> 3), j_ = 8 * (x_ >> 2) + (idx & 7);
  const int rp_ = rb_, m_ = (idx & 7) < 7 ? 7 * (x_ >> 2) + (idx & 7) : 14 + (x_ >> 2);
  const bool active = m_ < 14;
  const unsigned lds0 = __builtin_amdgcn_readfirstlane((unsigned)(uintptr_t)smem);
  const rsrc_t rs = __builtin_amdgcn_make_buffer_rsrc(ws, 0, WS_END, 0x00020000);
#define IDS() \
  int w = w_, rb = rb_, j = j_, rp = rp_, mm = m_; asm volatile("" : "+s"(w), "+s"(rb), "+s"(j), "+s"(rp), "+s"(mm)); \
  const int lane = lane_id(), r = lane & 31, h2 = lane >> 5; \
  const int th = mm >= 7 ? 1 : 0, fs = mm - 7 * th, tt2 = w & 1, kh = w >> 1; \
  const int tt = 4 * rb + w, ttp = 4 * rp + 2 * th + tt2; \
  const unsigned voff = lane * 16; \
  const LAS char* ring = (const LAS char*)smem + lane * 16; \
  LAS char* aux = (LAS char*)smem + AUX; \
  (void)r; (void)h2; (void)fs; (void)kh; (void)tt; (void)ttp; (void)voff; (void)ring; (void)aux; (void)tt2; (void)rp;
#define slot(c) opq(ring + ((c) & 3) * CHB)
#define XLD(p) hld(rs, voff + (p) * 1024, xo)
  constexpr int CSQ_L = 0;
  constexpr int CSG_L = 16384;
  constexpr int WOT_L = 24576;
  if (helper) {
    auto issue = [&](int c, int l) {
      const int sl = c & 3;
      if (c >= 20) { c -= 20; ++l; }
      const bool proj = (c >= 3 && c < 5) || c >= 12;
      int w = w_, j = j_, mm = m_; asm volatile("" : "+s"(w), "+s"(j), "+s"(mm));
      const int fs = mm >= 7 ? mm - 7 : mm;
      unsigned src;
      if (c < 3) src = OFF_WQKV + (j * 3 + c) * CHB;
      else if (c < 5) src = OFF_WO + fs * 65536 + (c - 3) * CHB;
      else if (c < 12) src = OFF_WGU + (j * 7 + c - 5) * CHB;
      else src = OFF_WDN + fs * 229376 + (c - 12) * CHB;
      if (!(l < 2 && (active || !proj))) src = WS_END;
      if (w != 0) dma_chunk(rs, lane_id() * 16, lds0 + sl * CHB, src, w);
    };
    unsigned tv = __hip_atomic_load((gu32*)(ws + OFF_FLAG + FL_GEN * 128), RLX);
    {
      const int lane = lane_id(), q = w_, j = j_;
      const rsrc_t rq = __builtin_amdgcn_make_buffer_rsrc((void*)wqkv, 0, D_ * 3 * D_ * 2, 0x00020000);
      const unsigned wvo = (lane >> 2) * (3 * D_ * 2) + (lane & 3) * 16;
#pragma unroll
      for (int p = 0; p < 3; ++p)
#pragma unroll
        for (int n = 0; n < 7; ++n)
          asm volatile("s_mov_b32 m0, %0\n\ts_nop 0\n\tbuffer_load_dwordx4 %1, %2, %3 offen lds"
                       ::"s"(lds0 + p * CHB + (q + 4 * n) * 1024), "v"(wvo), "s"(rq), "s"(16 * (q + 4 * n) * (3 * D_ * 2) + 56 * j - 8 * (j & 1) + (p == 2 ? 0 : (p + 1) * D_ * 2)) : "memory");
    }
    auto qkv_tiles = [&](int p) {
      const int lane = lane_id(), q = w_, j = j_;
      float sum = 0.f;
      u32x4 fr[7];
#pragma unroll
      for (int n = 0; n < 7; ++n) fr[n] = tr_frag((const LAS char*)smem + p * CHB + (q + 4 * n) * 1024 + 8 * (j & 1), lane);
      asm volatile("s_waitcnt lgkmcnt(0)" ::: "memory");
#pragma unroll
      for (int n = 0; n < 7; ++n) {
        u32x4 f = fr[n];
        if ((lane & 31) >= 28) f = u32x4{0u, 0u, 0u, 0u};
        *(LAS u32x4*)((LAS char*)smem + p * CHB + (q + 4 * n) * 1024 + lane * 16) = f;
        if (rb_ == 0) hst(rs, lane * 16, OFF_WQKV + (j * 3 + p) * CHB + (q + 4 * n) * 1024, f);
        sum += frag_sum(f);
      }
      sum = sum32(sum);
      if (lane < 32) { *(LAS float*)((LAS char*)smem + AUX + 512 + (p * 4 + q) * 128 + lane * 4) = sum;
        if (rb_ == 0) __builtin_amdgcn_raw_buffer_store_b32(__float_as_uint(sum), rs, lane * 4, OFF_CSQ + (j * 3 + p) * 512 + q * 128, 16); }
    };
    wait_vm<7>();
    qkv_tiles(0); qkv_tiles(1);
    PIN(tv);
    const unsigned T = __builtin_amdgcn_readfirstlane(tv) + 1u;
    auto stage_cs = [&](unsigned src, int n16, int dst) {
      const int lane = lane_id();
      if (lane < n16) {
        f32x4 a = {0.f, 0.f, 0.f, 0.f};
#pragma unroll
        for (int q = 0; q < 4; ++q) a += __builtin_bit_cast(f32x4, hld(rs, (lane >> 3) * 512 + (lane & 7) * 16 + q * 128, src));
        *(LAS f32x4*)((LAS char*)smem + AUX + dst + lane * 16) = a;
      }
    };
    auto stage_csq = [&]() { if (w_ == 3) stage_cs(OFF_CSQ + j_ * 3 * 512, 24, CSQ_L); };
    u32x4 hd[4][10];
#pragma unroll
    for (int c = 0; c < 4; ++c)
#pragma unroll
      for (int p = 0; p < 10; ++p) hd[c][p] = u32x4{0u, 0u, 0u, 0u};
    auto hold = [&](auto C) {
      constexpr int c = C;
      int w = w_, mm = m_; asm volatile("" : "+s"(w), "+s"(mm));
      if (w == 0 || !active) return;
      const int fs = mm >= 7 ? mm - 7 : mm;
      const char* src = ws + OFF_WDN + (size_t)fs * 229376 + (4 + c) * CHB + (w - 1) * 9216 + lane_id() * 16;
#pragma unroll
      for (int p = 0; p < 9; ++p) hd[c][p] = gld(src + p * 1024);
      if (w == 3) hd[c][9] = gld(src + 9 * 1024);
      SB();
    };
    auto drop = [&](auto C) {
      constexpr int c = C;
      int w = w_; asm volatile("" : "+s"(w));
      if (w == 0) return;
      LAS char* dst = (LAS char*)smem + (c & 3) * CHB + (w - 1) * 9216 + lane_id() * 16;
#pragma unroll
      for (int p = 0; p < 9; ++p) lds_wr(dst, p * 1024, hd[c][p]);
      if (w == 3) lds_wr(dst, 9 * 1024, hd[c][9]);
    };
    using I0 = std::integral_constant<int, 0>; using I1 = std::integral_constant<int, 1>; using I2 = std::integral_constant<int, 2>; using I3 = std::integral_constant<int, 3>;
#pragma unroll 1
    for (int l = 0; l < 2; ++l) {
      IDS();
      if (l == 0) {
        bar_lgkm();
        wait_vm<0>();
        qkv_tiles(2);
        if (w == 1 && lane == 0) put(ws, OFF_FLAG + FL_STARTED * 128 + b * 4, T);
        bar_lgkm();
        if (w == 3 && lane < 24) {
          const LAS char* ps = (const LAS char*)aux + 512 + (lane >> 3) * 512 + (lane & 7) * 16;
          const f32x4 a = (*(const LAS f32x4*)(ps) + *(const LAS f32x4*)(ps + 128)) + (*(const LAS f32x4*)(ps + 256) + *(const LAS f32x4*)(ps + 384));
          *(LAS f32x4*)(aux + CSQ_L + lane * 16) = a;
        }
        bar_lgkm();
        wait_vm<0>();
        if (rb == 0) { if (lane < 48) put(ws, OFF_FLAG + (FL_START + (lane & 15) * 16 + j) * 128 + ((lane >> 4) * 4 + w) * 4, T); }
        if (w == 2) {
          poll(ws, OFF_FLAG + (lane < 12 ? (FL_START + rb * 16 + j) * 128 + lane * 4 : (FL_WG + rb * 16 + j) * 128 + min(lane - 12, 27) * 4), T);
          if (active) poll(ws, OFF_FLAG + (FL_WP + (rp * 16 + mm) * 2) * 128 + (lane < 32 ? lane * 4 : 128 + min(lane - 32, 7) * 4), T);
          __builtin_amdgcn_fence(__ATOMIC_ACQUIRE, "agent");
        }
      } else {
        if (w == 0) poll_line(ws, FL_X2 + rb * 16 + j, 14, T, lane); else wait_vm<0>();
        bar_lgkm();
        bar();
        bar();
      }
      bar();
      if (w == 1 && lane < 4) put(ws, OFF_FLAG + (FL_KV + ((l * 16 + rb) * 16 + j) * 2 + (lane >> 1)) * 128 + (lane & 1) * 4, T);
      if (w == 0) {
        if (rb >= 1) {
          const int far = (lane & 2) && rb >= 2;
          poll(ws, OFF_FLAG + (FL_KV + ((l * 16 + rb - 1 - far) * 16 + j) * 2 + far) * 128 + (lane & 1) * 4, T);
        }
      }
      bar();
      if (rb >= 1) {
        u32x4 hv[8];
#pragma unroll
        for (int n = 0; n < 2; ++n) {
          const int rel = 2 * w + n, kt = max(4 * rb - 8 + rel, 0);
#pragma unroll
          for (int q = 0; q < 4; ++q) hv[n * 4 + q] = hld(rs, voff + q * 1024, OFF_KV + l * SZ_KV + (kt * 16 + j) * 4096);
        }
        SB();
#pragma unroll
        for (int n = 0; n < 8; ++n) lds_wr(aux + lane * 16, (2 * w) * 4096 + n * 1024, hv[n]);
      }
      if (w == 2) wait_vm<0>();
      bar_lgkm();
      issue(3, l); issue(4, l); issue(5, l); issue(6, l);
      bar();
      if (w == 1) sig16(ws, FL_O + (l * 16 + rb) * 16, 1, j, T, lane);
      if (w == 3) stage_cs(OFF_CSG + j * 7 * 512, 56, CSG_L);
      if (active) {
        if (w == 1 || w == 2) dma_n<4>(rs, voff, lds0 + AUX + WOT_L + (w - 1) * 4096, OFF_WO + fs * 65536 + 2 * CHB + (w - 1) * 4096);
        if (w == 0) poll_line(ws, FL_O + (l * 16 + rp) * 16 + mm, 16, T, lane);
        if (w != 0) wait_vm<0>();
        bar_lgkm();
        bar();
        bar();
        if (w == 1) sig16(ws, FL_X1 + (l * 16 + rp) * 16, 1, mm, T, lane);
      }
      issue(7, l); issue(8, l);
      if (w == 0) poll_line(ws, FL_X1 + (l * 16 + rb) * 16 + j, 14, T, lane);
      if (w != 0) wait_vm<18>();
      bar_lgkm();
      wait_vm_bar<9>();
      issue(9, l); issue(10, l);
      wait_vm_bar<18>();
      const bool first = l == 0 && active;
      wait_vm_bar<9>(); issue(11, l);
      if (l == 0) hold(I0{});
      if (first) wait_vm<18>(); else wait_vm<9>();
      bar(); issue(12, l);
      if (l == 0) hold(I1{});
      if (first) wait_vm<27>(); else wait_vm<9>();
      bar(); issue(13, l);
      if (l == 0) hold(I2{});
      bar();
      if (w == 1) sig16(ws, FL_H + (l * 16 + rb) * 16, 1, j, T, lane);
      issue(14, l);
      if (l == 0) hold(I3{});
      bar();
      if (w == 1) sig16(ws, FL_H + 512 + (l * 16 + rb) * 16, 1, j, T, lane);
      if (active) {
        issue(15, l);
        if (w == 0) poll_line(ws, FL_H + (l * 16 + rp) * 16 + mm, 16, T, lane);
        if (first) wait_vm<36>(); else wait_vm<18>();
        bar();
        bar();
        if (first) wait_vm<18>(); else wait_vm<9>();
        bar();
        drop(I0{});
        if (w == 0) poll_line(ws, FL_H + 512 + (l * 16 + rp) * 16 + mm, 16, T, lane);
        if (w != 0) wait_vm<0>();
        bar_lgkm();
        drop(I1{});
        bar_lgkm();
        drop(I2{});
        bar_lgkm();
        drop(I3{});
        bar_lgkm();
        issue(20, l);
        bar_lgkm();
        issue(21, l);
        bar();
        issue(22, l);
        bar();
        if (l == 1) bar();
        if (l == 0) bar();
        if (l == 0 && w == 1) sig16(ws, FL_X2 + rp * 16, 1, mm, T, lane);
        if (l == 0) stage_csq();
      } else if (l == 0) { stage_csq(); issue(20, l); issue(21, l); issue(22, l); }
      else if (b == 56 && w == 0) {
#pragma unroll 1
        for (int n = 0; n < 4; ++n) poll(ws, OFF_FLAG + FL_STARTED * 128 + (n * 64 + lane) * 4, T);
        if (lane == 0) put(ws, OFF_FLAG + FL_GEN * 128, T);
      }
    }
    return;
  }

#pragma unroll 1
  for (int l = 0; l < 2; ++l) {
    u32x4 qf[2];
    {
      IDS();
      if (l == 1) bar();
      constexpr int AH = 10;
      u32x4 af[KSD];
      const unsigned xo = OFF_XA + 2 * l * SZ_XA + tt * (KSD * 1024);
      Stats st;
      const LAS char* wk = slot(0); const LAS char* wv = slot(1); const LAS char* wq = slot(2);
      f32x16 ak = {}, av = {};
      u32x4 rq[4];
      const LAS float* csl = (const LAS float*)(aux + CSQ_L);
      LAS float* stl = (LAS float*)(aux + 8 * 4096 + w * 4096);
      f32x4 ck[4], srs[4], smn[4];
      float csv;
      auto passA = [&](auto FIRST) {
        StatLd sl;
        unsigned tv = 0;
        const int uu = x_ * 14 + idx, uj = uu / 7, ut = uu % 7;
        const int ukind = idx < 14 ? 1 : (x_ < 7 && idx < 22) ? 3 : (x_ < 7 && idx < 24) ? 2 : 0;
        if constexpr (FIRST) {
#pragma unroll
          for (int m = 0; m < 3; ++m)
#pragma unroll
            for (int n = 0; n < 8; ++n) af[8 * m + n] = *(const GAS u32x4*)(x + (size_t)(tt * 32 + 4 * n + (lane >> 4)) * D_ + 128 * m + (lane & 15) * 8);
#pragma unroll
          for (int n = 0; n < 4; ++n) af[24 + n] = *(const GAS u32x4*)(x + (size_t)(tt * 32 + 8 * n + (lane >> 3)) * D_ + 384 + (lane & 7) * 8);
          tv = __hip_atomic_load((gu32*)(ws + OFF_FLAG + FL_GEN * 128), RLX);
          u32x4 uv[8];
#pragma unroll
          for (int n = 0; n < 8; ++n) uv[n] = u32x4{0u, 0u, 0u, 0u};
          if (ukind == 1) unit_load<1>(uv, w1, w3, F_, 112 * uj + 16 * ut, 0, w, lane);
          else if (ukind == 3) unit_load<3>(uv, w2, nullptr, D_, 64 * x_ + 32 * ((idx - 14) & 1), (idx - 14) >> 1, w, lane);
          else if (ukind == 2) unit_load<2>(uv, wo, nullptr, D_, 64 * x_ + 32 * (idx - 22), 0, w, lane);
          SB();
          LAS char* scr = w < 3 ? (LAS char*)smem + 3 * CHB + w * 8704 : aux + 39936;
          float s1a = 0.f, s1b = 0.f, s2a = 0.f, s2b = 0.f;
          static_for<4>([&](auto M) {
            constexpr int m = M, NK = m < 3 ? 8 : 4;
#pragma unroll
            for (int n = 0; n < NK; ++n) {
              if (m < 3) *(LAS u32x4*)(scr + (4 * n + (lane >> 4)) * 272 + (lane & 15) * 16) = af[8 * m + n];
              else *(LAS u32x4*)(scr + (8 * n + (lane >> 3)) * 272 + (lane & 7) * 16) = af[8 * m + n];
            }
            asm volatile("" ::: "memory");
#pragma unroll
            for (int n = 0; n < NK; ++n) {
              const u32x2 a = *(const LAS u32x2*)(scr + r * 272 + n * 32 + 8 * h2), c = *(const LAS u32x2*)(scr + r * 272 + n * 32 + 16 + 8 * h2);
              af[8 * m + n] = u32x4{a.x, a.y, c.x, c.y};
            }
            asm volatile("s_waitcnt lgkmcnt(0)" ::: "memory");
#pragma unroll
            for (int n = 0; n < NK; ++n)
#pragma unroll
              for (int e = 0; e < 4; ++e) { const float lo = bflo(af[8 * m + n][e]), hi = bfhi(af[8 * m + n][e]); s1a += lo; s1b += hi; s2a = __builtin_fmaf(lo, lo, s2a); s2b = __builtin_fmaf(hi, hi, s2b); }
            SB();
          });
          const float s1 = sum32(s1a + s1b), s2 = sum32(s2a + s2b);
          st.mean = s1 * (1.0f / D_);
          st.rs = __builtin_amdgcn_rsqf(fmaxf(s2 * (1.0f / D_) - st.mean * st.mean, 0.f) + 1e-5f);
          if (ukind == 1) unit_finish<1>(uv, rs, OFF_WGU + uu * CHB, OFF_CSG + uu * 512, 0, 0, scr, w, lane);
          else if (ukind == 3) unit_finish<3>(uv, rs, OFF_WDN + x_ * 229376, 0, (idx - 14) & 1, ((idx - 14) >> 1) + 4 * x_, scr, w, lane);
          else if (ukind == 2) unit_finish<2>(uv, rs, OFF_WO + x_ * 65536, 0, idx - 22, x_, scr, w, lane);
          PIN(tv);
          bar_lgkm();
        } else {
          static_for<AH>([&](auto I) { constexpr int ks = I; af[ks] = XLD(ks); });
          stats_issue(sl, rs, OFF_ST + 2 * l * SZ_ST + tt * 3584, lane);
        }
        constexpr int PF = 3;
        u32x4 rk[PF], rv[PF];
#pragma unroll
        for (int i = 0; i < PF; ++i) { rk[i] = lds_rd(wk, i * 1024); rv[i] = lds_rd(wv, i * 1024); }
        SB();
        static_for<KSD>([&](auto I) {
          constexpr int ks = I;
          if constexpr (FIRST && ks == 8) {
            if (ukind != 0) {
              wait_vm<0>();
              const unsigned T = __builtin_amdgcn_readfirstlane(tv) + 1u;
              if (ukind == 1) sig16(ws, FL_WG + uj, 16, ut * 4 + w, T, lane);
              else {
                const int u8 = idx - 14, line = (ukind == 3 && u8 >= 6) ? 1 : 0, word = (ukind == 2 ? (idx - 22) * 4 : u8 < 6 ? 8 + u8 * 4 : (u8 - 6) * 4) + w;
                if (lane < 32) put(ws, OFF_FLAG + (FL_WP + ((lane >> 1) * 16 + x_ + 7 * (lane & 1)) * 2 + line) * 128 + word * 4, T);
              }
            }
            SB();
          }
          if constexpr (ks == 14 || ks == 21) { bar(); SB(); }
          ak = mfma(rk[ks % PF], af[ks], ak); PIN(ak);
          if constexpr (ks + PF < KSD) rk[ks % PF] = lds_rd(wk, (ks + PF) * 1024);
          if constexpr (!FIRST && ks + AH < KSD) af[ks + AH] = XLD(ks + AH);
          if constexpr (!FIRST && ks == 20) { st = stats_finish(sl); PIN(st.mean); PIN(st.rs); }
          SB();
          av = mfma(af[ks], rv[ks % PF], av); PIN(av);
          if constexpr (ks + PF < KSD) rv[ks % PF] = lds_rd(wv, (ks + PF) * 1024);
          if constexpr (ks >= KSD - 4) rq[ks - (KSD - 4)] = lds_rd(wq, (ks - (KSD - 4)) * 1024);
          if constexpr (ks == 21) { if (h2 == 0) stl[r] = st.rs; else stl[32 + r] = st.mean; }
          if constexpr (ks == 22) {
#pragma unroll
            for (int g = 0; g < 4; ++g) ck[g] = *(const LAS f32x4*)(csl + 8 * g + 4 * h2);
            csv = csl[32 + r];
          }
          if constexpr (ks >= 23 && ks < 27) { constexpr int g = ks - 23; srs[g] = *(const LAS f32x4*)(stl + 8 * g + 4 * h2); smn[g] = *(const LAS f32x4*)(stl + 32 + 8 * g + 4 * h2); }
          SB();
        });
      };
      if (l == 0) passA(std::true_type{}); else passA(std::false_type{});
      f32x16 aq = {};
      u32x4 ko[2], vo[2];
      f32x4 cq[4];
      const unsigned kvo = OFF_KV + l * SZ_KV + (tt * 16 + j) * 4096;
      static_for<KSD>([&](auto I) {
        constexpr int ks = I;
        aq = mfma(rq[ks & 3], af[ks], aq); PIN(aq);
        if constexpr (ks + 4 < KSD) rq[ks & 3] = lds_rd(wq, (ks + 4) * 1024);
        if constexpr (ks < 2) {
#pragma unroll
          for (int g = 2 * ks; g < 2 * ks + 2; ++g) {
            float v[4];
#pragma unroll
            for (int e = 0; e < 4; ++e) { v[e] = ak[4 * g + e] - st.mean * ck[g][e]; PIN(v[e]); }
#pragma unroll
            for (int e = 0; e < 4; ++e) { v[e] *= st.rs; PIN(v[e]); }
            ko[g >> 1][(g & 1) * 2] = pk2(v[0], v[1]); ko[g >> 1][(g & 1) * 2 + 1] = pk2(v[2], v[3]);
          }
          PIN(ko[ks]);
        }
        if constexpr (ks == 2) { hst(rs, voff, kvo, ko[0]); hst(rs, voff + 1024, kvo, ko[1]); }
        if constexpr (ks >= 2 && ks < 6) {
          constexpr int g = ks - 2;
          float v[4];
#pragma unroll
          for (int e = 0; e < 4; ++e) { v[e] = av[4 * g + e] - smn[g][e] * csv; PIN(v[e]); }
#pragma unroll
          for (int e = 0; e < 4; ++e) { v[e] *= srs[g][e]; PIN(v[e]); }
          const unsigned p0 = pk2(v[0], v[1]), p1 = pk2(v[2], v[3]);
          vo[g >> 1][(g & 1) * 2] = r == 28 ? 0x3f803f80u : p0; vo[g >> 1][(g & 1) * 2 + 1] = r == 28 ? 0x3f803f80u : p1;
          PIN(vo[g >> 1]);
        }
        if constexpr (ks == 6) { hst(rs, voff + 2048, kvo, vo[0]); hst(rs, voff + 3072, kvo, vo[1]); }
        if constexpr (ks == 7) { LAS char* kvl = aux + (8 + w) * 4096 + lane * 16; lds_wr(kvl, 0, ko[0]); lds_wr(kvl, 1024, ko[1]); lds_wr(kvl, 2048, vo[0]); lds_wr(kvl, 3072, vo[1]); }
        if constexpr (ks >= 16 && ks < 20) cq[ks - 16] = *(const LAS f32x4*)(csl + 64 + 8 * (ks - 16) + 4 * h2);
        if constexpr (ks == 24) { wait_vm_bar<0>(); }
        SB();
      });
      const float qs = st.rs * (0.18898223650461362f * 1.4426950408889634f);
#pragma unroll
      for (int g = 0; g < 4; ++g) {
        float v[4];
#pragma unroll
        for (int e = 0; e < 4; ++e) { v[e] = aq[4 * g + e] - st.mean * cq[g][e]; PIN(v[e]); v[e] *= qs; PIN(v[e]); }
        qf[g >> 1][(g & 1) * 2] = pk2(v[0], v[1]); qf[g >> 1][(g & 1) * 2 + 1] = pk2(v[2], v[3]);
      }
    }
    {
      IDS();
      const float NEG = -1e30f;
      f32x16 cd, co;
#pragma unroll
      for (int i = 0; i < 16; ++i) { const int key = (i & 3) + 8 * (i >> 2) + 4 * h2; cd[i] = key <= r ? 0.f : NEG; co[i] = key > r ? 0.f : NEG; }
      PIN(cd); PIN(co);
      bar();
      const LAS char* kw = opq((const LAS char*)aux + lane * 16 + w * 4096);
      bar();
      float m = NEG;
      f32x16 o = {};
      if (rb >= 2) {
        u32x4 kf[9][2];
#pragma unroll
        for (int it = 0; it < 9; ++it) { kf[it][0] = lds_rd(kw, it * 4096); kf[it][1] = lds_rd(kw, it * 4096 + 1024); }
        SB();
        {
          const f32x16 z = {};
          f32x16 s[3];
          static_for<11>([&](auto IT) {
            constexpr int it = IT;
            if constexpr (it < 9) { s[it % 3] = mfma(kf[it][0], qf[0], it == 0 ? co : it == 8 ? cd : z); s[it % 3] = mfma(kf[it][1], qf[1], s[it % 3]); PIN(s[it % 3]); }
            if constexpr (it >= 2) {
              const f32x16& t = s[(it - 2) % 3];
              float ma = fmaxf(fmaxf(t[0], t[1]), t[2]), mb = fmaxf(fmaxf(t[3], t[4]), t[5]);
              ma = fmaxf(fmaxf(ma, t[6]), t[7]); mb = fmaxf(fmaxf(mb, t[8]), t[9]);
              ma = fmaxf(fmaxf(ma, t[10]), t[11]); mb = fmaxf(fmaxf(mb, t[12]), t[13]);
              ma = fmaxf(fmaxf(ma, t[14]), t[15]);
              m = fmaxf(fmaxf(m, ma), mb);
              PIN(m);
            }
            SB();
          });
        }
        m = -max32(m);
        PIN(m);
        f32x16 cm;
#pragma unroll
        for (int i = 0; i < 16; ++i) { cm[i] = m; co[i] = co[i] + m; }
        u32x4 vf[2][2];
        vf[0][0] = lds_rd(kw, 2048); vf[0][1] = lds_rd(kw, 3072);
        f32x16 s[3];
        s[0] = mfma(kf[0][0], qf[0], co); s[0] = mfma(kf[0][1], qf[1], s[0]);
        s[1] = mfma(kf[1][0], qf[0], cm); s[1] = mfma(kf[1][1], qf[1], s[1]);
#pragma unroll
        for (int i = 0; i < 16; ++i) cd[i] = cd[i] + m;
        SB();
        static_for<9>([&](auto IT) {
          constexpr int it = IT;
          if constexpr (it < 8) { vf[(it + 1) & 1][0] = lds_rd(kw, (it + 1) * 4096 + 2048); vf[(it + 1) & 1][1] = lds_rd(kw, (it + 1) * 4096 + 3072); }
          u32x4 p[2];
          const f32x16& c = s[it % 3];
          if constexpr (it + 2 < 9) { s[(it + 2) % 3] = mfma(kf[it + 2][0], qf[0], it + 2 == 8 ? cd : cm); }
#pragma unroll
          for (int q = 0; q < 4; ++q) p[0][q] = pk2(__builtin_amdgcn_exp2f(c[2 * q]), __builtin_amdgcn_exp2f(c[2 * q + 1]));
          PIN(p[0]);
          SB();
          if constexpr (it + 2 < 9) { s[(it + 2) % 3] = mfma(kf[it + 2][1], qf[1], s[(it + 2) % 3]); PIN(s[(it + 2) % 3]); }
          o = mfma(vf[it & 1][0], p[0], o);
#pragma unroll
          for (int q = 0; q < 4; ++q) p[1][q] = pk2(__builtin_amdgcn_exp2f(c[8 + 2 * q]), __builtin_amdgcn_exp2f(c[8 + 2 * q + 1]));
          PIN(p[1]);
          SB();
          o = mfma(vf[it & 1][1], p[1], o); PIN(o);
          SB();
        });
      } else {
        const LAS char* kq = kw;
        const f32x16 z = {};
#pragma unroll
        for (int it = 0; it < 9; ++it) {
          if (tt - 8 + it >= 0) {
            f32x16 s = mfma(lds_rd(kq, it * 4096), qf[0], it == 0 ? co : it == 8 ? cd : z);
            s = mfma(lds_rd(kq, it * 4096 + 1024), qf[1], s);
#pragma unroll
            for (int i = 0; i < 16; i += 2) m = fmaxf(fmaxf(m, s[i]), s[i + 1]);
          }
        }
        m = -max32(m);
        f32x16 cm;
#pragma unroll
        for (int i = 0; i < 16; ++i) { cd[i] += m; cm[i] = m; }
#pragma unroll
        for (int it = 0; it < 9; ++it) {
          if (it >= 1 && tt - 8 + it >= 0) {
            f32x16 s = mfma(lds_rd(kq, it * 4096), qf[0], it == 8 ? cd : cm);
            s = mfma(lds_rd(kq, it * 4096 + 1024), qf[1], s);
            u32x4 p[2];
            PACK16(p, pk2(__builtin_amdgcn_exp2f(s[i0]), __builtin_amdgcn_exp2f(s[i1])));
            o = mfma(lds_rd(kq, it * 4096 + 2048), p[0], o);
            o = mfma(lds_rd(kq, it * 4096 + 3072), p[1], o);
          }
        }
      }
      const unsigned lu = __float_as_uint(o[12]);
      auto rr = __builtin_amdgcn_permlane32_swap(lu, lu, false, false);
      const float inv = __builtin_amdgcn_rcpf(__uint_as_float(rr[1]));
      u32x4 ob[2];
      PACK16(ob, pk2(o[i0] * inv, o[i1] * inv));
      const unsigned oo = OFF_OA + l * SZ_OA + (tt * KSO + 2 * j) * 1024;
      hst(rs, voff, oo, ob[0]); hst(rs, voff + 1024, oo, ob[1]);
      wait_vm_bar<0>();
    }
    auto proj_init = [&](int xin) { IDS(); const unsigned xo = OFF_XA + xin * SZ_XA + (ttp * KSD + 4 * fs + 2 * kh) * 1024;
      u32x4 xr[2];
      if (xin == 0) {
        const unsigned short* xp = x + (size_t)(ttp * 32 + r) * D_ + 64 * fs + 32 * kh + 4 * h2;
#pragma unroll
        for (int g = 0; g < 4; ++g) { const u32x2 v = *(const GAS u32x2*)(xp + 8 * g); xr[g >> 1][(g & 1) * 2] = v.x; xr[g >> 1][(g & 1) * 2 + 1] = v.y; }
      } else { xr[0] = hld(rs, voff, xo); xr[1] = hld(rs, voff + 1024, xo); }
      f32x16 a;
#pragma unroll
      for (int q = 0; q < 8; ++q) { const unsigned xv = xr[q >> 2][q & 3]; a[2 * q] = bflo(xv); a[2 * q + 1] = bfhi(xv); }
      PIN(a);
      return a; };
    auto proj_finish = [&](f32x16 own, const f32x16& snd, int xout, bool final_) {
      IDS();
      u32x4 xres[2];
      LAS f32x4* xs = (LAS f32x4*)(aux) + (tt2 * 2 + kh) * 256 + lane;
      LAS f32x4* xrv = (LAS f32x4*)(aux) + (tt2 * 2 + (kh ^ 1)) * 256 + lane;
#pragma unroll
      for (int g = 0; g < 4; ++g) { f32x4 v = {snd[4 * g], snd[4 * g + 1], snd[4 * g + 2], snd[4 * g + 3]}; xs[g * 64] = v; }
      bar_lgkm();
#pragma unroll
      for (int g = 0; g < 4; ++g) { const f32x4 v = xrv[g * 64]; own[4 * g] += v.x; own[4 * g + 1] += v.y; own[4 * g + 2] += v.z; own[4 * g + 3] += v.w; }
#pragma unroll
      for (int q = 0; q < 8; ++q) xres[q >> 2][q & 3] = pk2(own[2 * q], own[2 * q + 1]);
      if (final_) {
        LAS char* img = aux + 16384;
#pragma unroll
        for (int g = 0; g < 4; ++g) { u32x2 v = {xres[g >> 1][(g & 1) * 2], xres[g >> 1][(g & 1) * 2 + 1]}; *(LAS u32x2*)(img + (tt2 * 32 + r) * 144 + kh * 64 + g * 16 + h2 * 8) = v; }
        bar_lgkm();
#pragma unroll
        for (int n = 0; n < 2; ++n) {
          const int row = w * 16 + n * 8 + (lane >> 3);
          const u32x4 v = *(const LAS u32x4*)(img + row * 144 + (lane & 7) * 16);
          *(GAS u32x4*)(out + (size_t)((4 * rp + 2 * th) * 32 + row) * D_ + 64 * fs + (lane & 7) * 8) = v;
        }
      } else {
        const unsigned xo = OFF_XA + xout * SZ_XA + (ttp * KSD + 4 * fs + 2 * kh) * 1024;
        hst(rs, voff, xo, xres[0]); hst(rs, voff + 1024, xo, xres[1]);
        float s1[4], s2[4];
#pragma unroll
        for (int g = 0; g < 4; ++g) {
          s1[g] = (own[4 * g] + own[4 * g + 1]) + (own[4 * g + 2] + own[4 * g + 3]);
          s2[g] = __builtin_fmaf(own[4 * g], own[4 * g], own[4 * g + 1] * own[4 * g + 1]) + __builtin_fmaf(own[4 * g + 2], own[4 * g + 2], own[4 * g + 3] * own[4 * g + 3]);
        }
        const float t1 = sum32((s1[0] + s1[1]) + (s1[2] + s1[3])), t2 = sum32((s2[0] + s2[1]) + (s2[2] + s2[3]));
        if (h2 == 0) { u32x2 v = {__float_as_uint(t1), __float_as_uint(t2)}; __builtin_amdgcn_raw_buffer_store_b64(v, rs, r * 8, OFF_ST + xout * SZ_ST + (ttp * 14 + 2 * fs + kh) * 256, 16); }
      }
    };
    if (active) {
      IDS();
      f32x16 own = proj_init(2 * l), snd = {};
      bar();
      u32x4 af[16];
      const unsigned ao = OFF_OA + l * SZ_OA + (ttp * KSO + kh * 16) * 1024;
      constexpr int AH = 10;
      const unsigned oa = ao + 2 * fs * 1024, ob_ = oa - 16 * 1024;
#define OLD(i) hld(rs, voff + (i) * 1024, (i) + 2 * fs < 16 ? oa : ob_)
      static_for<AH>([&](auto I) { constexpr int i = I; af[i] = OLD(i); });
      const LAS char* wbo[3] = {opq(ring + 3 * CHB + kh * 3072), opq(ring + 0 * CHB + kh * 3072), opq((const LAS char*)aux + WOT_L + lane * 16 + kh * 3072)};
      const LAS char* wbs[3] = {opq(ring + 3 * CHB + kh * 2048 + (kh ^ 1) * 1024), opq(ring + 0 * CHB + kh * 2048 + (kh ^ 1) * 1024), opq((const LAS char*)aux + WOT_L + lane * 16 + kh * 2048 + (kh ^ 1) * 1024)};
      constexpr int PF = 2;
      u32x4 wo_[PF], wsn[PF];
#pragma unroll
      for (int i = 0; i < PF; ++i) { wo_[i] = lds_rd(wbo[0], i * 4096); wsn[i] = lds_rd(wbs[0], i * 4096); }
      SB();
      static_for<16>([&](auto I) {
        constexpr int i = I, n = i + PF;
        own = mfma(wo_[i % PF], af[i], own); PIN(own);
        if constexpr (n < 16) wo_[i % PF] = lds_rd(wbo[n / 7], (n % 7) * 4096);
        if constexpr (i + AH < 16) af[i + AH] = OLD(i + AH);
        SB();
        snd = mfma(wsn[i % PF], af[i], snd); PIN(snd);
        if constexpr (n < 16) wsn[i % PF] = lds_rd(wbs[n / 7], (n % 7) * 4096);
        SB();
      });
      proj_finish(own, snd, 2 * l + 1, false);
      wait_vm_bar<0>();
    }
    {
      IDS();
      bar();
      constexpr int AH = 10;
      u32x4 af[KSD];
      const unsigned xo = OFF_XA + (2 * l + 1) * SZ_XA + tt * (KSD * 1024);
      StatLd sl;
      Stats st;
      static_for<AH>([&](auto I) { constexpr int ks = I; af[ks] = XLD(ks); });
      stats_issue(sl, rs, OFF_ST + (2 * l + 1) * SZ_ST + tt * 3584, lane);
      const LAS float* csl = (const LAS float*)(aux + CSG_L) + 4 * h2;
      const unsigned ho = OFF_HA + l * SZ_HA + tt * HSTR + 7 * j * 1024;
      f32x16 prev = {}, acc = {};
      u32x4 wr[4];
      {
        const LAS char* w0 = slot(5); const LAS char* w1 = slot(6);
        constexpr int PF = 3;
        u32x4 r0[PF], r1[PF];
#pragma unroll
        for (int i = 0; i < PF; ++i) { r0[i] = lds_rd(w0, i * 1024); r1[i] = lds_rd(w1, i * 1024); }
        SB();
        static_for<KSD>([&](auto I) {
          constexpr int ks = I;
          prev = mfma(r0[ks % PF], af[ks], prev); PIN(prev);
          if constexpr (ks + PF < KSD) r0[ks % PF] = lds_rd(w0, (ks + PF) * 1024);
          if constexpr (ks + AH < KSD) af[ks + AH] = XLD(ks + AH);
          if constexpr (ks == 20) { st = stats_finish(sl); PIN(st.mean); PIN(st.rs); }
          SB();
          acc = mfma(r1[ks % PF], af[ks], acc); PIN(acc);
          if constexpr (ks + PF < KSD) r1[ks % PF] = lds_rd(w1, (ks + PF) * 1024);
          SB();
        });
      }
      const float c1 = -1.4426950408889634f * st.rs, vk = __builtin_amdgcn_rcpf(st.rs * st.rs);
      u32x4 ov;
      f32x4 cc[4];
      float ea[8], eb[8], ex[8];
#define GU_STAGE(P, v, S) do { \
        if constexpr ((S) == 0) { ea[v] = P[v] - st.mean * cc[(v) >> 2][(v) & 3]; PIN(ea[v]); eb[v] = P[(v) + 8] - st.mean * cc[2 + ((v) >> 2)][(v) & 3]; PIN(eb[v]); } \
        else if constexpr ((S) == 1) { ex[v] = c1 * ea[v]; PIN(ex[v]); eb[v] = ea[v] * eb[v]; PIN(eb[v]); } \
        else if constexpr ((S) == 2) { ex[v] = __builtin_amdgcn_exp2f(ex[v]); PIN(ex[v]); } \
        else if constexpr ((S) == 3) { ex[v] = __builtin_fmaf(ex[v], vk, vk); PIN(ex[v]); } \
        else if constexpr ((S) == 4) { ex[v] = __builtin_amdgcn_rcpf(ex[v]); PIN(ex[v]); } \
        else { eb[v] = eb[v] * ex[v]; PIN(eb[v]); if constexpr ((v) & 1) { ov[(v) >> 1] = pk2(eb[(v) - 1], eb[v]); PIN(ov); } } } while (0)
#define GU_GAP(P, g, base, sp) static_for<8>([&](auto V_) { constexpr int v_ = V_; constexpr int s_ = (g) - (base) - (sp) * v_; if constexpr (s_ >= 0 && s_ < 6) GU_STAGE(P, v_, s_); })
#define GU_CC(t) _Pragma("unroll") for (int g = 0; g < 4; ++g) cc[g] = *(const LAS f32x4*)(csl + (t) * 32 + 8 * g)
      GU_CC(0);
      bar();
      { const LAS char* w2 = slot(7);
#pragma unroll
        for (int i = 0; i < 4; ++i) wr[i] = lds_rd(w2, i * 1024); }
      SB();
      f32x16 t1 = acc;
      static_for<5>([&](auto T) {
        constexpr int t = T + 2;
        const LAS char* wl = slot(5 + t);
        const LAS char* wn = slot(6 + t);
        acc = f32x16{};
        static_for<KSD>([&](auto I) {
          constexpr int ks = I;
          if constexpr (ks == 14) {
            if constexpr (t < 6) bar();
            else asm volatile("s_waitcnt vmcnt(1)\n\ts_barrier" ::: "memory");
            SB();
          }
          acc = mfma(wr[ks & 3], af[ks], acc); PIN(acc);
          if constexpr (ks + 4 < KSD) wr[ks & 3] = lds_rd(wl, (ks + 4) * 1024);
          else if constexpr (t < 6) wr[ks & 3] = lds_rd(wn, (ks + 4 - KSD) * 1024);
          if constexpr (t == 2) {
            if constexpr (ks < 14) { GU_GAP(prev, ks, 0, 1); }
            if constexpr (ks == 12) { GU_CC(1); }
            if constexpr (ks == 13) hst(rs, voff, ho, ov);
            if constexpr (ks >= 14) { GU_GAP(t1, ks, 15, 1); }
            if constexpr (ks == 27) { hst(rs, voff + 1024, ho, ov); GU_CC(2); }
          } else {
            GU_GAP(prev, ks, 1, 3);
            if constexpr (ks == 27) { hst(rs, voff + (t - 1) * 1024, ho, ov); GU_CC(t); }
          }
          SB();
        });
        prev = acc;
      });
      static_for<13>([&](auto G) { constexpr int g = G; GU_GAP(prev, g, 0, 1); });
      hst(rs, voff + 6 * 1024, ho, ov);
      wait_vm_bar<0>();
    }
    if (active) {
      IDS();
      f32x16 own = proj_init(2 * l + 1), snd = {};
      bar();
      constexpr int WIN = DN_WIN;
      constexpr int NR = WIN + 2;
      u32x4 af[NR];
      const unsigned ao = OFF_HA + l * SZ_HA + ttp * HSTR + kh * 56 * 1024;
#define HLD(i) hld(rs, voff + (kk_of(i) % 7) * 1024, ao + (((kk_of(i) / 7) + fs) & 7) * 7168)
      static_for<WIN>([&](auto I) { constexpr int i = I; af[i] = HLD(i); });
      const int oo_ = kh * 3072, so_ = kh * 2048 + (kh ^ 1) * 1024;
      constexpr int PF = 2;
      u32x4 wo_[PF], wsn[PF];
      const LAS char* po = opq(ring + 0 * CHB + oo_); const LAS char* ps = opq(ring + 0 * CHB + so_);
#pragma unroll
      for (int i = 0; i < PF; ++i) { wo_[i] = lds_rd(po, i * 4096); wsn[i] = lds_rd(ps, i * 4096); }
      SB();
      const LAS char* pon = po; const LAS char* psn = ps;
      static_for<56>([&](auto I) {
        constexpr int i = I, c = i / 7, s = i % 7, n = s + PF;
        if constexpr (s == 0) { po = pon; ps = psn; if constexpr (c < 7) { pon = opq(ring + ((13 + c) & 3) * CHB + oo_); psn = opq(ring + ((13 + c) & 3) * CHB + so_); } }
        if constexpr (s == 3) { bar(); SB(); }
        const u32x4 a = af[i % NR];
        own = mfma(wo_[i % PF], a, own); PIN(own);
        if constexpr (n < 7) wo_[i % PF] = lds_rd(po, n * 4096); else if constexpr (c < 7) wo_[i % PF] = lds_rd(pon, (n - 7) * 4096);
        SB();
        snd = mfma(wsn[i % PF], a, snd); PIN(snd);
        if constexpr (n < 7) wsn[i % PF] = lds_rd(ps, n * 4096); else if constexpr (c < 7) wsn[i % PF] = lds_rd(psn, (n - 7) * 4096);
        if constexpr (i + WIN < 56) af[(i + WIN) % NR] = HLD(i + WIN);
        SB();
      });
      proj_finish(own, snd, 2, l == 1);
      if (l == 0) wait_vm_bar<0>();
    }
  }
}

}

extern "C" void kernel_launch(void* const* d_in, const int* in_sizes, int n_in, void* d_out, int out_size, void* d_ws, size_t ws_size,
                              hipStream_t stream) {
  (void)hipFuncSetAttribute((const void*)k_main, hipFuncAttributeMaxDynamicSharedMemorySize, LDS_BYTES);
  k_main<<<256, 512, LDS_BYTES, stream>>>((char*)d_ws, (unsigned short*)d_out, (const unsigned short*)d_in[0], (const unsigned short*)d_in[1], (const unsigned short*)d_in[2],
                                          (const unsigned short*)d_in[4], (const unsigned short*)d_in[5], (const unsigned short*)d_in[6]);
}
```
